```python
import torch

def _softmax(x, axis=-1):
    return torch.softmax(x, dim=axis)
def _gelu(x):
    return torch.nn.functional.gelu(x, approximate="tanh")
def _silu(x):
    return torch.nn.functional.silu(x)
def _sigmoid(x):
    return torch.sigmoid(x)
def _layernorm(x, axis=-1, eps=1e-5):
    m = x.mean(dim=axis, keepdim=True)
    v = x.var(dim=axis, keepdim=True, unbiased=False)
    return (x - m) / torch.sqrt(v + eps)
def _scatter_add(tgt, idx, src, axis=0):
    return tgt.clone().index_add_(axis, idx, src)
def _segment_sum(src, seg, n):
    out = torch.zeros((n,) + src.shape[1:], dtype=src.dtype)
    return out.index_add_(0, seg, src)
def _pad(x, pw):
    flat = tuple(v for pair in reversed(pw) for v in pair)
    return torch.nn.functional.pad(x, flat)

kernel_name = 'gated_mix__linear_kernel_hedgehog__glu_gated_geglu__prompt'

def setup_inputs():

    _w = (lambda a: (lambda t: t.to(torch.bfloat16) if t.is_floating_point() else t)(torch.as_tensor(a)))
    def _A_setup():
        B, S, D, H = 1, 2048, 448, 16
        Dh = D // H
        return {
            "x": _w((lambda *s: torch.randn(*s, dtype=torch.bfloat16) * 0.05)(B, S, D)),
            "wqkv": _w((lambda *s: torch.randn(*s, dtype=torch.bfloat16) * 0.8)(D, 3 * D)),
            "w_phi": _w((lambda *s: torch.randn(*s, dtype=torch.bfloat16) * 0.8)(H, Dh, Dh) * (Dh ** -0.5)),
            "wo": _w((lambda *s: torch.randn(*s, dtype=torch.bfloat16) * 0.05)(D, D)),
        }
    _A_in = _A_setup()

    def _B_setup():
        B, S, Dm, Df = 1, 2048, 448, 1792
        return {
            "x": _w((lambda *s: torch.randn(*s, dtype=torch.bfloat16) * 0.05)(B, S, Dm)),
            "w_gate": _w((lambda *s: torch.randn(*s, dtype=torch.bfloat16) * 1.6)(Dm, Df) * (Dm ** -0.5)),
            "w_up": _w((lambda *s: torch.randn(*s, dtype=torch.bfloat16) * 1.6)(Dm, Df) * (Dm ** -0.5)),
            "w_down": _w((lambda *s: torch.randn(*s, dtype=torch.bfloat16) * 1.6)(Df, Dm) * (Df ** -0.5)),
        }
    _B_in = _B_setup()

    return {
        "A_x": _A_in["x"],
        "A_wqkv": _A_in["wqkv"],
        "A_w_phi": _A_in["w_phi"],
        "A_wo": _A_in["wo"],
        "B_x": _B_in["x"],
        "B_w_gate": _B_in["w_gate"],
        "B_w_up": _B_in["w_up"],
        "B_w_down": _B_in["w_down"],
        "gate_w": _w((lambda *s: torch.randn(*s, dtype=torch.bfloat16) * 0.05)(448) * 16.0),
    }

def reference(A_x, A_wqkv, A_w_phi, A_wo, B_x, B_w_gate, B_w_up, B_w_down, gate_w):
    def _A_ref(x, wqkv, w_phi, wo):
        B, S, D = x.shape
        H = 16; Dh = D // H
        qkv = (lambda x, *d: x.permute(*d))((x @ wqkv).reshape(B, S, 3, H, Dh), 2, 0, 3, 1, 4)
        q, k, v = qkv[0], qkv[1], qkv[2]
        pq = torch.einsum("bhsd,hdf->bhsf", q, w_phi)
        pk = torch.einsum("bhsd,hdf->bhsf", k, w_phi)
        phi_q = torch.concatenate([_softmax(pq, axis=-1), _softmax(-pq, axis=-1)], axis=-1)
        phi_k = torch.concatenate([_softmax(pk, axis=-1), _softmax(-pk, axis=-1)], axis=-1)
        kv = torch.einsum("bhsf,bhsd->bhfd", phi_k, v)
        z  = torch.einsum("bhsf->bhf", phi_k)
        num = torch.einsum("bhqf,bhfd->bhqd", phi_q, kv)
        den = torch.einsum("bhqf,bhf->bhq", phi_q, z)[..., None] + 1e-6
        o = num / den
        return (lambda x, *d: x.permute(*d))(o, 0, 2, 1, 3).reshape(B, S, D) @ wo

    def _B_ref(x, w_gate, w_up, w_down):
        g = torch.einsum("bsd,df->bsf", x, w_gate)
        u = torch.einsum("bsd,df->bsf", x, w_up)
        h = _gelu(g) * u
        return torch.einsum("bsf,fd->bsd", h, w_down)

    x = A_x
    g = _sigmoid((x * gate_w).sum(-1, keepdims=True))
    a = _A_ref(x, A_wqkv, A_w_phi, A_wo)
    b = _B_ref(x, B_w_gate, B_w_up, B_w_down)
    return g * a + (1 - g) * b
```

```cpp
#include <hip/hip_runtime.h>
#include <hip/hip_bf16.h>
typedef __hip_bfloat16 bf16;
constexpr int S=2048, D=448, H=16, DH=28, DF=1792;
__device__ __forceinline__ float b2f(bf16 v){ return __bfloat162float(v); }
__device__ __forceinline__ float rb(float v){ return __bfloat162float(__float2bfloat16(v)); }

template<bool AF>
__global__ void gemm_naive(const void* Ap, const bf16* B, float* C, int M, int N, int K){
  __shared__ float As[16][17], Bs[16][17];
  int tx=threadIdx.x&15, ty=threadIdx.x>>4;
  int r=blockIdx.y*16+ty, c=blockIdx.x*16+tx;
  float acc=0;
  for(int k0=0;k0<K;k0+=16){
    As[ty][tx]= AF? ((const float*)Ap)[(size_t)r*K+k0+tx] : b2f(((const bf16*)Ap)[(size_t)r*K+k0+tx]);
    Bs[ty][tx]= b2f(B[(size_t)(k0+ty)*N+c]);
    __syncthreads();
    for(int k=0;k<16;k++) acc+=As[ty][k]*Bs[k][tx];
    __syncthreads();
  }
  C[(size_t)r*N+c]=rb(acc);
}
__global__ void phi_k(const float* qkv, const bf16* wphi, float* phiq, float* phik){
  int idx=blockIdx.x*blockDim.x+threadIdx.x;
  int s=idx/(2*H), h=(idx/2)%H, w=idx&1;
  const float* q=qkv+(size_t)s*3*D+w*D+h*DH;
  float* o=(w?phik:phiq)+((size_t)s*H+h)*56;
  float mx=-1e30f, mn=1e30f;
  for(int f=0;f<DH;f++){ float a=0; for(int d=0;d<DH;d++) a+=q[d]*b2f(wphi[(h*DH+d)*DH+f]); a=rb(a); o[f]=a; mx=fmaxf(mx,a); mn=fminf(mn,a);}
  float sp=0,sn=0;
  for(int f=0;f<DH;f++){ sp+=expf(o[f]-mx); sn+=expf(mn-o[f]); }
  for(int f=0;f<DH;f++){ float a=o[f]; o[DH+f]=rb(expf(mn-a)/sn); o[f]=rb(expf(a-mx)/sp); }
}
__global__ void kv_k(const float* qkv, const float* phik, float* kv){
  int h=blockIdx.x; int f=blockIdx.y; int d=threadIdx.x; if(d>DH) return;
  float a=0;
  for(int s=0;s<S;s++){ float p=phik[((size_t)s*H+h)*56+f]; float v= d<DH? qkv[(size_t)s*3*D+2*D+h*DH+d]:1.f; a+=p*v; }
  kv[(h*56+f)*29+d]=rb(a);
}
__global__ void o_k(const float* phiq, const float* kv, float* o){
  int idx=blockIdx.x*blockDim.x+threadIdx.x;
  int s=idx/D, c=idx%D, h=c/DH, d=c%DH;
  const float* p=phiq+((size_t)s*H+h)*56;
  float num=0, den=0;
  for(int f=0;f<56;f++){ num+=p[f]*kv[(h*56+f)*29+d]; den+=p[f]*kv[(h*56+f)*29+28]; }
  o[idx]=rb(rb(num)/rb(rb(den)+1e-6f));
}
__global__ void h_k(const float* g, const float* u, float* h){
  int idx=blockIdx.x*blockDim.x+threadIdx.x;
  float x=g[idx]; float t=tanhf(0.7978845608028654f*(x+0.044715f*x*x*x));
  h[idx]=rb(rb(0.5f*x*(1.f+t))*u[idx]);
}
__global__ void out_k(const bf16* x, const bf16* gw, const float* a, const float* b, bf16* out){
  int s=blockIdx.x; __shared__ float red[64];
  float acc=0; for(int d=threadIdx.x; d<D; d+=64) acc+=rb(b2f(x[s*D+d])*b2f(gw[d]));
  red[threadIdx.x]=acc; __syncthreads();
  float t=0; for(int i=0;i<64;i++) t+=red[i];
  t=rb(t); float g=rb(1.f/(1.f+expf(-t)));
  for(int d=threadIdx.x; d<D; d+=64) out[s*D+d]=__float2bfloat16(rb(g*a[s*D+d])+rb(rb(1.f-g)*b[s*D+d]));
}
extern "C" void kernel_launch(void* const* d_in, const int* in_sizes, int n_in, void* d_out, int out_size, void* d_ws, size_t ws_size, hipStream_t stream){
  const bf16* x=(const bf16*)d_in[0]; const bf16* wqkv=(const bf16*)d_in[1]; const bf16* wphi=(const bf16*)d_in[2]; const bf16* wo=(const bf16*)d_in[3];
  const bf16* wg=(const bf16*)d_in[5]; const bf16* wu=(const bf16*)d_in[6]; const bf16* wd=(const bf16*)d_in[7]; const bf16* gw=(const bf16*)d_in[8];
  float* ws=(float*)d_ws;
  float* qkv=ws; ws+=S*3*D; float* phiq=ws; ws+=S*H*56; float* phik=ws; ws+=S*H*56; float* kv=ws; ws+=H*56*32;
  float* o=ws; ws+=S*D; float* a=ws; ws+=S*D; float* g=ws; ws+=S*DF; float* u=ws; ws+=S*DF; float* h=ws; ws+=S*DF; float* b=ws; ws+=S*D;
  gemm_naive<false><<<dim3(3*D/16,S/16),256,0,stream>>>(x,wqkv,qkv,S,3*D,D);
  gemm_naive<false><<<dim3(DF/16,S/16),256,0,stream>>>(x,wg,g,S,DF,D);
  gemm_naive<false><<<dim3(DF/16,S/16),256,0,stream>>>(x,wu,u,S,DF,D);
  phi_k<<<S*H*2/256,256,0,stream>>>(qkv,wphi,phiq,phik);
  kv_k<<<dim3(H,56),64,0,stream>>>(qkv,phik,kv);
  o_k<<<S*D/256,256,0,stream>>>(phiq,kv,o);
  gemm_naive<true><<<dim3(D/16,S/16),256,0,stream>>>(o,wo,a,S,D,D);
  h_k<<<S*DF/256,256,0,stream>>>(g,u,h);
  gemm_naive<true><<<dim3(D/16,S/16),256,0,stream>>>(h,wd,b,S,D,DF);
  out_k<<<S,64,0,stream>>>(x,gw,a,b,(bf16*)d_out);
}
```

```cpp
#include <hip/hip_runtime.h>
#include <hip/hip_bf16.h>
typedef __hip_bfloat16 bf16;
constexpr int S=2048, D=448, H=16, DH=28, DF=1792;
__device__ __forceinline__ float b2f(bf16 v){ return __bfloat162float(v); }
__device__ __forceinline__ float rb(float v){ return __bfloat162float(__float2bfloat16(v)); }

template<bool AF>
__global__ void gemm_naive(const void* Ap, const bf16* B, float* C, int M, int N, int K){
  __shared__ float As[16][17], Bs[16][17];
  int tx=threadIdx.x&15, ty=threadIdx.x>>4;
  int r=blockIdx.y*16+ty, c=blockIdx.x*16+tx;
  float acc=0;
  for(int k0=0;k0<K;k0+=16){
    As[ty][tx]= AF? ((const float*)Ap)[(size_t)r*K+k0+tx] : b2f(((const bf16*)Ap)[(size_t)r*K+k0+tx]);
    Bs[ty][tx]= b2f(B[(size_t)(k0+ty)*N+c]);
    __syncthreads();
    for(int k=0;k<16;k++) acc+=As[ty][k]*Bs[k][tx];
    __syncthreads();
  }
  C[(size_t)r*N+c]=rb(acc);
}
__global__ void phi_k(const float* qkv, const bf16* wphi, float* phiq, float* phik){
  int idx=blockIdx.x*blockDim.x+threadIdx.x;
  int s=idx/(2*H), h=(idx/2)%H, w=idx&1;
  const float* q=qkv+(size_t)s*3*D+w*D+h*DH;
  float* o=(w?phik:phiq)+((size_t)s*H+h)*56;
  float mx=-1e30f, mn=1e30f;
  for(int f=0;f<DH;f++){ float a=0; for(int d=0;d<DH;d++) a+=q[d]*b2f(wphi[(h*DH+d)*DH+f]); a=rb(a); o[f]=a; mx=fmaxf(mx,a); mn=fminf(mn,a);}
  float sp=0,sn=0;
  for(int f=0;f<DH;f++){ sp+=expf(o[f]-mx); sn+=expf(mn-o[f]); }
  for(int f=0;f<DH;f++){ float a=o[f]; o[DH+f]=rb(expf(mn-a)/sn); o[f]=rb(expf(a-mx)/sp); }
}
__global__ void kv_k(const float* qkv, const float* phik, float* kv){
  int h=blockIdx.x; int f=blockIdx.y; int d=threadIdx.x; if(d>DH) return;
  float a=0;
  for(int s=0;s<S;s++){ float p=phik[((size_t)s*H+h)*56+f]; float v= d<DH? qkv[(size_t)s*3*D+2*D+h*DH+d]:1.f; a+=p*v; }
  kv[(h*56+f)*29+d]=rb(a);
}
__global__ void o_k(const float* phiq, const float* kv, float* o){
  int idx=blockIdx.x*blockDim.x+threadIdx.x;
  int s=idx/D, c=idx%D, h=c/DH, d=c%DH;
  const float* p=phiq+((size_t)s*H+h)*56;
  float num=0, den=0;
  for(int f=0;f<56;f++){ num+=p[f]*kv[(h*56+f)*29+d]; den+=p[f]*kv[(h*56+f)*29+28]; }
  o[idx]=rb(rb(num)/rb(rb(den)+1e-6f));
}
__global__ void h_k(const float* g, const float* u, float* h){
  int idx=blockIdx.x*blockDim.x+threadIdx.x;
  float x=g[idx]; float t=tanhf(0.7978845608028654f*(x+0.044715f*x*x*x));
  h[idx]=rb(rb(0.5f*x*(1.f+t))*u[idx]);
}
__global__ void out_k(const bf16* x, const bf16* gw, const float* a, const float* b, bf16* out){
  int s=blockIdx.x; __shared__ float red[64];
  float acc=0; for(int d=threadIdx.x; d<D; d+=64) acc+=rb(b2f(x[s*D+d])*b2f(gw[d]));
  red[threadIdx.x]=acc; __syncthreads();
  float t=0; for(int i=0;i<64;i++) t+=red[i];
  t=rb(t); float g=rb(1.f/(1.f+expf(-t)));
  for(int d=threadIdx.x; d<D; d+=64) out[s*D+d]=__float2bfloat16(rb(g*a[s*D+d])+rb(rb(1.f-g)*b[s*D+d]));
}
#define METRIC 0

typedef unsigned u32x4 __attribute__((ext_vector_type(4)));
typedef unsigned long long u64;
__global__ __launch_bounds__(512) void probe_k(const char* x,const char* wqkv,const char* wo,const char* wg,const char* wu,const char* wd,u64* stamps,unsigned* sink){
  int tid=threadIdx.x, bid=blockIdx.x;
  u64 t0=__builtin_amdgcn_s_memrealtime();
  u32x4 acc={0,0,0,0};
  u64 tf=0;
  u32x4 v0={0,0,0,0},v1=v0,v2=v0,v3=v0,v4=v0,v5=v0;
  if(tid*16<7168) v0=*(const u32x4*)(x+bid*7168+tid*16);
  if(METRIC==2){ acc^=v0; asm volatile("s_waitcnt vmcnt(0)":"+v"(acc)::"memory"); tf=__builtin_amdgcn_s_memrealtime(); }
  if(tid*16<4704) v1=*(const u32x4*)(wqkv+bid*4704+tid*16);
  if(tid*16<1568) v2=*(const u32x4*)(wo+bid*1568+tid*16);
  if(tid*16<6272) v3=*(const u32x4*)(wg+bid*6272+tid*16);
  if(tid*16<6272) v4=*(const u32x4*)(wu+bid*6272+tid*16);
  if(tid*16<6272) v5=*(const u32x4*)(wd+bid*6272+tid*16);
  acc^=v0^v1^v2^v3^v4^v5;
  asm volatile("s_waitcnt vmcnt(0)":"+v"(acc)::"memory");
  __syncthreads();
  u64 t1=__builtin_amdgcn_s_memrealtime();
  if(acc[0]==0x12345678 && acc[1]==0x9abcdef && acc[3]==77) sink[tid]=acc[2]^acc[3];
  if(tid==0){ stamps[bid*4+0]=t0; stamps[bid*4+1]=t1; stamps[bid*4+2]=tf; }
}
__global__ void amp_k(const u64* stamps){
  u64 mn=~0ull,mx=0,mxs=0,sum=0,sumf=0;
  for(int b=0;b<256;b++){ u64 a=stamps[b*4],e=stamps[b*4+1]; mn=a<mn?a:mn; mx=e>mx?e:mx; mxs=a>mxs?a:mxs; sum+=e-a; sumf+=stamps[b*4+2]-a;}
  u64 m = METRIC==0?0: (METRIC==1||METRIC==5)? (mx-mn)*100 : METRIC==2? sumf*100/256 : METRIC==3? (mxs-mn)*100 : sum*100/256;
  if(m>2000000) m=2000000;
  u64 t0=__builtin_amdgcn_s_memrealtime();
  while(__builtin_amdgcn_s_memrealtime()-t0<m) __builtin_amdgcn_s_sleep(4);
}

extern "C" void kernel_launch(void* const* d_in, const int* in_sizes, int n_in, void* d_out, int out_size, void* d_ws, size_t ws_size, hipStream_t stream){
  const bf16* x=(const bf16*)d_in[0]; const bf16* wqkv=(const bf16*)d_in[1]; const bf16* wphi=(const bf16*)d_in[2]; const bf16* wo=(const bf16*)d_in[3];
  const bf16* wg=(const bf16*)d_in[5]; const bf16* wu=(const bf16*)d_in[6]; const bf16* wd=(const bf16*)d_in[7]; const bf16* gw=(const bf16*)d_in[8];
  float* ws=(float*)d_ws;
  float* qkv=ws; ws+=S*3*D; float* phiq=ws; ws+=S*H*56; float* phik=ws; ws+=S*H*56; float* kv=ws; ws+=H*56*32;
  float* o=ws; ws+=S*D; float* a=ws; ws+=S*D; float* g=ws; ws+=S*DF; float* u=ws; ws+=S*DF; float* h=ws; ws+=S*DF; float* b=ws; ws+=S*D;
  u64* stamps=(u64*)((char*)d_ws+200000000); probe_k<<<256,512,0,stream>>>((const char*)x,(const char*)wqkv,(const char*)wo,(const char*)wg,(const char*)wu,(const char*)wd,stamps,(unsigned*)((char*)d_ws+210000000)); amp_k<<<1,64,0,stream>>>(stamps);
  gemm_naive<false><<<dim3(3*D/16,S/16),256,0,stream>>>(x,wqkv,qkv,S,3*D,D);
  gemm_naive<false><<<dim3(DF/16,S/16),256,0,stream>>>(x,wg,g,S,DF,D);
  gemm_naive<false><<<dim3(DF/16,S/16),256,0,stream>>>(x,wu,u,S,DF,D);
  phi_k<<<S*H*2/256,256,0,stream>>>(qkv,wphi,phiq,phik);
  kv_k<<<dim3(H,56),64,0,stream>>>(qkv,phik,kv);
  o_k<<<S*D/256,256,0,stream>>>(phiq,kv,o);
  gemm_naive<true><<<dim3(D/16,S/16),256,0,stream>>>(o,wo,a,S,D,D);
  h_k<<<S*DF/256,256,0,stream>>>(g,u,h);
  gemm_naive<true><<<dim3(D/16,S/16),256,0,stream>>>(h,wd,b,S,D,DF);
  out_k<<<S,64,0,stream>>>(x,gw,a,b,(bf16*)d_out);
}
```

```cpp
#include <hip/hip_runtime.h>
#include <hip/hip_bf16.h>
#include <utility>
#pragma clang diagnostic ignored "-Wint-to-pointer-cast"

typedef unsigned u32;
typedef unsigned long long u64;
typedef u32 u32x4 __attribute__((ext_vector_type(4)));
typedef u32 u32x2 __attribute__((ext_vector_type(2)));
typedef short s16x4 __attribute__((ext_vector_type(4)));
typedef __bf16 bf16x8 __attribute__((ext_vector_type(8)));
typedef __bf16 bf16x2 __attribute__((ext_vector_type(2)));
typedef float f32x16 __attribute__((ext_vector_type(16)));
typedef float f32x4 __attribute__((ext_vector_type(4)));
typedef float f32x2 __attribute__((ext_vector_type(2)));
typedef __amdgpu_buffer_rsrc_t rsrc_t;
#define DI __device__ __forceinline__
#define LAS __attribute__((address_space(3)))
#define SB0() __builtin_amdgcn_sched_barrier(0)
#define PIN2(a, b) asm volatile("" : "+v"(a), "+v"(b))

constexpr u32 WS_FLAG = 4096;
constexpr u32 WS_G    = WS_FLAG + 256 * 128;
constexpr u32 WS_KVP  = 65536;
constexpr u32 WS_O    = WS_KVP + 256 * 4096;
constexpr u32 WS_P    = WS_O + 64 * 32 * 1024;
constexpr u32 WS_H    = WS_P + 256 * 16384;
constexpr u32 WS_END  = WS_H + 64 * 112 * 1024;

constexpr int UNIT = 8192, RING = 18;
constexpr u32 SCR = RING * UNIT;
constexpr int LDS_BYTES = 163840;
constexpr u32 OOB = 0x80000000u;
constexpr u32 SPIN_MAX = 1u << 16;

constexpr u32 upos(int n) { return (u32)(n % RING) * UNIT; }

struct Ctx {
  const char *x, *wqkv, *wphi, *wo, *wg, *wu, *wd, *gw;
  char *out, *ws;
  int t, kh, jj, hd, j, w, tg, r;
  u32 lane, m0w;
  u32 vx, vw, vq, v2;
  rsrc_t sx, sg, su, sq, sp, sd, so, sw;
  u64 tag;
};

DI rsrc_t mk(const void* p, u32 bytes) { return __builtin_amdgcn_make_buffer_rsrc((void*)p, 0, bytes, 0x00020000); }

template <u32 LDSOFF, u32 SOFF>
DI void dma(const Ctx& c, u32 voff, rsrc_t rs) {
  u32 tmp;
  asm volatile("s_add_u32 m0, %1, %2\n\ts_mov_b32 %0, %3\n\tbuffer_load_dwordx4 %4, %5, %0 offen lds"
               : "=&s"(tmp) : "s"(c.m0w), "i"(LDSOFF), "i"(SOFF), "v"(voff), "s"(rs) : "memory", "scc");
}
template <u32 LDSOFF>
DI void dma_s(const Ctx& c, u32 voff, rsrc_t rs, u32 soff) {
  asm volatile("s_add_u32 m0, %0, %1\n\ts_nop 0\n\tbuffer_load_dwordx4 %2, %3, %4 offen lds"
               :: "s"(c.m0w), "i"(LDSOFF), "v"(voff), "s"(rs), "s"(soff) : "memory", "scc");
}
template <int N> DI void wait_bar() { asm volatile("s_waitcnt vmcnt(%0) lgkmcnt(0)\n\ts_barrier" :: "n"(N) : "memory"); }
DI void bar_l() { asm volatile("s_waitcnt lgkmcnt(0)\n\ts_barrier" ::: "memory"); }

template <int M, u32 L> DI void issue_qkv(const Ctx& c) {
  if constexpr (M < 9) dma<L, (u32)((M / 3) * 128 * 2688 + (M % 3) * 896)>(c, c.vq, c.sq);
  else if constexpr (M == 9) dma_s<L>(c, c.vq, c.sq, (u32)(384 * 2688 + c.r * (896 - 64 * 2688)));
  else {
    if (c.r == 0) dma<L, (u32)(384 * 2688 + 2 * 896)>(c, c.vq, c.sq);
    else dma_s<L>(c, c.lane * 16, c.sp, c.w == 5 ? 0u : c.w == 6 ? 1024u : 0x40000000u);
  }
}
template <int N> DI void issue_unit(const Ctx& c) {
  constexpr u32 L = upos(N);
  if constexpr (N < 28) {
    constexpr int cc = N / 4, k = N % 4;
    if constexpr (k < 2) dma<L, (u32)(k * 64 * 896 + cc * 128)>(c, c.vx, c.sx);
    else if constexpr (k == 2) dma<L, (u32)(cc * 64 * 3584)>(c, c.vw, c.sg);
    else dma<L, (u32)(cc * 64 * 3584)>(c, c.vw, c.su);
  } else if constexpr (N < 39) issue_qkv<N - 28, L>(c);
  else if constexpr (N < 53) {
    constexpr int cc = (N - 39) / 2, k = (N - 39) % 2;
    if constexpr (k == 0) dma<L, (u32)(cc * 64 * 3584 + 128)>(c, c.vw, c.sg);
    else dma<L, (u32)(cc * 64 * 3584 + 128)>(c, c.vw, c.su);
  } else if constexpr (N < 60) dma<L, (u32)((N - 53) * 128 * 896)>(c, c.v2, c.sd);
  else if constexpr (N < 67) dma<L, (u32)(((N - 60) * 128 + 64) * 896)>(c, c.v2, c.sd);
  else dma<L, (u32)((N - 67) * 64 * 896)>(c, c.v2, c.so);
}
template <int A, int B> DI void issue_range(const Ctx& c) {
  if constexpr (A < B) { issue_unit<A>(c); issue_range<A + 1, B>(c); }
}

struct Seg { u32 b[3]; };
DI Seg mkseg(u32 a) { Seg s; s.b[0] = a; s.b[1] = a + 65536; s.b[2] = a + 131072; asm volatile("" : "+v"(s.b[0]), "+v"(s.b[1]), "+v"(s.b[2])); return s; }
DI u32 sa(const Seg& s, u32 off) { return s.b[off >> 16] + (off & 0xffffu); }
DI f32x16 mfma(bf16x8 a, bf16x8 b, f32x16 c) { return __builtin_amdgcn_mfma_f32_32x32x16_bf16(a, b, c, 0, 0, 0); }
DI bf16x8 as_frag(u32x4 v) { return __builtin_bit_cast(bf16x8, v); }
DI bf16x8 tr_frag(u32 a0, u32 a1) {
  s16x4 lo = __builtin_amdgcn_ds_read_tr16_b64_v4i16((LAS s16x4*)a0);
  s16x4 hi = __builtin_amdgcn_ds_read_tr16_b64_v4i16((LAS s16x4*)a1);
  return __builtin_bit_cast(bf16x8, __builtin_shufflevector(lo, hi, 0, 1, 2, 3, 4, 5, 6, 7));
}
DI u32x4 lds_rd128(u32 a) { return *(LAS u32x4*)a; }
DI void lds_wr128(u32 a, u32x4 v) { *(LAS u32x4*)a = v; }
DI u32 pk(float a, float b) { f32x2 t = {a, b}; return __builtin_bit_cast(u32, __builtin_convertvector(t, bf16x2)); }
template <int S> DI u32x4 pack8(const f32x16& v) {
  u32x4 r = {pk(v[8 * S], v[8 * S + 1]), pk(v[8 * S + 2], v[8 * S + 3]), pk(v[8 * S + 4], v[8 * S + 5]), pk(v[8 * S + 6], v[8 * S + 7])};
  return r;
}
DI float blo(u32 d) { return __uint_as_float(d << 16); }
DI float bhi(u32 d) { return __uint_as_float(d & 0xffff0000u); }
DI u32x4 sanit(u32x4 v) { u32x4 r = {min(v[0], 0xfffffffeu), min(v[1], 0xfffffffeu), min(v[2], 0xfffffffeu), min(v[3], 0xfffffffeu)}; return r; }
DI void st_dev(const Ctx& c, u32x4 v, u32 voff, u32 soff) { __builtin_amdgcn_raw_buffer_store_b128(v, c.sw, voff, soff, 16); }
DI u32x4 ld_dev(const Ctx& c, u32 voff, u32 soff) { return __builtin_amdgcn_raw_buffer_load_b128(c.sw, voff, soff, 16); }
DI u32 mx4(u32 m, u32x4 v) { return max(max(m, v[0]), max(v[1], max(v[2], v[3]))); }
DI f32x2 xhalf(float v) { auto p = __builtin_amdgcn_permlane32_swap(__float_as_uint(v), __float_as_uint(v), false, false); f32x2 r = {__uint_as_float(p[0]), __uint_as_float(p[1])}; return r; }

#define STAMP(i)

DI void gelu_store(const Ctx& c, const f32x16& g, const f32x16& u, int blk) {
  constexpr float K1 = -2.0f * 0.7978845608028654f * 1.4426950408889634f, K2 = K1 * 0.044715f;
  f32x16 h;
#pragma unroll
  for (int i = 0; i < 16; ++i) {
    float x = g[i];
    float e = __builtin_amdgcn_exp2f(x * __builtin_fmaf(x * x, K2, K1));
    h[i] = (x * u[i]) * __builtin_amdgcn_rcpf(1.0f + e);
  }
  u32 so = WS_H + (u32)(((c.t * 4 + c.tg) * 112 + 8 * c.j + 4 * blk + 2 * c.r) * 1024);
  st_dev(c, sanit(pack8<0>(h)), c.lane * 16, so);
  st_dev(c, sanit(pack8<1>(h)), c.lane * 16 + 1024, so);
}

DI void feature_map(const f32x16& a, const u32x4 (&wp)[2], u32x4 (&ph)[4]) {
  f32x16 p = {};
  p = mfma(as_frag(wp[0]), as_frag(pack8<0>(a)), p);
  p = mfma(as_frag(wp[1]), as_frag(pack8<1>(a)), p);
  float mx = p[0], mn = p[0];
#pragma unroll
  for (int i = 1; i < 14; ++i) { mx = fmaxf(mx, p[i]); mn = fminf(mn, p[i]); }
  f32x2 t = xhalf(mx); mx = fmaxf(t[0], t[1]);
  t = xhalf(mn); mn = fminf(t[0], t[1]);
  constexpr float L = 1.4426950408889634f;
  float mxl = mx * L, mnl = mn * L, sp = 0.f, sn = 0.f;
  f32x16 ep, en;
#pragma unroll
  for (int i = 0; i < 14; ++i) {
    ep[i] = __builtin_amdgcn_exp2f(__builtin_fmaf(p[i], L, -mxl)); sp += ep[i];
    en[i] = __builtin_amdgcn_exp2f(__builtin_fmaf(p[i], -L, mnl)); sn += en[i];
  }
  t = xhalf(sp); sp = __builtin_amdgcn_rcpf(t[0] + t[1]);
  t = xhalf(sn); sn = __builtin_amdgcn_rcpf(t[0] + t[1]);
#pragma unroll
  for (int i = 0; i < 14; ++i) { ep[i] *= sp; en[i] *= sn; }
  ep[14] = ep[15] = en[14] = en[15] = 0.f;
  ph[0] = pack8<0>(ep); ph[1] = pack8<1>(ep); ph[2] = pack8<0>(en); ph[3] = pack8<1>(en);
}

DI void load_wphi(const Ctx& c, u32 base, u32x4 (&wp)[2]) {
  u32 rho = c.lane & 31, h = c.lane >> 5;
  u32 f = rho < 26 ? rho : ((rho == 28 || rho == 29) ? rho - 2 : 0);
  u32 a = base + 2 * (28 * (4 * h - 4 * (c.hd & 1)) + f);
#pragma unroll
  for (int s = 0; s < 2; ++s)
#pragma unroll
    for (int d = 0; d < 4; ++d) {
      const int e0 = 2 * d, k0 = 16 * s + 8 * (e0 >> 2) + (e0 & 3);
      u32 lo = *(LAS unsigned short*)(a + 56 * k0), hi = *(LAS unsigned short*)(a + 56 * (k0 + 1));
      wp[s][d] = lo | (hi << 16);
    }
}

DI void k_side(const Ctx& c, const f32x16& ak, const f32x16& av, const u32x4 (&wp)[2]) {
  u32x4 ph[4];
  feature_map(ak, wp, ph);
  const u32 l = c.lane, tok = l & 31, h = l >> 5, base = SCR + c.tg * 4096;
  const u32 ft = (((tok >> 1) & 1) << 3) | (tok & 1) | (((tok >> 2) & 3) << 1);
  const u32 wa = base + tok * 128 + ((h ^ ft) << 3);
#pragma unroll
  for (int T = 0; T < 2; ++T)
#pragma unroll
    for (int g = 0; g < 4; ++g) {
      u32x2 v = {ph[2 * T + (g >> 1)][2 * (g & 1)], ph[2 * T + (g >> 1)][2 * (g & 1) + 1]};
      *(LAS u32x2*)(wa ^ (u32)((8 * T + 2 * g) << 3)) = v;
    }
  u32x4 vf[2] = {pack8<0>(av), pack8<1>(av)};
  const bool one = tok == ((c.hd & 1) ? 0u : 28u);
#pragma unroll
  for (int s = 0; s < 2; ++s)
#pragma unroll
    for (int d = 0; d < 4; ++d) vf[s][d] = one ? 0x3f803f80u : vf[s][d];
  const u32 q = (l & 15) >> 2, p = l & 3, blk = (l >> 4) & 1;
  const u32 ra = base + (4 * h + q) * 128 + (((4 * blk + p) ^ (q & 1) ^ (h << 1) ^ ((q >> 1) << 3)) << 3);
  bf16x8 af[2][2];
#pragma unroll
  for (int T = 0; T < 2; ++T)
#pragma unroll
    for (int s = 0; s < 2; ++s) af[T][s] = tr_frag((ra ^ (u32)(64 * T)) + s * 2048, (ra ^ (u32)(64 * T + 32)) + s * 2048 + 1024);
#pragma unroll
  for (int T = 0; T < 2; ++T) {
    f32x16 kv = {};
    kv = mfma(af[T][0], as_frag(vf[0]), kv);
    kv = mfma(af[T][1], as_frag(vf[1]), kv);
    lds_wr128(base + (2 * T) * 1024 + l * 16, pack8<0>(kv));
    lds_wr128(base + (2 * T + 1) * 1024 + l * 16, pack8<1>(kv));
  }
}
DI void add8(float (&s)[8], u32x4 v) {
#pragma unroll
  for (int d = 0; d < 4; ++d) { s[2 * d] += blo(v[d]); s[2 * d + 1] += bhi(v[d]); }
}
DI u32x4 pack_s8(const float (&s)[8]) { u32x4 r = {pk(s[0], s[1]), pk(s[2], s[3]), pk(s[4], s[5]), pk(s[6], s[7])}; return r; }
DI void kvp_publish(const Ctx& c) {
  float s[8] = {};
#pragma unroll
  for (int i = 0; i < 4; ++i) add8(s, lds_rd128(SCR + i * 4096 + c.tg * 1024 + c.lane * 16));
  st_dev(c, sanit(pack_s8(s)), c.lane * 16, WS_KVP + (u32)((c.hd * 16 + c.t) * 4096 + c.tg * 1024));
}
template <int N> DI void ld_batch(const Ctx& c, u32x4 (&v)[N], u32 soff, u32 pair_stride) {
#pragma unroll
  for (int i = 0; i < N; ++i) v[i] = ld_dev(c, c.lane * 16 + (i & 1) * 1024, soff + (i >> 1) * pair_stride);
}
template <int N> DI void chk_batch(const Ctx& c, u32x4 (&v)[N], u32 soff, u32 pair_stride) {
  for (u32 spin = 0;; ++spin) {
    u32 m = 0;
#pragma unroll
    for (int i = 0; i < N; ++i) m = mx4(m, v[i]);
    if (!__any(m == 0xffffffffu) || spin > SPIN_MAX) break;
    __builtin_amdgcn_s_sleep(2);
    asm volatile("" ::: "memory");
    ld_batch<N>(c, v, soff, pair_stride);
  }
}
DI void kv_ld(const Ctx& c, u32x4 (&v)[8]) {
#pragma unroll
  for (int i = 0; i < 8; ++i) v[i] = ld_dev(c, c.lane * 16, WS_KVP + (u32)((c.hd * 16 + 8 * c.r + i) * 4096 + c.tg * 1024));
}
DI void kv_sum(const Ctx& c, u32x4 (&v)[8]) {
  for (u32 spin = 0;; ++spin) {
    u32 m = 0;
#pragma unroll
    for (int i = 0; i < 8; ++i) m = mx4(m, v[i]);
    if (!__any(m == 0xffffffffu) || spin > SPIN_MAX) break;
    __builtin_amdgcn_s_sleep(2);
    asm volatile("" ::: "memory");
    kv_ld(c, v);
  }
  float s[8] = {};
#pragma unroll
  for (int i = 0; i < 8; ++i) add8(s, v[i]);
  lds_wr128(SCR + c.w * 1024 + c.lane * 16, pack_s8(s));
}
DI void o_publish(const Ctx& c, const u32x4 (&phq)[4], float g) {
  f32x16 n = {};
#pragma unroll
  for (int i = 0; i < 8; ++i) n = mfma(as_frag(lds_rd128(SCR + i * 1024 + c.lane * 16)), as_frag(phq[i & 3]), n);
  const bool odd = c.hd & 1;
  f32x2 t = xhalf(odd ? n[0] : n[12]);
  float sc = g * __builtin_amdgcn_rcpf((odd ? t[0] : t[1]) + 1e-6f);
#pragma unroll
  for (int i = 0; i < 16; ++i) n[i] *= sc;
  u32x4 f0 = sanit(pack8<0>(n)), f1 = sanit(pack8<1>(n));
  const bool z0 = odd && c.lane < 32, z1 = !odd && c.lane >= 32;
  f0[0] = z0 ? 0 : f0[0]; f0[1] = z0 ? 0 : f0[1];
  f1[2] = z1 ? 0 : f1[2]; f1[3] = z1 ? 0 : f1[3];
  u32 so = WS_O + (u32)(((c.t * 4 + c.tg) * 16 + c.hd) * 2048);
  st_dev(c, f0, c.lane * 16, so);
  st_dev(c, f1, c.lane * 16 + 1024, so);
}
DI u32 flag_off(const Ctx& c) {
  u32 i = c.lane & 15;
  return WS_FLAG + (((c.lane & 16) ? i * 16 + c.hd : c.t * 16 + i) << 7);
}
DI void flag_spin(const Ctx& c, u32x2 f) {
  for (u32 spin = 0;; ++spin) {
    if (__all(f[0] == (u32)c.tag && f[1] == (u32)(c.tag >> 32)) || spin > SPIN_MAX) break;
    __builtin_amdgcn_s_sleep(4);
    asm volatile("" ::: "memory");
    f = __builtin_amdgcn_raw_buffer_load_b64(c.sw, flag_off(c), 0, 16);
  }
  asm volatile("" ::: "memory");
}
DI void flag_publish(const Ctx& c) {
  if (c.w == 0) {
    u32x2 v = {(u32)c.tag, (u32)(c.tag >> 32)};
    __builtin_amdgcn_raw_buffer_store_b64(v, c.sw, c.lane == 0 ? 0u : OOB, WS_FLAG + (u32)(c.t * 16 + c.hd) * 128, 16);
  }
}
DI float ld_g(const Ctx& c, int tgroup) {
  return __uint_as_float(__builtin_amdgcn_raw_buffer_load_b32(c.sw, (c.lane & 31) * 4, WS_G + (u32)(c.kh * 2048 + c.t * 128 + tgroup * 32) * 4, 16));
}

struct Addr { Seg aw; u32 aq, ax; };

template <int ROLE, int G, int QBASE> DI u32 qkv_piece(int part, int i) {
  if (G < 3) return upos(QBASE + 3 * G + part) + i * 1024;
  return part == 0 ? upos(QBASE + 9) + i * 1024 : part == 1 ? upos(QBASE + 9) + 4096 + i * 1024 : upos(QBASE + 10) + i * 1024;
}
template <int ROLE, int G, int QBASE>
DI void qkv_compute(const bf16x8 (&xf)[28], const Seg& aq, f32x16& a0, f32x16& a1) {
  constexpr int NK = G < 3 ? 8 : 4;
#pragma unroll
  for (int i = 0; i < NK; ++i) {
    if constexpr (ROLE == 0) {
      u32 pk_ = qkv_piece<ROLE, G, QBASE>(1, i), pv = qkv_piece<ROLE, G, QBASE>(2, i);
      bf16x8 wk = tr_frag(sa(aq, pk_), sa(aq, pk_ + 256)), wv = tr_frag(sa(aq, pv), sa(aq, pv + 256));
      a0 = mfma(wk, xf[8 * G + i], a0);
      a1 = mfma(xf[8 * G + i], wv, a1);
    } else {
      u32 pq = qkv_piece<ROLE, G, QBASE>(0, i);
      bf16x8 wq = tr_frag(sa(aq, pq), sa(aq, pq + 256));
      a0 = mfma(wq, xf[8 * G + i], a0);
    }
  }
}

template <int UG, int S0> DI void ffn_rd(const Seg& aw, bf16x8 (&f)[4]) {
#pragma unroll
  for (int s = 0; s < 2; ++s) {
    f[2 * s]     = tr_frag(sa(aw, upos(UG) + (S0 + s) * 2048), sa(aw, upos(UG) + (S0 + s) * 2048 + 512));
    f[2 * s + 1] = tr_frag(sa(aw, upos(UG + 1) + (S0 + s) * 2048), sa(aw, upos(UG + 1) + (S0 + s) * 2048 + 512));
  }
}
template <int K0> DI void ffn_mm(const bf16x8 (&f)[4], const bf16x8 (&xf)[28], f32x16& g, f32x16& u) {
  g = mfma(f[0], xf[K0], g); u = mfma(f[1], xf[K0], u);
  g = mfma(f[2], xf[K0 + 1], g); u = mfma(f[3], xf[K0 + 1], u);
}

constexpr int imin(int a, int b) { return a < b ? a : b; }

template <int C> DI void ffn1_step(const Ctx& c, const Addr& A, bf16x8 (&xf)[28], bf16x8 (&fa)[4], bf16x8 (&fb)[4], f32x16& g, f32x16& u) {
  constexpr int issued = C == 0 ? 18 : 18 + 4 * (C - 1);
  wait_bar<issued - 4 * (C + 1) + (C < 4 ? 5 : 0)>();
  if constexpr (C > 0) issue_range<18 + 4 * (C - 1), 18 + 4 * C>(c);
  static_assert(upos(4 * C + 1) == upos(4 * C) + UNIT, "x units must be adjacent");
  u32 axc = A.ax + upos(4 * C);
  asm volatile("" : "+v"(axc));
#pragma unroll
  for (int s = 0; s < 4; ++s) xf[4 * C + s] = as_frag(lds_rd128(axc ^ (u32)(32 * s)));
  ffn_rd<4 * C + 2, 0>(A.aw, fa);
  SB0();
  if constexpr (C > 0) ffn_mm<4 * C - 2>(fb, xf, g, u);
  SB0();
  ffn_rd<4 * C + 2, 2>(A.aw, fb);
  SB0();
  ffn_mm<4 * C>(fa, xf, g, u);
  if constexpr (C >= 5) PIN2(g, u);
  SB0();
}

template <int ROLE, int C> DI void ffn2_step(const Ctx& c, const Addr& A, const bf16x8 (&xf)[28], bf16x8 (&fa)[4], bf16x8 (&fb)[4], f32x16& g, f32x16& u, u32x2& poll) {
  constexpr int cons_prev = C == 0 ? 37 : 39 + 2 * (C - 1);
  constexpr int cons = 39 + 2 * C;
  constexpr int issued = imin(71, 18 + cons_prev);
  constexpr int n0 = issued - (cons + 2);
  if constexpr (C == 6) { asm volatile("s_waitcnt vmcnt(2) lgkmcnt(0)\n\ts_barrier" : "+v"(poll) :: "memory"); }
  else wait_bar<n0>();
  if constexpr (C == 5) asm volatile("buffer_load_dwordx2 %0, %1, %2, 0 offen sc1" : "=v"(poll) : "v"(flag_off(c)), "s"(c.sw) : "memory");
  issue_range<issued, imin(71, 18 + cons)>(c);
  if constexpr (C == 0 && ROLE == 1) kvp_publish(c);
  ffn_rd<cons, 0>(A.aw, fa);
  SB0();
  if constexpr (C > 0) ffn_mm<4 * C - 2>(fb, xf, g, u);
  SB0();
  ffn_rd<cons, 2>(A.aw, fb);
  SB0();
  ffn_mm<4 * C>(fa, xf, g, u);
  SB0();
}

template <int U0> DI void p2_h(const Seg& a0, const Seg& a1, const u32x4 (&hf)[14], f32x16& c0, f32x16& c1) {
#pragma unroll
  for (int i = 0; i < 14; ++i) {
    const u32 o = upos(U0 + (i >> 1)) + (i & 1) * 2048;
    bf16x8 w0 = tr_frag(sa(a0, o), sa(a0, o + 1024)), w1 = tr_frag(sa(a1, o), sa(a1, o + 1024));
    c0 = mfma(w0, as_frag(hf[i]), c0);
    c1 = mfma(w1, as_frag(hf[i]), c1);
  }
}

template <int ROLE> DI void ffn_tail(Ctx& c, const Addr& A, bf16x8 (&xf)[28], bf16x8 (&fb)[4], f32x16& g, f32x16& u) {
  const u32 l = c.lane;
  f32x16 a0 = {}, a1 = {};
  u32x4 wp[2];
  const Seg aq = mkseg(A.aq);
  wait_bar<42 - 31>(); issue_range<42, 46>(c);
  SB0(); ffn_mm<26>(fb, xf, g, u); SB0();
  STAMP(2);
  gelu_store(c, g, u, 0);
  STAMP(3);
  qkv_compute<ROLE, 0, 28>(xf, aq, a0, a1);
  wait_bar<46 - 34>(); issue_range<46, 49>(c);
  qkv_compute<ROLE, 1, 28>(xf, aq, a0, a1);
  wait_bar<49 - 37>(); issue_range<49, 52>(c);
  qkv_compute<ROLE, 2, 28>(xf, aq, a0, a1);
  PIN2(a0, a1);
  wait_bar<6>();
  issue_range<52, 55>(c);
  flag_publish(c);
  load_wphi(c, upos(38) + 4096 + 1024, wp);
  qkv_compute<ROLE, 3, 28>(xf, aq, a0, a1);
  STAMP(4);
  u32x4 phq[4];
  if constexpr (ROLE == 0) k_side(c, a0, a1, wp); else { feature_map(a0, wp, phq); PIN2(phq[0], phq[1]); PIN2(phq[2], phq[3]); }
  STAMP(5);
  bf16x8 fa[4];
  u32x2 poll;
  g = (f32x16){}; u = (f32x16){};
  ffn2_step<ROLE, 0>(c, A, xf, fa, fb, g, u, poll); ffn2_step<ROLE, 1>(c, A, xf, fa, fb, g, u, poll);
  ffn2_step<ROLE, 2>(c, A, xf, fa, fb, g, u, poll); ffn2_step<ROLE, 3>(c, A, xf, fa, fb, g, u, poll);
  ffn2_step<ROLE, 4>(c, A, xf, fa, fb, g, u, poll); ffn2_step<ROLE, 5>(c, A, xf, fa, fb, g, u, poll);
  ffn2_step<ROLE, 6>(c, A, xf, fa, fb, g, u, poll);
  bar_l();
  issue_range<69, 71>(c);
  SB0(); ffn_mm<26>(fb, xf, g, u); PIN2(g, u); SB0();
  STAMP(6);
  flag_spin(c, poll);
  const int tg2 = c.tg & 1, ks = c.tg >> 1, TG = 2 * (ROLE == 1 ? c.kh : 1 - c.kh) + tg2;
  u32x4 kvv[8], h1[14];
  kv_ld(c, kvv);
  float g2 = ld_g(c, TG), g1 = 0.f;
  if constexpr (ROLE == 1) g1 = ld_g(c, c.tg);
  const u32 hbase = WS_H + (u32)(((c.t * 4 + TG) * 112 + 56 * c.kh + 2 * ks) * 1024);
  ld_batch<14>(c, h1, hbase, 8192);
  SB0();
  gelu_store(c, g, u, 1);
  STAMP(7);
  kv_sum(c, kvv);
  bar_l();
  STAMP(8);
  if constexpr (ROLE == 1) o_publish(c, phq, g1);
  STAMP(9);
  const u32 q = (l & 15) >> 2, p = l & 3, blk = (l >> 4) & 1, h = l >> 5;
  const u32 b0 = (4 * h + q) * 128 + ((q >> 1) << 6) + 32 * blk + 8 * p + ks * 4096, b1 = b0 ^ 64;
  f32x16 c0 = {}, c1 = {};
  const Seg s0 = mkseg(b0), s1 = mkseg(b1);
  p2_h<53>(s0, s1, h1, c0, c1);
  PIN2(c0, c1);
  STAMP(10);
  u32x4 h2[14], of[8];
  ld_batch<14>(c, h2, hbase + 4096, 8192);
  const u32 obase = WS_O + (u32)(((c.t * 4 + TG) * 16 + 8 * c.kh + 4 * ks) * 2048);
  ld_batch<8>(c, of, obase, 2048);
  chk_batch<14>(c, h2, hbase + 4096, 8192);
  STAMP(11);
  p2_h<60>(s0, s1, h2, c0, c1);
  const float om = 1.0f - g2;
#pragma unroll
  for (int i = 0; i < 16; ++i) { c0[i] *= om; c1[i] *= om; }
  PIN2(c0, c1);
  STAMP(12);
  chk_batch<8>(c, of, obase, 2048);
  STAMP(13);
  static_assert(upos(70) == upos(67) + 3 * UNIT, "Wo units must be contiguous");
  u32 ob0 = b0 + ks * (80 * 128) + upos(67), ob1 = b1 + ks * (80 * 128) + upos(67);
  asm volatile("" : "+v"(ob0), "+v"(ob1));
#pragma unroll
  for (int i = 0; i < 8; ++i) {
    const int hh = i >> 1;
    const u32 o = (28 * hh - 4 * (hh & 1) + 16 * (i & 1)) * 128;
    const u32 e0 = ob0 + o, e1 = ob1 + o;
    c0 = mfma(tr_frag(e0, e0 + 1024), as_frag(of[i]), c0);
    c1 = mfma(tr_frag(e1, e1 + 1024), as_frag(of[i]), c1);
  }
  PIN2(c0, c1);
  STAMP(14);
  if constexpr (ROLE == 0) {
    const u32 so = WS_P + (u32)((c.t * 16 + c.hd) * 16384 + c.tg * 4096);
    st_dev(c, sanit(pack8<0>(c0)), l * 16, so);
    st_dev(c, sanit(pack8<1>(c0)), l * 16 + 1024, so);
    st_dev(c, sanit(pack8<0>(c1)), l * 16 + 2048, so);
    st_dev(c, sanit(pack8<1>(c1)), l * 16 + 3072, so);
    STAMP(15);
  } else {
    u32x4 pp[4];
    const u32 so = WS_P + (u32)((c.t * 16 + (c.hd ^ 8)) * 16384 + c.tg * 4096);
    ld_batch<4>(c, pp, so, 2048);
    chk_batch<4>(c, pp, so, 2048);
    STAMP(15);
#pragma unroll
    for (int d = 0; d < 4; ++d) {
      c0[2 * d] += blo(pp[0][d]); c0[2 * d + 1] += bhi(pp[0][d]); c0[8 + 2 * d] += blo(pp[1][d]); c0[9 + 2 * d] += bhi(pp[1][d]);
      c1[2 * d] += blo(pp[2][d]); c1[2 * d + 1] += bhi(pp[2][d]); c1[8 + 2 * d] += blo(pp[3][d]); c1[9 + 2 * d] += bhi(pp[3][d]);
    }
    bar_l();
    const u32 wa = (u32)(ks * 64 + tg2 * 32 + (l & 31)) * 272 + h * 16;
#pragma unroll
    for (int gq = 0; gq < 4; ++gq) {
      f32x4 v0 = {c0[4 * gq], c0[4 * gq + 1], c0[4 * gq + 2], c0[4 * gq + 3]}, v1 = {c1[4 * gq], c1[4 * gq + 1], c1[4 * gq + 2], c1[4 * gq + 3]};
      *(LAS f32x4*)(wa + gq * 32) = v0;
      *(LAS f32x4*)(wa + 128 + gq * 32) = v1;
    }
    bar_l();
#pragma unroll
    for (int it = 0; it < 2; ++it) {
      const u32 tok = c.tg * 16 + it * 8 + (l >> 3), ra = tok * 272 + (l & 7) * 32;
      f32x4 x0 = *(LAS f32x4*)(ra), x1 = *(LAS f32x4*)(ra + 16), y0 = *(LAS f32x4*)(ra + 64 * 272), y1 = *(LAS f32x4*)(ra + 64 * 272 + 16);
      x0 += y0; x1 += y1;
      u32x4 o = {pk(x0[0], x0[1]), pk(x0[2], x0[3]), pk(x1[0], x1[1]), pk(x1[2], x1[3])};
      *(u32x4*)(c.out + (size_t)(c.t * 128 + c.kh * 64 + tok) * 896 + c.jj * 128 + (l & 7) * 16) = o;
    }
    if (blockIdx.x == 0 && c.w == 4) {
      u32x2 v = {(u32)c.tag, (u32)(c.tag >> 32)};
      __builtin_amdgcn_raw_buffer_store_b64(v, c.sw, l == 0 ? 0u : OOB, 0, 16);
    }
    STAMP(16);
  }
}

template <int N> DI void spare_unit(const Ctx& c) {
  if constexpr (N < 14) dma<upos(N), (u32)((N & 1) * 64 * 896 + (N >> 1) * 128)>(c, c.vx, c.sx);
  else issue_qkv<N - 14, upos(N)>(c);
}
template <int A, int B> DI void spare_range(const Ctx& c) { if constexpr (A < B) { spare_unit<A>(c); spare_range<A + 1, B>(c); } }

template <int ROLE> DI void spare_tail(Ctx& c, const Addr& A, bf16x8 (&xf)[28]) {
  f32x16 a0 = {}, a1 = {};
  float g1 = 0.f;
  if constexpr (ROLE == 1) {
#pragma unroll
    for (int s = 0; s < 28; ++s) a1 = mfma(*(const bf16x8*)(c.gw + s * 32 + (c.lane >> 5) * 16), xf[s], a1);
    g1 = __builtin_amdgcn_rcpf(1.0f + __builtin_amdgcn_exp2f(-1.4426950408889634f * a1[0]));
    __builtin_amdgcn_raw_buffer_store_b32(__float_as_uint(g1), c.sw, c.lane < 32 ? c.lane * 4 : OOB, WS_G + (u32)(c.kh * 2048 + c.t * 128 + c.tg * 32) * 4, 16);
  }
  wait_bar<0>();
  flag_publish(c);
  u32x4 wp[2];
  const Seg aq = mkseg(A.aq);
  load_wphi(c, upos(24) + 4096 + 1024, wp);
  qkv_compute<ROLE, 0, 14>(xf, aq, a0, a1);
  qkv_compute<ROLE, 1, 14>(xf, aq, a0, a1);
  qkv_compute<ROLE, 2, 14>(xf, aq, a0, a1);
  qkv_compute<ROLE, 3, 14>(xf, aq, a0, a1);
  u32x4 phq[4];
  if constexpr (ROLE == 0) k_side(c, a0, a1, wp); else { feature_map(a0, wp, phq); PIN2(phq[0], phq[1]); PIN2(phq[2], phq[3]); }
  bar_l();
  if constexpr (ROLE == 1) kvp_publish(c);
  bar_l();
  STAMP(5);
  u32x2 f = __builtin_amdgcn_raw_buffer_load_b64(c.sw, flag_off(c), 0, 16);
  flag_spin(c, f);
  u32x4 kvv[8];
  kv_ld(c, kvv);
  kv_sum(c, kvv);
  bar_l();
  if constexpr (ROLE == 1) o_publish(c, phq, g1);
  STAMP(9);
}

__global__ __launch_bounds__(512, 2) void fused_kernel(const char* x, const char* wqkv, const char* wphi, const char* wo, const char* wg,
                                                        const char* wu, const char* wd, const char* gw, char* out, char* ws) {
  extern __shared__ __attribute__((aligned(16))) char smem[];
  Ctx c;
  const int bid = blockIdx.x, xcd = bid & 7, idx = bid >> 3;
  c.x = x; c.gw = gw; c.out = out; c.ws = ws;
  c.t = 4 * (xcd >> 1) + (idx & 3); c.kh = xcd & 1; c.jj = idx >> 2; c.hd = 8 * c.kh + c.jj; c.j = 7 * c.kh + c.jj;
  c.w = __builtin_amdgcn_readfirstlane(threadIdx.x >> 6); c.tg = c.w & 3; c.r = c.w >> 2;
  c.lane = threadIdx.x & 63; c.m0w = c.w * 1024;
  const u32 l = c.lane, w = c.w;
  STAMP(0);
  u64 epoch;
  asm volatile("s_load_dwordx2 %0, %1, 0x0" : "=s"(epoch) : "s"(ws) : "memory");
  const u32 row8 = 8 * w + (l >> 3), sw2 = ((l & 7) ^ (((l >> 4) & 1) << 2)) << 4;
  c.vx = row8 * 896 + (((l & 7) ^ ((4 * w + (l >> 4)) & 7)) << 4);
  c.vw = row8 * 3584 + sw2;
  c.v2 = row8 * 896 + sw2;
  c.vq = (16 * w + (l >> 2)) * 2688 + (l & 3) * 16;
  const u32 win = 56 * c.hd - 8 * (c.hd & 1);
  c.sx = mk(x + c.t * 114688, 114688);
  c.sq = mk(wqkv + win, 448 * 2688 - win);
  c.sp = mk(wphi + c.hd * 1568, 1568);
  c.sw = mk(ws, WS_END);
  Addr A;
  {
    const u32 q = (l & 15) >> 2, p = l & 3, blk = (l >> 4) & 1, h = l >> 5, r = l & 31;
    A.aw = mkseg((8 * h + q) * 128 + ((64 * c.r) ^ ((q >> 1) << 6)) + 32 * blk + 8 * p);
    A.aq = (8 * h + q) * 64 + 32 * blk + 8 * p;
    A.ax = (32 * c.tg + r) * 128 + ((h ^ ((r >> 1) & 7)) << 4);
  }
  const u32x4 ones = {~0u, ~0u, ~0u, ~0u};
  bf16x8 xf[28];
  if (c.jj < 7) {
    c.sg = mk(wg + c.j * 256, 448 * 3584 - c.j * 256);
    c.su = mk(wu + c.j * 256, 448 * 3584 - c.j * 256);
    c.sd = mk(wd + c.kh * 896 * 896 + c.jj * 128, 896 * 896 - c.jj * 128);
    c.so = mk(wo + c.kh * 224 * 896 + c.jj * 128, 224 * 896 - c.jj * 128);
    issue_range<0, 18>(c);

    {
      const u32 sh = WS_H + (u32)(((c.t * 4 + c.tg) * 112 + 8 * c.j + 4 + 2 * c.r) * 1024);
      st_dev(c, ones, l * 16, sh); st_dev(c, ones, l * 16 + 1024, sh);
      const u32 sp = WS_P + (u32)((c.t * 16 + c.hd) * 16384 + w * 2048);
      st_dev(c, ones, l * 16, sp); st_dev(c, ones, l * 16 + 1024, sp);
      st_dev(c, ones, l * 16, WS_O + (u32)(((c.t * 4 + c.tg) * 16 + c.hd) * 2048 + c.r * 1024));
      if (c.r == 1) st_dev(c, ones, l * 16, WS_KVP + (u32)((c.hd * 16 + c.t) * 4096 + c.tg * 1024));
    }
    asm volatile("s_waitcnt lgkmcnt(0)" : "+s"(epoch) :: "memory");
    c.tag = epoch + 1;
    bf16x8 fa[4], fb[4];
    f32x16 g = {}, u = {};
    ffn1_step<0>(c, A, xf, fa, fb, g, u);
    STAMP(1);
    ffn1_step<1>(c, A, xf, fa, fb, g, u); ffn1_step<2>(c, A, xf, fa, fb, g, u); ffn1_step<3>(c, A, xf, fa, fb, g, u);
    ffn1_step<4>(c, A, xf, fa, fb, g, u); ffn1_step<5>(c, A, xf, fa, fb, g, u); ffn1_step<6>(c, A, xf, fa, fb, g, u);
    if (c.r == 0) { ffn_tail<0>(c, A, xf, fb, g, u); __builtin_amdgcn_endpgm(); }
    ffn_tail<1>(c, A, xf, fb, g, u);
    __builtin_amdgcn_endpgm();
  } else {
    spare_range<0, 18>(c);

    st_dev(c, ones, l * 16, WS_O + (u32)(((c.t * 4 + c.tg) * 16 + c.hd) * 2048 + c.r * 1024));
    if (c.r == 1) st_dev(c, ones, l * 16, WS_KVP + (u32)((c.hd * 16 + c.t) * 4096 + c.tg * 1024));
    asm volatile("s_waitcnt lgkmcnt(0)" : "+s"(epoch) :: "memory");
    c.tag = epoch + 1;
    wait_bar<4>();
#pragma unroll
    for (int s = 0; s < 28; ++s) xf[s] = as_frag(lds_rd128((A.ax ^ (u32)(32 * (s & 3))) + upos(2 * (s >> 2))));
    bar_l();
    spare_range<18, 25>(c);
    if (c.r == 0) { spare_tail<0>(c, A, xf); __builtin_amdgcn_endpgm(); }
    spare_tail<1>(c, A, xf);
  }
}

extern "C" void kernel_launch(void* const* d_in, const int* in_sizes, int n_in, void* d_out, int out_size, void* d_ws, size_t ws_size,
                              hipStream_t stream) {
  (void)hipFuncSetAttribute((const void*)fused_kernel, hipFuncAttributeMaxDynamicSharedMemorySize, LDS_BYTES);
  fused_kernel<<<256, 512, LDS_BYTES, stream>>>((const char*)d_in[0], (const char*)d_in[1], (const char*)d_in[2], (const char*)d_in[3],
                                                (const char*)d_in[5], (const char*)d_in[6], (const char*)d_in[7], (const char*)d_in[8],
                                                (char*)d_out, (char*)d_ws);
}
```

```cpp
#include <hip/hip_runtime.h>
#include <hip/hip_bf16.h>
#include <type_traits>
#pragma clang diagnostic ignored "-Wint-to-pointer-cast"

typedef unsigned u32;
typedef unsigned long long u64;
typedef u32 u32x4 __attribute__((ext_vector_type(4)));
typedef u32 u32x2 __attribute__((ext_vector_type(2)));
typedef short s16x4 __attribute__((ext_vector_type(4)));
typedef __bf16 bf16x8 __attribute__((ext_vector_type(8)));
typedef __bf16 bf16x2 __attribute__((ext_vector_type(2)));
typedef float f32x16 __attribute__((ext_vector_type(16)));
typedef float f32x4 __attribute__((ext_vector_type(4)));
typedef float f32x2 __attribute__((ext_vector_type(2)));
typedef __amdgpu_buffer_rsrc_t rsrc_t;
#define DI __device__ __forceinline__
#define LAS __attribute__((address_space(3)))
#define SB0() __builtin_amdgcn_sched_barrier(0)
#define PIN1(a) asm volatile("" : "+v"(a))
#define PIN2(a, b) asm volatile("" : "+v"(a), "+v"(b))

constexpr u32 WS_FLAG = 4096;
constexpr u32 WS_G    = WS_FLAG + 256 * 128;
constexpr u32 WS_KVR  = 65536;
constexpr u32 WS_KVP  = 262144;
constexpr u32 WS_O    = WS_KVP + 256 * 4096;
constexpr u32 WS_P    = WS_O + 64 * 32 * 1024;
constexpr u32 WS_H    = WS_P + 256 * 16384;
constexpr u32 WS_END  = WS_H + 64 * 112 * 1024;

constexpr int UNIT = 8192, RING = 18;
constexpr u32 SCR = 0, RBASE = 2 * UNIT;
constexpr u32 TOUCH_LDS = RBASE + UNIT + 4096;
constexpr int LDS_BYTES = 163840;
constexpr u32 OOB = 0x80000000u;
constexpr u32 CODE_BYTES = 40960;
#define ABL 0
#define MARKV 0
#define TOUCH2 1
#define PSLEEP 8
#define PRIO 1
#define PROV 9
constexpr u32 SPIN_MAX = ABL ? 0 : 1u << 16;
constexpr float LOG2E = 1.4426950408889634f;

#if defined(__HIP_DEVICE_COMPILE__)
#pragma clang attribute push(__attribute__((target("no-packed-fp32-ops"))), apply_to = function)
#endif
constexpr u32 spos(int k) { return RBASE + k * UNIT; }
constexpr int w2c_(int i) { constexpr int t[18] = {0, 1, 14, 2, 3, 15, 4, 5, 16, 6, 7, 17, 8, 9, 10, 11, 12, 13}; return t[i]; }
constexpr int w2s_(int c) { constexpr int t[18] = {8, 2, 3, 4, 5, 6, 7, 9, 10, 11, 12, 13, 14, 15, -2, -1, 0, 1}; return t[c]; }
constexpr u32 upos(int n) { return spos(n < 44 ? n % RING : n < 53 ? (n + 1) % RING : w2s_(w2c_(n - 53))); }
constexpr int imin(int a, int b) { return a < b ? a : b; }

struct Ctx {
  const char* gw;
  char *out, *ws;
  int t, kh, jj, hd, j, w, tg, r;
  u32 lane, l16, m0w;
  u32 vx, vx2, vw, vq, vkv, v2, v2b;
  rsrc_t sx, sg, su, sq, sp, sd, so, sw;
  u64 tag;
  u32 rot[7];
  u32 oh, oo, op, okvp;
  u32 vflag, vtouch, awp;
};

DI rsrc_t mk(const void* p, u32 bytes) { return __builtin_amdgcn_make_buffer_rsrc((void*)p, 0, bytes, 0x00020000); }
template <u32 L> DI void set_m0(const Ctx& c) { if (!(ABL & 1)) asm volatile("s_add_u32 m0, %0, %1" :: "s"(c.m0w), "i"(L) : "memory", "scc"); }
template <u32 OFF> DI void dma(u32 voff, rsrc_t rs, u32 soff) {
  if (!(ABL & 1)) asm volatile("buffer_load_dwordx4 %0, %1, %2 offen offset:%3 lds" :: "v"(voff), "s"(rs), "s"(soff), "i"(OFF) : "memory");
}
template <int N> DI void wait_bar() { asm volatile("s_waitcnt vmcnt(%0) lgkmcnt(0)\n\ts_barrier" :: "n"(N) : "memory"); }
template <int N> DI void wait_bar_v() { asm volatile("s_waitcnt vmcnt(%0)\n\ts_barrier" :: "n"(N) : "memory"); }
DI void bar_l() { asm volatile("s_waitcnt lgkmcnt(0)\n\ts_barrier" ::: "memory"); }

constexpr int uq_(int ch) { return ch == 0 ? 28 : 30 + 3 * ((ch - 1) / 2); }
constexpr int uu_(int ch) { return ch == 0 ? 29 : 31 + 3 * ((ch - 1) / 2) + ((ch - 1) & 1); }
constexpr u32 ioff_(int n) { return n >= 39 && n < 53 ? 128 : 0; }
template <int N> DI void unit_m0(const Ctx& c) { set_m0<upos(N) - ioff_(N)>(c); }
template <int R, int N> DI void unit_go(const Ctx& c) {
  if constexpr (N < 21) {
    if constexpr (N % 3 < 2) dma<0>(N % 3 ? c.vx2 : c.vx, c.sx, c.rot[N / 3] * 128);
    else dma<0>(c.vw, c.sg, c.rot[N / 3] * (64 * 3584));
  } else if constexpr (N < 28) dma<0>(c.vkv, c.sq, c.rot[N - 21] * (64 * 2688));
  else if constexpr (N == 28) {
    if constexpr (R == 0) dma<0>(c.vq, c.sq, c.rot[0] * (64 * 2688));
    else dma<0>(c.l16, c.sp, c.w == 5 ? 0u : c.w == 6 ? 1024u : 0x40000000u);
  } else if constexpr (N < 39) {
    if constexpr (N % 3 == 0) dma<0>(c.vq, c.sq, c.rot[2 * ((N - 30) / 3) + 1 + R] * (64 * 2688));
    else dma<0>(c.vw, c.su, c.rot[N == 29 ? 0 : 2 * ((N - 30) / 3) + N % 3] * (64 * 3584));
  } else if constexpr (N < 53) dma<128>(c.vw, N < 46 ? c.sg : c.su, c.rot[(N - 39) % 7] * (64 * 3584));
  else {
    constexpr int k = w2c_(N - 53);
    if constexpr (k < 14) dma<0>(k < 7 ? c.v2 : c.v2b, c.sd, c.rot[k % 7] * (128 * 896));
    else dma<0>((k & 1) ? c.v2b : c.v2, c.so, k < 16 ? 0u : 128u * 896u);
  }
}
template <int R, int A, int B> DI void issue_range(const Ctx& c) {
  if constexpr (A < B) { unit_m0<A>(c); asm volatile("s_nop 0"); unit_go<R, A>(c); issue_range<R, A + 1, B>(c); }
}

struct Seg { u32 b[3]; };
DI Seg mkseg(u32 a) { Seg s; s.b[0] = a; s.b[1] = a + 65536; s.b[2] = a + 131072; asm volatile("" : "+v"(s.b[0]), "+v"(s.b[1]), "+v"(s.b[2])); return s; }
DI u32 sa(const Seg& s, u32 off) { return s.b[off >> 16] + (off & 0xffffu); }
DI f32x16 mfma(bf16x8 a, bf16x8 b, f32x16 c) { return __builtin_amdgcn_mfma_f32_32x32x16_bf16(a, b, c, 0, 0, 0); }
DI bf16x8 as_frag(u32x4 v) { return __builtin_bit_cast(bf16x8, v); }
DI bf16x8 tr_frag(u32 a0, u32 a1) {
  s16x4 lo = __builtin_amdgcn_ds_read_tr16_b64_v4i16((LAS s16x4*)a0);
  s16x4 hi = __builtin_amdgcn_ds_read_tr16_b64_v4i16((LAS s16x4*)a1);
  return __builtin_bit_cast(bf16x8, __builtin_shufflevector(lo, hi, 0, 1, 2, 3, 4, 5, 6, 7));
}
DI u32x4 lds_rd128(u32 a) { return *(LAS u32x4*)a; }
DI void lds_wr128(u32 a, u32x4 v) { *(LAS u32x4*)a = v; }
DI u32 f2u(float f) { return __builtin_bit_cast(u32, f); }
DI float u2f(u32 u) { return __builtin_bit_cast(float, u); }
DI u32 pk(float a, float b) { f32x2 t = {a, b}; return __builtin_bit_cast(u32, __builtin_convertvector(t, bf16x2)); }
template <int S, class V> DI u32x4 pack8(const V& v) {
  u32x4 r = {pk(v[8 * S], v[8 * S + 1]), pk(v[8 * S + 2], v[8 * S + 3]), pk(v[8 * S + 4], v[8 * S + 5]), pk(v[8 * S + 6], v[8 * S + 7])};
  return r;
}
DI float blo(u32 d) { return u2f(d << 16); }
DI float bhi(u32 d) { return u2f(d & 0xffff0000u); }
DI u32 umax(u32 a, u32 b) { return a > b ? a : b; }
DI bool any_(bool p) { return __builtin_amdgcn_ballot_w64(p) != 0; }
DI u32x4 sanit(u32x4 v) { return __builtin_elementwise_min(v, (u32x4){0xfffffffeu, 0xfffffffeu, 0xfffffffeu, 0xfffffffeu}); }
DI void st_dev(const Ctx& c, u32x4 v, u32 voff, u32 soff) {
  if (ABL & 8) { asm volatile("" :: "v"(v)); return; }
  __builtin_amdgcn_raw_buffer_store_b128(v, c.sw, voff, soff, 16);
  asm volatile("s_nop 1" :: "v"(v) : "memory");
}
DI u32x4 ld_dev(const Ctx& c, u32 voff, u32 soff) {
  if (ABL & 16) { u32x4 z = {voff, soff, voff, soff}; asm volatile("" : "+v"(z)); return z; }
  return __builtin_amdgcn_raw_buffer_load_b128(c.sw, voff, soff, 16);
}
DI f32x2 xhalf(float v) { auto p = __builtin_amdgcn_permlane32_swap(f2u(v), f2u(v), false, false); f32x2 r = {u2f(p[0]), u2f(p[1])}; return r; }

#define STAMP(i)

struct P1 {
  Seg aw, aq; u32 ax;
  bf16x8 xf[28], f[8], af[8], vf[8];
  f32x16 g1, u1, a0, a1, g2, u2, fp, k0;
  u32x4 wp[2], ph[4], h1[14], kv[4];
  float y[16], e[14], sum, mxl, mnl;
  u32x2 poll;
  float gq, gt;
  u32 hbase; int TG;
};

DI void load_wphi(const Ctx& c, u32x4 (&wp)[2]) {
  const u32 a = c.awp;
#pragma unroll
  for (int s = 0; s < 2; ++s)
#pragma unroll
    for (int d = 0; d < 4; ++d) {
      const int e0 = 2 * d, k0 = 16 * s + 8 * (e0 >> 2) + (e0 & 3);
      u32 lo = *(LAS unsigned short*)(a + 56 * k0), hi = *(LAS unsigned short*)(a + 56 * (k0 + 1));
      wp[s][d] = lo | (hi << 16);
    }
}
template <int K> DI void fm_slice(P1& p) {
  if constexpr (K == 0) { p.fp = mfma(as_frag(p.wp[0]), as_frag(pack8<0>(p.a0)), (f32x16){}); }
  else if constexpr (K == 1) { p.fp = mfma(as_frag(p.wp[1]), as_frag(pack8<1>(p.a0)), p.fp); }
  else if constexpr (K == 2) { }
  else if constexpr (K == 3) {
    PIN1(p.fp);
    float mx = p.fp[0];
#pragma unroll
    for (int i = 1; i < 14; ++i) mx = __builtin_fmaxf(mx, p.fp[i]);
    p.mxl = mx; PIN1(p.mxl);
  } else if constexpr (K == 4) {
    float mn = p.fp[0];
#pragma unroll
    for (int i = 1; i < 14; ++i) mn = __builtin_fminf(mn, p.fp[i]);
    p.mnl = mn; PIN1(p.mnl);
  } else if constexpr (K == 5) {
    f32x2 t = xhalf(p.mxl); p.mxl = __builtin_fmaxf(t[0], t[1]) * LOG2E;
    t = xhalf(p.mnl); p.mnl = __builtin_fminf(t[0], t[1]) * LOG2E;
    PIN2(p.mxl, p.mnl);
  } else {
    constexpr int NEG = K >= 15, k = K - 6 - 9 * NEG;
    if constexpr (k < 5) {
#pragma unroll
      for (int i = 3 * k; i < imin(14, 3 * k + 3); ++i) {
        p.e[i] = __builtin_amdgcn_exp2f(NEG ? __builtin_fmaf(p.fp[i], -LOG2E, p.mnl) : __builtin_fmaf(p.fp[i], LOG2E, -p.mxl));
        PIN1(p.e[i]);
      }
      if constexpr (k == 1) p.sum = p.e[0] + p.e[1] + p.e[2];
      if constexpr (k > 1) p.sum += p.e[3 * k - 3] + p.e[3 * k - 2] + p.e[3 * k - 1];
      if constexpr (k > 0) PIN1(p.sum);
    } else if constexpr (k == 5) {
      f32x2 t = xhalf(p.sum + p.e[12] + p.e[13]); p.sum = __builtin_amdgcn_rcpf(t[0] + t[1]); PIN1(p.sum);
    } else if constexpr (k < 8) {
#pragma unroll
      for (int i = 7 * (k - 6); i < 7 * (k - 5); ++i) { p.e[i] *= p.sum; PIN1(p.e[i]); }
    } else {
      u32x4 o1 = {pk(p.e[8], p.e[9]), pk(p.e[10], p.e[11]), pk(p.e[12], p.e[13]), 0u};
      p.ph[2 * NEG] = pack8<0>(p.e); p.ph[2 * NEG + 1] = o1; PIN2(p.ph[2 * NEG], p.ph[2 * NEG + 1]);
    }
  }
}
template <int A, int B> DI void fm_slices(P1& p) { if constexpr (A < B) { fm_slice<A>(p); fm_slices<A + 1, B>(p); } }
template <u32 VB, int S> DI void v_handoff(const Ctx& c, const f32x16& av) {
  u32x4 vf = pack8<S>(av);
  const bool one = (c.lane & 31) == ((c.hd & 1) ? 0u : 28u);
#pragma unroll
  for (int d = 0; d < 4; ++d) vf[d] = one ? 0x3f803f80u : vf[d];
  lds_wr128(VB + c.tg * 2048 + c.l16 + S * 1024, vf);
}
template <int T> DI void phi_image(const Ctx& c, P1& p) {
  const u32 tok = c.lane & 31, h = c.lane >> 5;
  const u32 ft = (((tok >> 1) & 1) << 3) | (tok & 1) | (((tok >> 2) & 3) << 1);
  const u32 wa = SCR + c.tg * 4096 + tok * 128 + ((h ^ ft) << 3);
#pragma unroll
  for (int g = 0; g < 4; ++g) {
    u32x2 v = {p.ph[2 * T + (g >> 1)][2 * (g & 1)], p.ph[2 * T + (g >> 1)][2 * (g & 1) + 1]};
    *(LAS u32x2*)(wa ^ (u32)((8 * T + 2 * g) << 3)) = v;
  }
}
template <u32 VB, int K> DI void kv_stage(const Ctx& c, P1& p) {
  const u32 l = c.lane, h = l >> 5;
  if constexpr (K >= 1 && K <= 4) {
    p.k0 = mfma(p.af[2 * K - 2], p.vf[2 * K - 2], K == 1 ? (f32x16){} : p.k0);
    p.k0 = mfma(p.af[2 * K - 1], p.vf[2 * K - 1], p.k0);
  }
  if constexpr (K < 4) {
    const u32 q = (l & 15) >> 2, pp = l & 3, blk = (l >> 4) & 1;
    const u32 ra = SCR + (4 * h + q) * 128 + (((4 * blk + pp) ^ (q & 1) ^ (h << 1) ^ ((q >> 1) << 3) ^ ((c.tg & 1) << 3)) << 3);
#pragma unroll
    for (int s = 0; s < 2; ++s) {
      p.af[2 * K + s] = tr_frag(ra + K * 4096 + s * 2048, (ra ^ 32u) + K * 4096 + s * 2048 + 1024);
      p.vf[2 * K + s] = as_frag(lds_rd128(VB + c.l16 + K * 2048 + s * 1024));
    }
  }
  if constexpr (K == 6) {
    const u32 vo = c.tg < 2 ? c.l16 : OOB;
    st_dev(c, sanit(pack8<0>(p.k0)), vo, c.okvp + c.tg * 1024);
    st_dev(c, sanit(pack8<1>(p.k0)), vo + 1024, c.okvp + c.tg * 1024);
  }
}
DI void add8(float (&s)[8], u32x4 v) {
#pragma unroll
  for (int d = 0; d < 4; ++d) { s[2 * d] += blo(v[d]); s[2 * d + 1] += bhi(v[d]); }
}
DI void set8(float (&s)[8], u32x4 v) {
#pragma unroll
  for (int d = 0; d < 4; ++d) { s[2 * d] = blo(v[d]); s[2 * d + 1] = bhi(v[d]); }
}
template <int A, int B, int N> DI bool has_mark(const u32x4 (&v)[N]) {
  u32 m = 0;
#pragma unroll
  for (int i = A; i < B; ++i) m = umax(umax(m, v[i][0]), umax(v[i][1], umax(v[i][2], v[i][3])));
  return any_(m == 0xffffffffu);
}
template <int A, int B, int N, class F> DI void chk(u32x4 (&v)[N], F&& reload) {
  if (__builtin_expect(SPIN_MAX && has_mark<A, B>(v), 0)) {
    u32 spin = 0;
    do { asm volatile("" ::: "memory"); reload(); } while (has_mark<A, B>(v) && ++spin < SPIN_MAX);
  }
}
template <int A, int B, int N> DI void ld_lin(const Ctx& c, u32x4 (&v)[N], u32 soff, u32 stride) {
#pragma unroll
  for (int i = A; i < B; ++i) v[i] = ld_dev(c, c.l16, soff + i * stride);
}
template <int A, int B, int N> DI void ld_1k(const Ctx& c, u32x4 (&v)[N], u32 soff) {
#pragma unroll
  for (int i = A; i < B; ++i) v[i] = ld_dev(c, c.l16 + (i & 3) * 1024, soff + (i >> 2) * 4096);
}
template <int A, int B> DI void ld_h(const Ctx& c, u32x4 (&v)[14], u32 voff, u32 soff) {
#pragma unroll
  for (int i = A; i < B; ++i) v[i] = ld_dev(c, voff + (i & 1) * 1024, soff + c.rot[i >> 1] * 8192);
}
DI void o_publish(const Ctx& c, const u32x4 (&kv)[4], const u32x4 (&phq)[4], float g) {
  f32x16 n = {};
#pragma unroll
  for (int i = 0; i < 4; ++i) n = mfma(as_frag(kv[i]), as_frag(phq[i]), n);
  const bool odd = c.hd & 1;
  f32x2 t = xhalf(odd ? n[0] : n[12]);
  float sc = g * __builtin_amdgcn_rcpf((odd ? t[0] : t[1]) + 1e-6f);
#pragma unroll
  for (int i = 0; i < 16; ++i) n[i] *= sc;
  u32x4 f0 = sanit(pack8<0>(n)), f1 = sanit(pack8<1>(n));
  const bool z0 = odd && c.lane < 32, z1 = !odd && c.lane >= 32;
  f0[0] = z0 ? 0 : f0[0]; f0[1] = z0 ? 0 : f0[1];
  f1[2] = z1 ? 0 : f1[2]; f1[3] = z1 ? 0 : f1[3];
  st_dev(c, f0, c.l16, c.oo);
  st_dev(c, f1, c.l16 + 1024, c.oo);
}
DI u32 flag_off(const Ctx& c, bool spare) {
  const u32 i = c.lane & 15;
  u32 red = (c.hd * 16 + 7 + 8 * (i & 1)) << 7, late = ((c.t * 16 + 7 + 2 * (i & 4)) << 7) + 32 + (i & 3) * 8;
  u32 lo = (spare ? i * 16 + c.t : c.t * 16 + i) << 7;
  asm volatile("" : "+v"(red), "+v"(late), "+v"(lo));
  return WS_FLAG + (!(c.lane & 16) ? lo : (spare || (i & 8)) ? red : late);
}
DI bool flag_bad(const Ctx& c, u32x2 f) { return any_(f[0] != (u32)c.tag || f[1] != (u32)(c.tag >> 32)); }
DI void flag_spin(const Ctx& c, u32x2 f) {
  if (__builtin_expect(SPIN_MAX && flag_bad(c, f), 0)) {
    u32 spin = 0;
    do {
      __builtin_amdgcn_s_sleep(2);
      asm volatile("" ::: "memory");
      f = __builtin_amdgcn_raw_buffer_load_b64(c.sw, c.vflag, 0, 16);
    } while (flag_bad(c, f) && ++spin < SPIN_MAX);
  }
  asm volatile("" ::: "memory");
}
DI void flag_publish(const Ctx& c) {
  u32x2 v = {(u32)c.tag, (u32)(c.tag >> 32)};
  __builtin_amdgcn_raw_buffer_store_b64(v, c.sw, c.lane + c.w == 0 ? 0u : OOB, WS_FLAG + (u32)(c.t * 16 + c.hd) * 128, 16);
}
DI float ld_g(const Ctx& c, int tgroup) {
  return u2f(__builtin_amdgcn_raw_buffer_load_b32(c.sw, (c.lane & 31) * 4, WS_G + (u32)(c.kh * 2048 + c.t * 128 + tgroup * 32) * 4, 16));
}
template <int TG> DI void mark_tg(const Ctx& c, u32 voff, u32 soff) {
  if (ABL & 8) return;
  const u32x4 ones = {~0u, ~0u, ~0u, ~0u};
  asm volatile("s_cmp_lg_u32 %0, %1\n\ts_cbranch_scc1 1f\n\tbuffer_store_dwordx4 %2, %3, %4, %5 offen sc1\n\ts_nop 1\n1:"
               :: "s"(c.tg), "i"(TG), "v"(ones), "v"(voff), "s"(c.sw), "s"(soff) : "memory", "scc");
}
DI void touch(const Ctx& c, u32 soff) {
  if (ABL & 8) return;
  asm volatile("s_mov_b32 m0, %0\n\ts_nop 0\n\tbuffer_load_dword %1, %2, %3 offen sc1 lds"
               :: "i"(TOUCH_LDS), "v"(c.vtouch), "s"(c.sw), "s"(soff) : "memory");
}

constexpr int PRO = PROV;
constexpr int need_(int s) { return s < 0 ? 0 : s < 7 ? 3 * (s + 1) : s < 10 ? 23 + 2 * (s - 7) : s == 10 ? 28 : s < 15 ? 30 + 3 * (s - 11) : 41 + 2 * (s - 15); }
constexpr int iss_(int s) {
  constexpr int t[6] = {56, 59, 62, 65, 67, 71};
  return s < 0 ? PRO : s >= 16 ? t[s - 16] : imin(18 + need_(s - 2) - (s >= 11 ? 1 : 0), PRO + 3 * (s + 1));
}
static_assert(iss_(10) <= 44 && iss_(15) == 53, "ring schedule");
constexpr int S_FLAG = 13, S_POLL = 18;
constexpr int nmark_(int r, int s) { return s == 6 ? 1 - r : (s >= 7 && s <= 9) ? 1 : s == 10 ? 1 - r : s == 11 ? r : 0; }
constexpr int oth_(int r, int s) {
  return nmark_(r, s) + (s == S_FLAG ? 1 - r : s == 14 ? 2 - 2 * r : 0) + (s == 15 ? 2 : s == S_POLL ? 2 : s == 19 ? 5 + r : s == 20 ? 5 : s == 21 ? 5 + 4 * r : 0);
}
constexpr int tot_(int r, int s) { return iss_(s) - iss_(s - 1) + oth_(r, s); }
constexpr int vm_(int r, int s) {
  int sp = -1; while (iss_(sp) < need_(s)) ++sp;
  int n = iss_(sp) - need_(s);
  for (int k = sp + 1; k < s; ++k) n += tot_(r, k);
  if (s == S_FLAG) n = imin(n, tot_(r, s - 1));
  if (s == 21) n = imin(n, tot_(r, 20) + 2);
  if (s == S_POLL + 1) n = imin(n, tot_(r, S_POLL) - 1);
  return n;
}
constexpr int F2_(int r) { return r ? 112 : 84; }
constexpr int item0_(int r, int s) { return s < 7 ? 4 * s : s < 11 ? 28 + 8 * (s - 7) : s == 11 ? 56 : s < 15 ? 56 + (1 + r) * (8 * (s - 12) + 4) : F2_(r) + 8 * (s - 15); }

template <int R, int J> DI void rd_item(P1& p) {
  constexpr bool att = (J >= 28 && J < 56) || (R == 1 && J >= 56 && J < 112 && (J & 1));
  if constexpr (att) {
    constexpr int k = J < 56 ? J - 28 : (J - 56) / 2, ch = k / 4;
    constexpr u32 o = (J < 56 ? upos(21 + ch) + R * 4096 : upos(uq_(ch)) + (ch > 0 && !(ch & 1) ? 4096 : 0)) + (k % 4) * 1024;
    p.f[J % 8] = tr_frag(sa(p.aq, o), sa(p.aq, o + 256));
  } else {
    constexpr int k = J < 28 ? J : J < F2_(R) ? (J - 56) / (1 + R) : J - F2_(R);
    constexpr u32 o = upos(J < 28 ? 3 * (k / 4) + 2 : J < F2_(R) ? uu_(k / 4) : 39 + k / 4) + (k % 4) * 2048;
    p.f[J % 8] = tr_frag(sa(p.aw, o), sa(p.aw, o + 512));
  }
}
template <int R, int J> DI void mm_item(P1& p) {
  if constexpr (J < 0) {}
  else if constexpr (J < 28) p.g1 = mfma(p.f[J % 8], p.xf[J], p.g1);
  else if constexpr (J < 56) { if constexpr (R == 0) p.a0 = mfma(p.f[J % 8], p.xf[J - 28], p.a0); else p.a1 = mfma(p.xf[J - 28], p.f[J % 8], p.a1); }
  else if constexpr (J < F2_(R)) {
    constexpr int k = (J - 56) / (1 + R);
    if constexpr (R == 1 && (J & 1)) p.a0 = mfma(p.f[J % 8], p.xf[k], p.a0); else p.u1 = mfma(p.f[J % 8], p.xf[k], p.u1);
  } else { constexpr int k = J - F2_(R); if constexpr (k < 28) p.g2 = mfma(p.f[J % 8], p.xf[k], p.g2); else p.u2 = mfma(p.f[J % 8], p.xf[k - 28], p.u2); }
}
template <int R, int A, int B> DI void mm_range(P1& p) { if constexpr (A < B) { mm_item<R, A>(p); mm_range<R, A + 1, B>(p); } }

constexpr float GK1 = -2.0f * 0.7978845608028654f * LOG2E, GK2 = GK1 * 0.044715f;
template <int OP> DI void gelu_op(float (&y)[16], const f32x16& g) {
  constexpr int st = OP % 4, i = OP / 4 - st;
  if constexpr (i >= 0 && i < 16) {
    if constexpr (st == 0) y[i] = g[i] * __builtin_fmaf(g[i] * g[i], GK2, GK1);
    else if constexpr (st == 1) y[i] = __builtin_amdgcn_exp2f(y[i]);
    else if constexpr (st == 2) y[i] = __builtin_amdgcn_rcpf(1.0f + y[i]);
    else y[i] = g[i] * y[i];
    PIN1(y[i]);
  }
}
constexpr int gcost(int op) { return op / 4 - op % 4 < 0 || op / 4 - op % 4 > 15 ? 0 : op % 4 == 0 ? 3 : op % 4 == 1 ? 2 : op % 4 == 2 ? 3 : 1; }
constexpr int gcum(int k, int n) { if (k >= n) return 76; int op = 0, cst = 0; while (cst * n < k * 144) cst += gcost(op++); return op; }
template <int A, int B> DI void gelu_ops(float (&y)[16], const f32x16& g) { if constexpr (A < B) { gelu_op<A>(y, g); gelu_ops<A + 1, B>(y, g); } }
template <int S> DI void h_store(const Ctx& c, const float (&y)[16], const f32x16& u, int blk) {
  float h[8];
#pragma unroll
  for (int i = 0; i < 8; ++i) h[i] = y[8 * S + i] * u[8 * S + i];
  st_dev(c, sanit(pack8<0>(h)), c.l16 + S * 1024, c.oh + blk * 4096);
}
template <int R, int S, int K, int J> DI void slot_work(Ctx& c, P1& p) {
  if constexpr (S < 7) p.xf[J] = as_frag(lds_rd128((p.ax + upos(3 * S)) ^ (u32)(32 * K)));
  if constexpr (nmark_(R, S) > 0) {
    if constexpr (S == 6) mark_tg<K>(c, c.l16, c.okvp);
    else if constexpr (S == 10) mark_tg<K>(c, c.l16 + 1024, c.oh + 4096);
    else if constexpr ((K & 1) == R) mark_tg<K / 2>(c, S == 8 || S == 11 ? c.l16 + 1024 : c.l16, S >= 9 ? c.oh + 4096 : c.oh);
  }
  if constexpr (R == 0) {
    if constexpr (J >= 33 && J < 60) gelu_ops<gcum(J - 33, 27), gcum(J - 32, 27)>(p.y, p.g1);
    if constexpr (J == 56) load_wphi(c, p.wp);
    if constexpr (J >= 60 && J < 76) fm_slices<(3 * (J - 60) + 1) / 2, J == 75 ? 24 : (3 * (J - 59) + 1) / 2>(p);
    if constexpr (J == 70) phi_image<0>(c, p);
    if constexpr (J == 75) phi_image<1>(c, p);
    if constexpr (J >= 76 && J < 83) kv_stage<spos(8), J - 76>(c, p);
  } else {
    constexpr int G = J < 56 ? J - 33 : J - 96 + 23;
    if constexpr ((J >= 33 && J < 56) || (J >= 96 && J < 112)) gelu_ops<gcum(G, 39), gcum(G + 1, 39)>(p.y, p.g1);
    if constexpr (J == 61 || J == 62) v_handoff<spos(8), J - 61>(c, p.a1);
    if constexpr (J == 57) load_wphi(c, p.wp);
    if constexpr (J == 60) PIN2(p.wp[0], p.wp[1]);
  }
  if constexpr (S >= 15) {
    constexpr int g = J - F2_(R);
    if constexpr (g == 5 || g == 6) h_store<g - 5>(c, p.y, p.u1, 0);
    if constexpr (R == 1 && g >= 7 && g < 31) fm_slice<g - 7>(p);
    if constexpr (g >= 33) gelu_ops<gcum(g - 33, 23), gcum(g - 32, 23)>(p.y, p.g2);
    if constexpr (g == 26) touch(c, c.oh);
    if constexpr (R == 1 && S == 21 && !(K & 1)) ld_1k<K / 2, K / 2 + 1>(c, p.kv, WS_KVR + (u32)c.hd * 4096);
    if constexpr (S == 19 && K == 0) { p.gt = ld_g(c, p.TG); if constexpr (R == 1) p.gq = ld_g(c, c.tg); }
    if constexpr (S == 19 && (K & 1)) ld_h<K / 2, K / 2 + 1>(c, p.h1, c.l16, p.hbase);
    if constexpr (S >= 20 && ((K & 1) || K == 4)) { constexpr int i = 4 + 5 * (S - 20) + (K < 4 ? K / 2 : (K + 1) / 2); ld_h<i, i + 1>(c, p.h1, c.l16, p.hbase); }
  }
}
template <int R, int S, int J0, int K, int N> DI void slots(Ctx& c, P1& p) {
  if constexpr (K < N) {
    constexpr int nu = iss_(S) - iss_(S - 1), u0 = iss_(S - 1);
    constexpr int ua = K == 0 ? 0 : ((2 * K + R) * nu) / (2 * N), ub = K == N - 1 ? nu : ((2 * K + 2 + R) * nu) / (2 * N);
    if constexpr (R == 1 && PRIO) {
      if constexpr (K == 0 && S >= 15 && S < 19) __builtin_amdgcn_s_setprio(1);
      if constexpr (S >= 19 && (K == 0 || K == 4)) __builtin_amdgcn_s_setprio(K == 0);
    }
    mm_item<R, J0 + K - 4>(p);
    if constexpr (ub > ua) unit_m0<u0 + ua>(c);
    rd_item<R, (J0 + K) ^ 1>(p);
    if constexpr (ub > ua) { unit_go<R, u0 + ua>(c); issue_range<R, u0 + ua + 1, u0 + ub>(c); }
    slot_work<R, S, K, J0 + K>(c, p);
    SB0();
    slots<R, S, J0, K + 1, N>(c, p);
  }
}
template <int R, int S> DI void step(Ctx& c, P1& p) {
  constexpr int j0 = item0_(R, S), n = item0_(R, S + 1) - j0;
  if constexpr (S == S_POLL + 1) {
    asm volatile("s_waitcnt vmcnt(%1)\n\ts_barrier" : "+v"(p.poll) : "n"(vm_(R, S)) : "memory");
    PIN1(p.g2);
    flag_spin(c, p.poll);
  } else if constexpr ((S == 14 && R == 0) || S == 21) wait_bar<vm_(R, S)>();
  else wait_bar_v<vm_(R, S)>();
  if constexpr (S < 2 || S >= 6) STAMP(S < 2 ? S + 1 : S - 3);
  if constexpr (S == S_FLAG && R == 0) flag_publish(c);
  if constexpr (S == S_POLL) asm volatile("buffer_load_dwordx2 %0, %1, %2, 0 offen sc1" : "=v"(p.poll) : "v"(c.vflag), "s"(c.sw) : "memory");
  SB0();
  slots<R, S, j0, 0, n>(c, p);
}
template <int R, int S, int E> DI void steps(Ctx& c, P1& p) { if constexpr (S < E) { step<R, S>(c, p); steps<R, S + 1, E>(c, p); } }

struct P2 { Seg s[2]; u32 ob[2]; bf16x8 f[8]; f32x16 c[2]; u32x4 h2[14], of[8]; };
template <int J> DI void rd2(P2& q) {
  if constexpr (J < 56) {
    constexpr u32 o = spos(w2s_(J / 4)) + ((J / 2) & 1) * 2048;
    q.f[J % 8] = tr_frag(sa(q.s[J & 1], o), sa(q.s[J & 1], o + 1024));
  } else if constexpr (J < 72) {
    constexpr int i = (J - 56) / 2, hh = i >> 1;
    constexpr u32 o = (28 * hh - 4 * (hh & 1) + 16 * (i & 1)) * 128;
    q.f[J % 8] = tr_frag(q.ob[J & 1] + o, q.ob[J & 1] + o + 1024);
  }
}
template <int J> DI void mm2(P2& q, const P1& p) {
  q.c[J & 1] = mfma(q.f[J % 8], as_frag(J < 28 ? p.h1[J / 2] : J < 56 ? q.h2[(J - 28) / 2] : q.of[(J - 56) / 2]), q.c[J & 1]);
}
template <int A, int B, class F> DI void run2(P2& q, const P1& p, F&& extra) {
  if constexpr (A < B) {
    mm2<A>(q, p); rd2<(A + 4) ^ 1>(q);
    extra(std::integral_constant<int, A>{});
    SB0();
    run2<A + 1, B>(q, p, extra);
  } else { PIN2(q.c[0], q.c[1]); }
}

template <int R> DI void ffn_wg(Ctx& c, P1& p, u64 epoch) {
  const u32 l = c.lane;
  const int tg2 = c.tg & 1, ks = c.tg >> 1;
  issue_range<R, 0, PRO>(c);
  p.TG = 2 * (R == 1 ? c.kh : 1 - c.kh) + tg2;
  p.hbase = WS_H + (u32)(((c.t * 4 + p.TG) * 112 + 56 * c.kh + 2 * ks) * 1024);
  c.tag = epoch + 1;
  asm volatile("" : "+s"(c.tag), "+s"(p.hbase), "+v"(c.vflag), "+v"(c.vtouch), "+v"(c.awp) :: "memory");
  p.g1 = p.u1 = p.a0 = p.a1 = p.g2 = p.u2 = (f32x16){};
  steps<R, 0, 22>(c, p);
  mm_range<R, F2_(R) + 52, F2_(R) + 56>(p);
  SB0();
  STAMP(19);
  h_store<0>(c, p.y, p.u2, 1);
  h_store<1>(c, p.y, p.u2, 1);
  if constexpr (R == 1) {
    chk<0, 4>(p.kv, [&] { ld_1k<0, 4>(c, p.kv, WS_KVR + (u32)c.hd * 4096); });
    STAMP(22);
    o_publish(c, p.kv, p.ph, p.gq);
  }
  P2 q;
  const u32 qq = (l & 15) >> 2, pp_ = l & 3, blk = (l >> 4) & 1, h = l >> 5;
  const u32 b0 = (4 * h + qq) * 128 + ((qq >> 1) << 6) + 32 * blk + 8 * pp_;
  q.s[0] = mkseg(b0 + ks * 4096); q.s[1] = mkseg((b0 ^ 64) + ks * 4096);
  q.ob[0] = b0 + ks * (112 * 128); q.ob[1] = (b0 ^ 64) + ks * (112 * 128);
  PIN2(q.ob[0], q.ob[1]);
  q.c[0] = q.c[1] = (f32x16){};
  rd2<1>(q); rd2<0>(q); rd2<3>(q); rd2<2>(q);
  auto none = [](auto) {};
  const u32 obase = WS_O + (u32)(((c.t * 4 + p.TG) * 16 + 8 * c.kh + 4 * ks) * 2048);
  const u32 l16h = c.l16 + 4096;
  chk<0, 14>(p.h1, [&] { ld_h<0, 14>(c, p.h1, c.l16, p.hbase); });
  STAMP(20);
  constexpr int JC = R ? 8 : 24;
  run2<0, JC>(q, p, none);
  wait_bar_v<2 + 2 * R>();
  STAMP(21);
  if (TOUCH2) touch(c, c.oh + 4096);
  run2<JC, 28>(q, p, [&](auto J) { if constexpr (TOUCH2 && R == 1 && J.value == 16) touch(c, c.oo); });
  STAMP(23);
  if constexpr (R == 0) __builtin_amdgcn_s_sleep(PSLEEP);
  ld_h<0, 14>(c, q.h2, l16h, p.hbase);
  STAMP(24);
  chk<0, 7>(q.h2, [&] { ld_h<0, 7>(c, q.h2, l16h, p.hbase); });
  STAMP(25);
  run2<28, 42>(q, p, [&](auto J) { if constexpr (J.value >= 34) ld_1k<J.value - 34, J.value - 33>(c, q.of, obase); });
  chk<7, 14>(q.h2, [&] { ld_h<7, 14>(c, q.h2, l16h, p.hbase); });
  STAMP(26);
  run2<42, 56>(q, p, none);
  const float om = 1.0f - p.gt;
#pragma unroll
  for (int i = 0; i < 16; ++i) { q.c[0][i] *= om; q.c[1][i] *= om; }
  PIN2(q.c[0], q.c[1]);
  STAMP(27);
  chk<0, 8>(q.of, [&] { ld_1k<0, 8>(c, q.of, obase); });
  STAMP(28);
  u32x4 pp[4];
  const u32 so = c.op ^ (R ? 8 * 16384 : 0);
  run2<56, 72>(q, p, [&](auto J) { if constexpr (R == 1 && J.value >= 68) ld_1k<J.value - 68, J.value - 67>(c, pp, so); });
  f32x16 &c0 = q.c[0], &c1 = q.c[1];
  STAMP(29);
  if constexpr (R == 0) {
    st_dev(c, sanit(pack8<0>(c0)), c.l16, so);
    st_dev(c, sanit(pack8<1>(c0)), c.l16 + 1024, so);
    st_dev(c, sanit(pack8<0>(c1)), c.l16 + 2048, so);
    st_dev(c, sanit(pack8<1>(c1)), c.l16 + 3072, so);
    STAMP(30);
  } else {
    chk<0, 4>(pp, [&] { ld_1k<0, 4>(c, pp, so); });
    STAMP(30);
#pragma unroll
    for (int d = 0; d < 4; ++d) {
      c0[2 * d] += blo(pp[0][d]); c0[2 * d + 1] += bhi(pp[0][d]); c0[8 + 2 * d] += blo(pp[1][d]); c0[9 + 2 * d] += bhi(pp[1][d]);
      c1[2 * d] += blo(pp[2][d]); c1[2 * d + 1] += bhi(pp[2][d]); c1[8 + 2 * d] += blo(pp[3][d]); c1[9 + 2 * d] += bhi(pp[3][d]);
    }
    bar_l();
    const u32 wa = (u32)(ks * 64 + tg2 * 32 + (l & 31)) * 272 + h * 16;
#pragma unroll
    for (int gq = 0; gq < 4; ++gq) {
      f32x4 v0 = {c0[4 * gq], c0[4 * gq + 1], c0[4 * gq + 2], c0[4 * gq + 3]}, v1 = {c1[4 * gq], c1[4 * gq + 1], c1[4 * gq + 2], c1[4 * gq + 3]};
      *(LAS f32x4*)(wa + gq * 32) = v0;
      *(LAS f32x4*)(wa + 128 + gq * 32) = v1;
    }
    bar_l();
#pragma unroll
    for (int it = 0; it < 2; ++it) {
      const u32 tok = c.tg * 16 + it * 8 + (l >> 3), ra = tok * 272 + (l & 7) * 32;
      f32x4 x0 = *(LAS f32x4*)(ra), x1 = *(LAS f32x4*)(ra + 16), y0 = *(LAS f32x4*)(ra + 64 * 272), y1 = *(LAS f32x4*)(ra + 64 * 272 + 16);
      x0 += y0; x1 += y1;
      u32x4 o = {pk(x0[0], x0[1]), pk(x0[2], x0[3]), pk(x1[0], x1[1]), pk(x1[2], x1[3])};
      *(u32x4*)(c.out + (size_t)(c.t * 128 + c.kh * 64 + tok) * 896 + c.jj * 128 + (l & 7) * 16) = o;
    }
    if (__builtin_amdgcn_workgroup_id_x() == 0 && c.w == 4) {
      u32x2 v = {(u32)c.tag, (u32)(c.tag >> 32)};
      __builtin_amdgcn_raw_buffer_store_b64(v, c.sw, l == 0 ? 0u : OOB, 0, 16);
    }
    STAMP(31);
  }
}

template <int R, int A0, int A, int B, int ST> DI void pipe(P1& p) {
  if constexpr (A - 3 * ST < B) {
    if constexpr (A - 3 * ST >= A0) mm_item<R, A - 3 * ST>(p);
    if constexpr (A < B) rd_item<R, A>(p);
    SB0();
    pipe<R, A0, A + ST, B, ST>(p);
  }
}
constexpr u32 sxpos(int ch) { return spos(ch < 2 ? 2 * ch - 2 : 3 + 2 * ch); }
constexpr u32 NB1 = 8, NB2 = 16;
template <int N, int E> DI void spare_x(const Ctx& c) {
  if constexpr (N < E) { set_m0<sxpos(N >> 1) + (N & 1) * UNIT>(c); asm volatile("s_nop 0"); dma<0>((N & 1) ? c.vx2 : c.vx, c.sx, c.rot[N >> 1] * 128); spare_x<N + 1, E>(c); }
}

DI void bulk_mark(const Ctx& c, u32 i) {
  const u32x4 ones = {~0u, ~0u, ~0u, ~0u};
  u32 o;
  if (i < 112) o = WS_P + (c.t * 16 + 8 * c.kh) * 16384 + i * 1024;
  else o = WS_O + ((c.t * 4 + ((i - 112) >> 4)) * 16 + 8 * c.kh) * 2048 + ((i - 112) & 15) * 1024;
  st_dev(c, ones, c.l16, o);
}
template <int R> DI void spare_wg(Ctx& c, P1& p, u64 epoch) {
  const u32 l = c.lane;
  spare_x<0, 4>(c);
  set_m0<spos(2)>(c); asm volatile("s_nop 0"); dma<0>(c.l16, mk(c.gw, 896), 0);
  const u32 okvr = WS_KVR + (u32)(c.t * 4096 + (2 * c.kh + (c.w & 1)) * 1024);
  { const u32x4 ones = {~0u, ~0u, ~0u, ~0u}; st_dev(c, ones, R == 0 || c.tg < 2 ? c.l16 : OOB, R == 0 ? c.okvp : okvr); }
#pragma unroll 1
  for (u32 i = 0; i < NB1; ++i) bulk_mark(c, 176 - 8 * NB1 + i * 8 + c.w);
  spare_x<4, 14>(c);
  issue_range<R, 21, 25>(c);
  c.tag = epoch + 1;
  asm volatile("" : "+s"(c.tag) :: "memory");
  wait_bar<4>();
#pragma unroll
  for (int s = 0; s < 28; ++s) p.xf[s] = as_frag(lds_rd128((p.ax ^ (u32)(32 * (s & 3))) + sxpos(s >> 2)));
  bar_l();
  STAMP(1);
  issue_range<R, 25, 29>(c);
  issue_range<R, 30, 31>(c);
  issue_range<R, 33, 34>(c);
  issue_range<R, 36, 37>(c);
  f32x16 ga = {};
#pragma unroll
  for (int s = 14 * R; s < 14 * R + 14; ++s) ga = mfma(as_frag(lds_rd128(spos(2) + c.rot[s >> 2] * 128 + (s & 3) * 32 + (l >> 5) * 16)), p.xf[s], ga);
  PIN1(ga);
  p.a0 = p.a1 = (f32x16){};
  float g1 = 0.f;
  if constexpr (R == 0) *(LAS float*)(SCR + c.tg * 4096 + l * 4) = ga[0];
  wait_bar<7>();
  if constexpr (R == 0) flag_publish(c);
  if constexpr (R == 1) {
    g1 = __builtin_amdgcn_rcpf(1.0f + __builtin_amdgcn_exp2f(-LOG2E * (ga[0] + *(LAS float*)(SCR + c.tg * 4096 + l * 4))));
    __builtin_amdgcn_raw_buffer_store_b32(f2u(g1), c.sw, l < 32 ? l * 4 : OOB, WS_G + (u32)(c.kh * 2048 + c.t * 128 + c.tg * 32) * 4, 16);
  }
  pipe<R, 28, 28, 44, 1>(p);
  wait_bar_v<1>();
  STAMP(2);
  load_wphi(c, p.wp);
  pipe<R, 44, 44, 56, 1>(p);
  if constexpr (R == 0) {
    fm_slices<0, 24>(p);
    phi_image<0>(c, p); phi_image<1>(c, p);
  } else {
    v_handoff<spos(11), 0>(c, p.a1); v_handoff<spos(11), 1>(c, p.a1);
#pragma unroll 1
    for (u32 i = 0; i < NB2; ++i) bulk_mark(c, i * 4 + c.tg);
  }
  bar_l();
  if constexpr (R == 0) {
    if (c.w >= 2) return;
    kv_stage<spos(11), 0>(c, p); kv_stage<spos(11), 1>(c, p); kv_stage<spos(11), 2>(c, p); kv_stage<spos(11), 3>(c, p); kv_stage<spos(11), 4>(c, p);
    kv_stage<spos(11), 6>(c, p);
    STAMP(3);
    flag_spin(c, __builtin_amdgcn_raw_buffer_load_b64(c.sw, c.vflag, 0, 16));
    STAMP(4);
    u32x4 v[16];
    const u32 ro = WS_KVP + (u32)(c.t * 65536 + (2 * c.kh + c.w) * 1024);
    ld_lin<0, 16>(c, v, ro, 4096);
    chk<0, 16>(v, [&] { ld_lin<0, 16>(c, v, ro, 4096); });
    STAMP(5);
    float s[8], s2[8];
    set8(s, v[0]); set8(s2, v[1]);
#pragma unroll
    for (int i = 2; i < 16; i += 2) { add8(s, v[i]); add8(s2, v[i + 1]); }
#pragma unroll
    for (int i = 0; i < 8; ++i) s[i] += s2[i];
    st_dev(c, sanit(pack8<0>(s)), c.l16, okvr);
    STAMP(6);
  } else {
#pragma unroll 1
    for (u32 i = NB2; i < (176 - 8 * NB1) / 4; ++i) bulk_mark(c, i * 4 + c.tg);
    pipe<R, 57, 57, 112, 2>(p); PIN1(p.a0);
    asm volatile("s_waitcnt vmcnt(0)" ::: "memory");
    { u32x2 v = {(u32)c.tag, (u32)(c.tag >> 32)};
      __builtin_amdgcn_raw_buffer_store_b64(v, c.sw, l == 0 ? 0u : OOB, WS_FLAG + (u32)(c.t * 16 + c.hd) * 128 + c.w * 8, 16); }
    STAMP(3);
    fm_slices<0, 24>(p);
    flag_spin(c, __builtin_amdgcn_raw_buffer_load_b64(c.sw, c.vflag, 0, 16));
    u32x4 v[4];
    const u32 kvo = WS_KVR + (u32)c.hd * 4096;
    ld_1k<0, 4>(c, v, kvo);
    chk<0, 4>(v, [&] { __builtin_amdgcn_s_sleep(4); ld_1k<0, 4>(c, v, kvo); });
    o_publish(c, v, p.ph, g1);
    STAMP(7);
  }
}

extern "C" __global__ __launch_bounds__(512, 2) void fused_kernel(const char* x, const char* wqkv, const char* wphi, const char* wo, const char* wg,
                                                                   const char* wu, const char* wd, const char* gw, char* out, char* ws) {
  Ctx c;
  const int bid = __builtin_amdgcn_workgroup_id_x(), xcd = bid & 7, idx = bid >> 3;
  const u32 tid = __builtin_amdgcn_workitem_id_x();
  c.gw = gw; c.out = out; c.ws = ws;
  c.t = 4 * (xcd >> 1) + (idx & 3); c.kh = xcd & 1; c.jj = idx >> 2; c.hd = 8 * c.kh + c.jj; c.j = 7 * c.kh + c.jj;
  c.w = __builtin_amdgcn_readfirstlane(tid >> 6); c.tg = c.w & 3; c.r = c.w >> 2;
  c.lane = tid & 63; c.l16 = c.lane * 16; c.m0w = c.w * 1024;
  const u32 l = c.lane, w = c.w;
  STAMP(0);
  const u64 epoch = *(const __attribute__((address_space(4))) u64*)ws;
  {
    u32 c0 = (u32)(c.jj + 2 * (idx & 3) + (xcd >> 1)) % 7u;
#pragma unroll
    for (int i = 0; i < 7; ++i) { c.rot[i] = c0; c0 = c0 == 6 ? 0 : c0 + 1; }
  }
  const u32 row8 = 8 * w + (l >> 3), sw2 = ((l & 7) ^ (((l >> 4) & 1) << 2)) << 4;
  c.vx = row8 * 896 + (((l & 7) ^ ((4 * w + (l >> 4)) & 7)) << 4); c.vx2 = c.vx + 64 * 896;
  c.vw = row8 * 3584 + sw2;
  c.v2 = row8 * 896 + sw2; c.v2b = c.v2 + 64 * 896;
  c.vq = (16 * c.tg + (l >> 2)) * 2688 + (l & 3) * 16; c.vkv = c.vq + 896 * (1 + c.r);
  const u32 win = 56 * c.hd - 8 * (c.hd & 1);
  c.sx = mk(x + c.t * 114688, 114688);
  c.sq = mk(wqkv + win, 448 * 2688 - win);
  c.sp = mk(wphi + c.hd * 1568, 1568);
  c.sw = mk(ws, WS_END);
  c.oh = WS_H + (u32)(((c.t * 4 + c.tg) * 112 + 8 * c.j + 2 * c.r) * 1024);
  c.oo = WS_O + (u32)(((c.t * 4 + c.tg) * 16 + c.hd) * 2048);
  c.op = WS_P + (u32)((c.t * 16 + c.hd) * 16384 + c.tg * 4096);
  c.okvp = WS_KVP + (u32)((c.hd * 16 + c.t) * 4096 + c.tg * 1024);
  static_assert(WS_P % (16 * 16384) == 0, "the partner's slot is op ^ (8 * 16384)");
  asm volatile("" : "+s"(c.oh), "+s"(c.oo), "+s"(c.op), "+s"(c.okvp));
  c.vflag = flag_off(c, c.jj == 7); c.vtouch = l < 16 ? l * 128 : OOB;
  { const u32 rho = l & 31, f = rho < 26 ? rho : ((rho == 28 || rho == 29) ? rho - 2 : 0);
    c.awp = upos(28) + 5 * 1024 + 2 * (28 * (4 * (l >> 5) - 4 * (c.hd & 1)) + f); }
  P1 p;
  {
    const u32 q = (l & 15) >> 2, pp = l & 3, blk = (l >> 4) & 1, h = l >> 5, r = l & 31;
    p.aw = mkseg((8 * h + q) * 128 + ((64 * c.r) ^ ((q >> 1) << 6)) + 32 * blk + 8 * pp);
    p.aq = mkseg((8 * h + q) * 64 + 32 * blk + 8 * pp);
    p.ax = (32 * c.tg + r) * 128 + ((h ^ ((r >> 1) & 7)) << 4);
  }
  if (c.jj < 7) {
    c.sg = mk(wg + c.j * 256, 448 * 3584 - c.j * 256);
    c.su = mk(wu + c.j * 256, 448 * 3584 - c.j * 256);
    c.sd = mk(wd + c.kh * 896 * 896 + c.jj * 128, 896 * 896 - c.jj * 128);
    c.so = mk(wo + c.kh * 224 * 896 + c.jj * 128, 224 * 896 - c.jj * 128);
    if (c.r == 0) { ffn_wg<0>(c, p, epoch); __builtin_amdgcn_endpgm(); }
    ffn_wg<1>(c, p, epoch);
    __builtin_amdgcn_endpgm();
  }
  {
    u32 lo; u64 pc;
    asm volatile("s_getpc_b64 %0\n.Lpf_here:\n\ts_mov_b32 %1, .Lpf_here-fused_kernel" : "=s"(pc), "=s"(lo));
    asm volatile("s_add_u32 m0, %0, %1\n\ts_nop 0\n\tbuffer_load_dword %2, %3, 0 offen lds"
                 :: "s"(c.m0w), "i"(spos(17)), "v"((w * 64 + l) * 128), "s"(mk((const char*)(pc - lo), CODE_BYTES)) : "memory", "scc");
  }
  if (c.r == 0) { spare_wg<0>(c, p, epoch); __builtin_amdgcn_endpgm(); }
  spare_wg<1>(c, p, epoch);
}

#if defined(__HIP_DEVICE_COMPILE__)
#pragma clang attribute pop
#endif

extern "C" void kernel_launch(void* const* d_in, const int* in_sizes, int n_in, void* d_out, int out_size, void* d_ws, size_t ws_size,
                              hipStream_t stream) {
  (void)hipFuncSetAttribute((const void*)fused_kernel, hipFuncAttributeMaxDynamicSharedMemorySize, LDS_BYTES);
  fused_kernel<<<256, 512, LDS_BYTES, stream>>>((const char*)d_in[0], (const char*)d_in[1], (const char*)d_in[2], (const char*)d_in[3],
                                                (const char*)d_in[5], (const char*)d_in[6], (const char*)d_in[7], (const char*)d_in[8],
                                                (char*)d_out, (char*)d_ws);
}
```

```cpp
#define STAMPS 4
#define PROBE_K 1
#include <hip/hip_runtime.h>
#include <hip/hip_bf16.h>
#include <type_traits>
#pragma clang diagnostic ignored "-Wint-to-pointer-cast"

typedef unsigned u32;
typedef unsigned long long u64;
typedef u32 u32x4 __attribute__((ext_vector_type(4)));
typedef u32 u32x2 __attribute__((ext_vector_type(2)));
typedef short s16x4 __attribute__((ext_vector_type(4)));
typedef __bf16 bf16x8 __attribute__((ext_vector_type(8)));
typedef __bf16 bf16x2 __attribute__((ext_vector_type(2)));
typedef float f32x16 __attribute__((ext_vector_type(16)));
typedef float f32x4 __attribute__((ext_vector_type(4)));
typedef float f32x2 __attribute__((ext_vector_type(2)));
typedef __amdgpu_buffer_rsrc_t rsrc_t;
#define DI __device__ __forceinline__
#define LAS __attribute__((address_space(3)))
#define SB0() __builtin_amdgcn_sched_barrier(0)
#define PIN1(a) asm volatile("" : "+v"(a))
#define PIN2(a, b) asm volatile("" : "+v"(a), "+v"(b))

constexpr u32 WS_FLAG = 4096;
constexpr u32 WS_G    = WS_FLAG + 256 * 128;
constexpr u32 WS_KVR  = 65536;
constexpr u32 WS_KVP  = 262144;
constexpr u32 WS_O    = WS_KVP + 256 * 4096;
constexpr u32 WS_P    = WS_O + 64 * 32 * 1024;
constexpr u32 WS_H    = WS_P + 256 * 16384;
constexpr u32 WS_END  = WS_H + 64 * 112 * 1024;
constexpr u32 WS_STAMP = 32u << 20;

constexpr int UNIT = 8192, RING = 18;
constexpr u32 SCR = 0, RBASE = 2 * UNIT;
constexpr u32 TOUCH_LDS = RBASE + UNIT + 4096;
constexpr int LDS_BYTES = 163840;
constexpr u32 OOB = 0x80000000u;
constexpr u32 CODE_BYTES = 40960;
#define ABL 0
#define MARKV 0
#define TOUCH2 1
#define PSLEEP 8
#define PRIO 1
#define PROV 9
constexpr u32 SPIN_MAX = ABL ? 0 : 1u << 16;
constexpr float LOG2E = 1.4426950408889634f;

#if defined(__HIP_DEVICE_COMPILE__)
#pragma clang attribute push(__attribute__((target("no-packed-fp32-ops"))), apply_to = function)
#endif
constexpr u32 spos(int k) { return RBASE + k * UNIT; }
constexpr int w2c_(int i) { constexpr int t[18] = {0, 1, 14, 2, 3, 15, 4, 5, 16, 6, 7, 17, 8, 9, 10, 11, 12, 13}; return t[i]; }
constexpr int w2s_(int c) { constexpr int t[18] = {8, 2, 3, 4, 5, 6, 7, 9, 10, 11, 12, 13, 14, 15, -2, -1, 0, 1}; return t[c]; }
constexpr u32 upos(int n) { return spos(n < 44 ? n % RING : n < 53 ? (n + 1) % RING : w2s_(w2c_(n - 53))); }
constexpr int imin(int a, int b) { return a < b ? a : b; }

struct Ctx {
  const char* gw;
  char *out, *ws;
  int t, kh, jj, hd, j, w, tg, r;
  u32 lane, l16, m0w;
  u32 vx, vx2, vw, vq, vkv, v2, v2b;
  rsrc_t sx, sg, su, sq, sp, sd, so, sw;
  u64 tag;
  u32 rot[7];
  u32 oh, oo, op, okvp;
  u32 vflag, vtouch, awp;
};

DI rsrc_t mk(const void* p, u32 bytes) { return __builtin_amdgcn_make_buffer_rsrc((void*)p, 0, bytes, 0x00020000); }
template <u32 L> DI void set_m0(const Ctx& c) { if (!(ABL & 1)) asm volatile("s_add_u32 m0, %0, %1" :: "s"(c.m0w), "i"(L) : "memory", "scc"); }
template <u32 OFF> DI void dma(u32 voff, rsrc_t rs, u32 soff) {
  if (!(ABL & 1)) asm volatile("buffer_load_dwordx4 %0, %1, %2 offen offset:%3 lds" :: "v"(voff), "s"(rs), "s"(soff), "i"(OFF) : "memory");
}
template <int N> DI void wait_bar() { asm volatile("s_waitcnt vmcnt(%0) lgkmcnt(0)\n\ts_barrier" :: "n"(N) : "memory"); }
template <int N> DI void wait_bar_v() { asm volatile("s_waitcnt vmcnt(%0)\n\ts_barrier" :: "n"(N) : "memory"); }
DI void bar_l() { asm volatile("s_waitcnt lgkmcnt(0)\n\ts_barrier" ::: "memory"); }

constexpr int uq_(int ch) { return ch == 0 ? 28 : 30 + 3 * ((ch - 1) / 2); }
constexpr int uu_(int ch) { return ch == 0 ? 29 : 31 + 3 * ((ch - 1) / 2) + ((ch - 1) & 1); }
constexpr u32 ioff_(int n) { return n >= 39 && n < 53 ? 128 : 0; }
template <int N> DI void unit_m0(const Ctx& c) { set_m0<upos(N) - ioff_(N)>(c); }
template <int R, int N> DI void unit_go(const Ctx& c) {
  if constexpr (N < 21) {
    if constexpr (N % 3 < 2) dma<0>(N % 3 ? c.vx2 : c.vx, c.sx, c.rot[N / 3] * 128);
    else dma<0>(c.vw, c.sg, c.rot[N / 3] * (64 * 3584));
  } else if constexpr (N < 28) dma<0>(c.vkv, c.sq, c.rot[N - 21] * (64 * 2688));
  else if constexpr (N == 28) {
    if constexpr (R == 0) dma<0>(c.vq, c.sq, c.rot[0] * (64 * 2688));
    else dma<0>(c.l16, c.sp, c.w == 5 ? 0u : c.w == 6 ? 1024u : 0x40000000u);
  } else if constexpr (N < 39) {
    if constexpr (N % 3 == 0) dma<0>(c.vq, c.sq, c.rot[2 * ((N - 30) / 3) + 1 + R] * (64 * 2688));
    else dma<0>(c.vw, c.su, c.rot[N == 29 ? 0 : 2 * ((N - 30) / 3) + N % 3] * (64 * 3584));
  } else if constexpr (N < 53) dma<128>(c.vw, N < 46 ? c.sg : c.su, c.rot[(N - 39) % 7] * (64 * 3584));
  else {
    constexpr int k = w2c_(N - 53);
    if constexpr (k < 14) dma<0>(k < 7 ? c.v2 : c.v2b, c.sd, c.rot[k % 7] * (128 * 896));
    else dma<0>((k & 1) ? c.v2b : c.v2, c.so, k < 16 ? 0u : 128u * 896u);
  }
}
template <int R, int A, int B> DI void issue_range(const Ctx& c) {
  if constexpr (A < B) { unit_m0<A>(c); asm volatile("s_nop 0"); unit_go<R, A>(c); issue_range<R, A + 1, B>(c); }
}

struct Seg { u32 b[3]; };
DI Seg mkseg(u32 a) { Seg s; s.b[0] = a; s.b[1] = a + 65536; s.b[2] = a + 131072; asm volatile("" : "+v"(s.b[0]), "+v"(s.b[1]), "+v"(s.b[2])); return s; }
DI u32 sa(const Seg& s, u32 off) { return s.b[off >> 16] + (off & 0xffffu); }
DI f32x16 mfma(bf16x8 a, bf16x8 b, f32x16 c) { return __builtin_amdgcn_mfma_f32_32x32x16_bf16(a, b, c, 0, 0, 0); }
DI bf16x8 as_frag(u32x4 v) { return __builtin_bit_cast(bf16x8, v); }
DI bf16x8 tr_frag(u32 a0, u32 a1) {
  s16x4 lo = __builtin_amdgcn_ds_read_tr16_b64_v4i16((LAS s16x4*)a0);
  s16x4 hi = __builtin_amdgcn_ds_read_tr16_b64_v4i16((LAS s16x4*)a1);
  return __builtin_bit_cast(bf16x8, __builtin_shufflevector(lo, hi, 0, 1, 2, 3, 4, 5, 6, 7));
}
DI u32x4 lds_rd128(u32 a) { return *(LAS u32x4*)a; }
DI void lds_wr128(u32 a, u32x4 v) { *(LAS u32x4*)a = v; }
DI u32 f2u(float f) { return __builtin_bit_cast(u32, f); }
DI float u2f(u32 u) { return __builtin_bit_cast(float, u); }
DI u32 pk(float a, float b) { f32x2 t = {a, b}; return __builtin_bit_cast(u32, __builtin_convertvector(t, bf16x2)); }
template <int S, class V> DI u32x4 pack8(const V& v) {
  u32x4 r = {pk(v[8 * S], v[8 * S + 1]), pk(v[8 * S + 2], v[8 * S + 3]), pk(v[8 * S + 4], v[8 * S + 5]), pk(v[8 * S + 6], v[8 * S + 7])};
  return r;
}
DI float blo(u32 d) { return u2f(d << 16); }
DI float bhi(u32 d) { return u2f(d & 0xffff0000u); }
DI u32 umax(u32 a, u32 b) { return a > b ? a : b; }
DI bool any_(bool p) { return __builtin_amdgcn_ballot_w64(p) != 0; }
DI u32x4 sanit(u32x4 v) { return __builtin_elementwise_min(v, (u32x4){0xfffffffeu, 0xfffffffeu, 0xfffffffeu, 0xfffffffeu}); }
DI void st_dev(const Ctx& c, u32x4 v, u32 voff, u32 soff) {
  if (ABL & 8) { asm volatile("" :: "v"(v)); return; }
  __builtin_amdgcn_raw_buffer_store_b128(v, c.sw, voff, soff, 16);
  asm volatile("s_nop 1" :: "v"(v) : "memory");
}
DI u32x4 ld_dev(const Ctx& c, u32 voff, u32 soff) {
  if (ABL & 16) { u32x4 z = {voff, soff, voff, soff}; asm volatile("" : "+v"(z)); return z; }
  return __builtin_amdgcn_raw_buffer_load_b128(c.sw, voff, soff, 16);
}
DI f32x2 xhalf(float v) { auto p = __builtin_amdgcn_permlane32_swap(f2u(v), f2u(v), false, false); f32x2 r = {u2f(p[0]), u2f(p[1])}; return r; }

DI void stamp(const Ctx& c, int i) {
  SB0();
  u64 tm, tc; asm volatile("s_memrealtime %0\n\ts_memtime %1\n\ts_waitcnt lgkmcnt(0)" : "=s"(tm), "=s"(tc) :: "memory");
  tm = (tm & 0xffffffffull) | (tc << 32);
  { u32x2 v = {(u32)tm, (u32)(tm >> 32)}; __builtin_amdgcn_raw_buffer_store_b64(v, mk(c.ws + WS_STAMP, 1 << 20), c.lane == 0 ? 0u : OOB, ((__builtin_amdgcn_workgroup_id_x() * 8 + c.w) * 32 + i) * 8, 0); }
  SB0();
}
#define STAMP(i) do { if ((i) == 0 || (i) == PROBE_K) stamp(c, i); } while (0)

struct P1 {
  Seg aw, aq; u32 ax;
  bf16x8 xf[28], f[8], af[8], vf[8];
  f32x16 g1, u1, a0, a1, g2, u2, fp, k0;
  u32x4 wp[2], ph[4], h1[14], kv[4];
  float y[16], e[14], sum, mxl, mnl;
  u32x2 poll;
  float gq, gt;
  u32 hbase; int TG;
};

DI void load_wphi(const Ctx& c, u32x4 (&wp)[2]) {
  const u32 a = c.awp;
#pragma unroll
  for (int s = 0; s < 2; ++s)
#pragma unroll
    for (int d = 0; d < 4; ++d) {
      const int e0 = 2 * d, k0 = 16 * s + 8 * (e0 >> 2) + (e0 & 3);
      u32 lo = *(LAS unsigned short*)(a + 56 * k0), hi = *(LAS unsigned short*)(a + 56 * (k0 + 1));
      wp[s][d] = lo | (hi << 16);
    }
}
template <int K> DI void fm_slice(P1& p) {
  if constexpr (K == 0) { p.fp = mfma(as_frag(p.wp[0]), as_frag(pack8<0>(p.a0)), (f32x16){}); }
  else if constexpr (K == 1) { p.fp = mfma(as_frag(p.wp[1]), as_frag(pack8<1>(p.a0)), p.fp); }
  else if constexpr (K == 2) { }
  else if constexpr (K == 3) {
    PIN1(p.fp);
    float mx = p.fp[0];
#pragma unroll
    for (int i = 1; i < 14; ++i) mx = __builtin_fmaxf(mx, p.fp[i]);
    p.mxl = mx; PIN1(p.mxl);
  } else if constexpr (K == 4) {
    float mn = p.fp[0];
#pragma unroll
    for (int i = 1; i < 14; ++i) mn = __builtin_fminf(mn, p.fp[i]);
    p.mnl = mn; PIN1(p.mnl);
  } else if constexpr (K == 5) {
    f32x2 t = xhalf(p.mxl); p.mxl = __builtin_fmaxf(t[0], t[1]) * LOG2E;
    t = xhalf(p.mnl); p.mnl = __builtin_fminf(t[0], t[1]) * LOG2E;
    PIN2(p.mxl, p.mnl);
  } else {
    constexpr int NEG = K >= 15, k = K - 6 - 9 * NEG;
    if constexpr (k < 5) {
#pragma unroll
      for (int i = 3 * k; i < imin(14, 3 * k + 3); ++i) {
        p.e[i] = __builtin_amdgcn_exp2f(NEG ? __builtin_fmaf(p.fp[i], -LOG2E, p.mnl) : __builtin_fmaf(p.fp[i], LOG2E, -p.mxl));
        PIN1(p.e[i]);
      }
      if constexpr (k == 1) p.sum = p.e[0] + p.e[1] + p.e[2];
      if constexpr (k > 1) p.sum += p.e[3 * k - 3] + p.e[3 * k - 2] + p.e[3 * k - 1];
      if constexpr (k > 0) PIN1(p.sum);
    } else if constexpr (k == 5) {
      f32x2 t = xhalf(p.sum + p.e[12] + p.e[13]); p.sum = __builtin_amdgcn_rcpf(t[0] + t[1]); PIN1(p.sum);
    } else if constexpr (k < 8) {
#pragma unroll
      for (int i = 7 * (k - 6); i < 7 * (k - 5); ++i) { p.e[i] *= p.sum; PIN1(p.e[i]); }
    } else {
      u32x4 o1 = {pk(p.e[8], p.e[9]), pk(p.e[10], p.e[11]), pk(p.e[12], p.e[13]), 0u};
      p.ph[2 * NEG] = pack8<0>(p.e); p.ph[2 * NEG + 1] = o1; PIN2(p.ph[2 * NEG], p.ph[2 * NEG + 1]);
    }
  }
}
template <int A, int B> DI void fm_slices(P1& p) { if constexpr (A < B) { fm_slice<A>(p); fm_slices<A + 1, B>(p); } }
template <u32 VB, int S> DI void v_handoff(const Ctx& c, const f32x16& av) {
  u32x4 vf = pack8<S>(av);
  const bool one = (c.lane & 31) == ((c.hd & 1) ? 0u : 28u);
#pragma unroll
  for (int d = 0; d < 4; ++d) vf[d] = one ? 0x3f803f80u : vf[d];
  lds_wr128(VB + c.tg * 2048 + c.l16 + S * 1024, vf);
}
template <int T> DI void phi_image(const Ctx& c, P1& p) {
  const u32 tok = c.lane & 31, h = c.lane >> 5;
  const u32 ft = (((tok >> 1) & 1) << 3) | (tok & 1) | (((tok >> 2) & 3) << 1);
  const u32 wa = SCR + c.tg * 4096 + tok * 128 + ((h ^ ft) << 3);
#pragma unroll
  for (int g = 0; g < 4; ++g) {
    u32x2 v = {p.ph[2 * T + (g >> 1)][2 * (g & 1)], p.ph[2 * T + (g >> 1)][2 * (g & 1) + 1]};
    *(LAS u32x2*)(wa ^ (u32)((8 * T + 2 * g) << 3)) = v;
  }
}
template <u32 VB, int K> DI void kv_stage(const Ctx& c, P1& p) {
  const u32 l = c.lane, h = l >> 5;
  if constexpr (K >= 1 && K <= 4) {
    p.k0 = mfma(p.af[2 * K - 2], p.vf[2 * K - 2], K == 1 ? (f32x16){} : p.k0);
    p.k0 = mfma(p.af[2 * K - 1], p.vf[2 * K - 1], p.k0);
  }
  if constexpr (K < 4) {
    const u32 q = (l & 15) >> 2, pp = l & 3, blk = (l >> 4) & 1;
    const u32 ra = SCR + (4 * h + q) * 128 + (((4 * blk + pp) ^ (q & 1) ^ (h << 1) ^ ((q >> 1) << 3) ^ ((c.tg & 1) << 3)) << 3);
#pragma unroll
    for (int s = 0; s < 2; ++s) {
      p.af[2 * K + s] = tr_frag(ra + K * 4096 + s * 2048, (ra ^ 32u) + K * 4096 + s * 2048 + 1024);
      p.vf[2 * K + s] = as_frag(lds_rd128(VB + c.l16 + K * 2048 + s * 1024));
    }
  }
  if constexpr (K == 6) {
    const u32 vo = c.tg < 2 ? c.l16 : OOB;
    st_dev(c, sanit(pack8<0>(p.k0)), vo, c.okvp + c.tg * 1024);
    st_dev(c, sanit(pack8<1>(p.k0)), vo + 1024, c.okvp + c.tg * 1024);
  }
}
DI void add8(float (&s)[8], u32x4 v) {
#pragma unroll
  for (int d = 0; d < 4; ++d) { s[2 * d] += blo(v[d]); s[2 * d + 1] += bhi(v[d]); }
}
DI void set8(float (&s)[8], u32x4 v) {
#pragma unroll
  for (int d = 0; d < 4; ++d) { s[2 * d] = blo(v[d]); s[2 * d + 1] = bhi(v[d]); }
}
template <int A, int B, int N> DI bool has_mark(const u32x4 (&v)[N]) {
  u32 m = 0;
#pragma unroll
  for (int i = A; i < B; ++i) m = umax(umax(m, v[i][0]), umax(v[i][1], umax(v[i][2], v[i][3])));
  return any_(m == 0xffffffffu);
}
template <int A, int B, int N, class F> DI void chk(u32x4 (&v)[N], F&& reload) {
  if (__builtin_expect(SPIN_MAX && has_mark<A, B>(v), 0)) {
    u32 spin = 0;
    do { asm volatile("" ::: "memory"); reload(); } while (has_mark<A, B>(v) && ++spin < SPIN_MAX);
  }
}
template <int A, int B, int N> DI void ld_lin(const Ctx& c, u32x4 (&v)[N], u32 soff, u32 stride) {
#pragma unroll
  for (int i = A; i < B; ++i) v[i] = ld_dev(c, c.l16, soff + i * stride);
}
template <int A, int B, int N> DI void ld_1k(const Ctx& c, u32x4 (&v)[N], u32 soff) {
#pragma unroll
  for (int i = A; i < B; ++i) v[i] = ld_dev(c, c.l16 + (i & 3) * 1024, soff + (i >> 2) * 4096);
}
template <int A, int B> DI void ld_h(const Ctx& c, u32x4 (&v)[14], u32 voff, u32 soff) {
#pragma unroll
  for (int i = A; i < B; ++i) v[i] = ld_dev(c, voff + (i & 1) * 1024, soff + c.rot[i >> 1] * 8192);
}
DI void o_publish(const Ctx& c, const u32x4 (&kv)[4], const u32x4 (&phq)[4], float g) {
  f32x16 n = {};
#pragma unroll
  for (int i = 0; i < 4; ++i) n = mfma(as_frag(kv[i]), as_frag(phq[i]), n);
  const bool odd = c.hd & 1;
  f32x2 t = xhalf(odd ? n[0] : n[12]);
  float sc = g * __builtin_amdgcn_rcpf((odd ? t[0] : t[1]) + 1e-6f);
#pragma unroll
  for (int i = 0; i < 16; ++i) n[i] *= sc;
  u32x4 f0 = sanit(pack8<0>(n)), f1 = sanit(pack8<1>(n));
  const bool z0 = odd && c.lane < 32, z1 = !odd && c.lane >= 32;
  f0[0] = z0 ? 0 : f0[0]; f0[1] = z0 ? 0 : f0[1];
  f1[2] = z1 ? 0 : f1[2]; f1[3] = z1 ? 0 : f1[3];
  st_dev(c, f0, c.l16, c.oo);
  st_dev(c, f1, c.l16 + 1024, c.oo);
}
DI u32 flag_off(const Ctx& c, bool spare) {
  const u32 i = c.lane & 15;
  u32 red = (c.hd * 16 + 7 + 8 * (i & 1)) << 7, late = ((c.t * 16 + 7 + 2 * (i & 4)) << 7) + 32 + (i & 3) * 8;
  u32 lo = (spare ? i * 16 + c.t : c.t * 16 + i) << 7;
  asm volatile("" : "+v"(red), "+v"(late), "+v"(lo));
  return WS_FLAG + (!(c.lane & 16) ? lo : (spare || (i & 8)) ? red : late);
}
DI bool flag_bad(const Ctx& c, u32x2 f) { return any_(f[0] != (u32)c.tag || f[1] != (u32)(c.tag >> 32)); }
DI void flag_spin(const Ctx& c, u32x2 f) {
  if (__builtin_expect(SPIN_MAX && flag_bad(c, f), 0)) {
    u32 spin = 0;
    do {
      __builtin_amdgcn_s_sleep(2);
      asm volatile("" ::: "memory");
      f = __builtin_amdgcn_raw_buffer_load_b64(c.sw, c.vflag, 0, 16);
    } while (flag_bad(c, f) && ++spin < SPIN_MAX);
  }
  asm volatile("" ::: "memory");
}
DI void flag_publish(const Ctx& c) {
  u32x2 v = {(u32)c.tag, (u32)(c.tag >> 32)};
  __builtin_amdgcn_raw_buffer_store_b64(v, c.sw, c.lane + c.w == 0 ? 0u : OOB, WS_FLAG + (u32)(c.t * 16 + c.hd) * 128, 16);
}
DI float ld_g(const Ctx& c, int tgroup) {
  return u2f(__builtin_amdgcn_raw_buffer_load_b32(c.sw, (c.lane & 31) * 4, WS_G + (u32)(c.kh * 2048 + c.t * 128 + tgroup * 32) * 4, 16));
}
template <int TG> DI void mark_tg(const Ctx& c, u32 voff, u32 soff) {
  if (ABL & 8) return;
  const u32x4 ones = {~0u, ~0u, ~0u, ~0u};
  asm volatile("s_cmp_lg_u32 %0, %1\n\ts_cbranch_scc1 1f\n\tbuffer_store_dwordx4 %2, %3, %4, %5 offen sc1\n\ts_nop 1\n1:"
               :: "s"(c.tg), "i"(TG), "v"(ones), "v"(voff), "s"(c.sw), "s"(soff) : "memory", "scc");
}
DI void touch(const Ctx& c, u32 soff) {
  if (ABL & 8) return;
  asm volatile("s_mov_b32 m0, %0\n\ts_nop 0\n\tbuffer_load_dword %1, %2, %3 offen sc1 lds"
               :: "i"(TOUCH_LDS), "v"(c.vtouch), "s"(c.sw), "s"(soff) : "memory");
}

constexpr int PRO = PROV;
constexpr int need_(int s) { return s < 0 ? 0 : s < 7 ? 3 * (s + 1) : s < 10 ? 23 + 2 * (s - 7) : s == 10 ? 28 : s < 15 ? 30 + 3 * (s - 11) : 41 + 2 * (s - 15); }
constexpr int iss_(int s) {
  constexpr int t[6] = {56, 59, 62, 65, 67, 71};
  return s < 0 ? PRO : s >= 16 ? t[s - 16] : imin(18 + need_(s - 2) - (s >= 11 ? 1 : 0), PRO + 3 * (s + 1));
}
static_assert(iss_(10) <= 44 && iss_(15) == 53, "ring schedule");
constexpr int S_FLAG = 13, S_POLL = 18;
constexpr int nmark_(int r, int s) { return s == 6 ? 1 - r : (s >= 7 && s <= 9) ? 1 : s == 10 ? 1 - r : s == 11 ? r : 0; }
constexpr int oth_(int r, int s) {
  return nmark_(r, s) + (s == S_FLAG ? 1 - r : s == 14 ? 2 - 2 * r : 0) + (s == 15 ? 2 : s == S_POLL ? 2 : s == 19 ? 5 + r : s == 20 ? 5 : s == 21 ? 5 + 4 * r : 0);
}
constexpr int tot_(int r, int s) { return iss_(s) - iss_(s - 1) + oth_(r, s); }
constexpr int vm_(int r, int s) {
  int sp = -1; while (iss_(sp) < need_(s)) ++sp;
  int n = iss_(sp) - need_(s);
  for (int k = sp + 1; k < s; ++k) n += tot_(r, k);
  if (s == S_FLAG) n = imin(n, tot_(r, s - 1));
  if (s == 21) n = imin(n, tot_(r, 20) + 2);
  if (s == S_POLL + 1) n = imin(n, tot_(r, S_POLL) - 1);
  return n;
}
constexpr int F2_(int r) { return r ? 112 : 84; }
constexpr int item0_(int r, int s) { return s < 7 ? 4 * s : s < 11 ? 28 + 8 * (s - 7) : s == 11 ? 56 : s < 15 ? 56 + (1 + r) * (8 * (s - 12) + 4) : F2_(r) + 8 * (s - 15); }

template <int R, int J> DI void rd_item(P1& p) {
  constexpr bool att = (J >= 28 && J < 56) || (R == 1 && J >= 56 && J < 112 && (J & 1));
  if constexpr (att) {
    constexpr int k = J < 56 ? J - 28 : (J - 56) / 2, ch = k / 4;
    constexpr u32 o = (J < 56 ? upos(21 + ch) + R * 4096 : upos(uq_(ch)) + (ch > 0 && !(ch & 1) ? 4096 : 0)) + (k % 4) * 1024;
    p.f[J % 8] = tr_frag(sa(p.aq, o), sa(p.aq, o + 256));
  } else {
    constexpr int k = J < 28 ? J : J < F2_(R) ? (J - 56) / (1 + R) : J - F2_(R);
    constexpr u32 o = upos(J < 28 ? 3 * (k / 4) + 2 : J < F2_(R) ? uu_(k / 4) : 39 + k / 4) + (k % 4) * 2048;
    p.f[J % 8] = tr_frag(sa(p.aw, o), sa(p.aw, o + 512));
  }
}
template <int R, int J> DI void mm_item(P1& p) {
  if constexpr (J < 0) {}
  else if constexpr (J < 28) p.g1 = mfma(p.f[J % 8], p.xf[J], p.g1);
  else if constexpr (J < 56) { if constexpr (R == 0) p.a0 = mfma(p.f[J % 8], p.xf[J - 28], p.a0); else p.a1 = mfma(p.xf[J - 28], p.f[J % 8], p.a1); }
  else if constexpr (J < F2_(R)) {
    constexpr int k = (J - 56) / (1 + R);
    if constexpr (R == 1 && (J & 1)) p.a0 = mfma(p.f[J % 8], p.xf[k], p.a0); else p.u1 = mfma(p.f[J % 8], p.xf[k], p.u1);
  } else { constexpr int k = J - F2_(R); if constexpr (k < 28) p.g2 = mfma(p.f[J % 8], p.xf[k], p.g2); else p.u2 = mfma(p.f[J % 8], p.xf[k - 28], p.u2); }
}
template <int R, int A, int B> DI void mm_range(P1& p) { if constexpr (A < B) { mm_item<R, A>(p); mm_range<R, A + 1, B>(p); } }

constexpr float GK1 = -2.0f * 0.7978845608028654f * LOG2E, GK2 = GK1 * 0.044715f;
template <int OP> DI void gelu_op(float (&y)[16], const f32x16& g) {
  constexpr int st = OP % 4, i = OP / 4 - st;
  if constexpr (i >= 0 && i < 16) {
    if constexpr (st == 0) y[i] = g[i] * __builtin_fmaf(g[i] * g[i], GK2, GK1);
    else if constexpr (st == 1) y[i] = __builtin_amdgcn_exp2f(y[i]);
    else if constexpr (st == 2) y[i] = __builtin_amdgcn_rcpf(1.0f + y[i]);
    else y[i] = g[i] * y[i];
    PIN1(y[i]);
  }
}
constexpr int gcost(int op) { return op / 4 - op % 4 < 0 || op / 4 - op % 4 > 15 ? 0 : op % 4 == 0 ? 3 : op % 4 == 1 ? 2 : op % 4 == 2 ? 3 : 1; }
constexpr int gcum(int k, int n) { if (k >= n) return 76; int op = 0, cst = 0; while (cst * n < k * 144) cst += gcost(op++); return op; }
template <int A, int B> DI void gelu_ops(float (&y)[16], const f32x16& g) { if constexpr (A < B) { gelu_op<A>(y, g); gelu_ops<A + 1, B>(y, g); } }
template <int S> DI void h_store(const Ctx& c, const float (&y)[16], const f32x16& u, int blk) {
  float h[8];
#pragma unroll
  for (int i = 0; i < 8; ++i) h[i] = y[8 * S + i] * u[8 * S + i];
  st_dev(c, sanit(pack8<0>(h)), c.l16 + S * 1024, c.oh + blk * 4096);
}
template <int R, int S, int K, int J> DI void slot_work(Ctx& c, P1& p) {
  if constexpr (S < 7) p.xf[J] = as_frag(lds_rd128((p.ax + upos(3 * S)) ^ (u32)(32 * K)));
  if constexpr (nmark_(R, S) > 0) {
    if constexpr (S == 6) mark_tg<K>(c, c.l16, c.okvp);
    else if constexpr (S == 10) mark_tg<K>(c, c.l16 + 1024, c.oh + 4096);
    else if constexpr ((K & 1) == R) mark_tg<K / 2>(c, S == 8 || S == 11 ? c.l16 + 1024 : c.l16, S >= 9 ? c.oh + 4096 : c.oh);
  }
  if constexpr (R == 0) {
    if constexpr (J >= 33 && J < 60) gelu_ops<gcum(J - 33, 27), gcum(J - 32, 27)>(p.y, p.g1);
    if constexpr (J == 56) load_wphi(c, p.wp);
    if constexpr (J >= 60 && J < 76) fm_slices<(3 * (J - 60) + 1) / 2, J == 75 ? 24 : (3 * (J - 59) + 1) / 2>(p);
    if constexpr (J == 70) phi_image<0>(c, p);
    if constexpr (J == 75) phi_image<1>(c, p);
    if constexpr (J >= 76 && J < 83) kv_stage<spos(8), J - 76>(c, p);
  } else {
    constexpr int G = J < 56 ? J - 33 : J - 96 + 23;
    if constexpr ((J >= 33 && J < 56) || (J >= 96 && J < 112)) gelu_ops<gcum(G, 39), gcum(G + 1, 39)>(p.y, p.g1);
    if constexpr (J == 61 || J == 62) v_handoff<spos(8), J - 61>(c, p.a1);
    if constexpr (J == 57) load_wphi(c, p.wp);
    if constexpr (J == 60) PIN2(p.wp[0], p.wp[1]);
  }
  if constexpr (S >= 15) {
    constexpr int g = J - F2_(R);
    if constexpr (g == 5 || g == 6) h_store<g - 5>(c, p.y, p.u1, 0);
    if constexpr (R == 1 && g >= 7 && g < 31) fm_slice<g - 7>(p);
    if constexpr (g >= 33) gelu_ops<gcum(g - 33, 23), gcum(g - 32, 23)>(p.y, p.g2);
    if constexpr (g == 26) touch(c, c.oh);
    if constexpr (R == 1 && S == 21 && !(K & 1)) ld_1k<K / 2, K / 2 + 1>(c, p.kv, WS_KVR + (u32)c.hd * 4096);
    if constexpr (S == 19 && K == 0) { p.gt = ld_g(c, p.TG); if constexpr (R == 1) p.gq = ld_g(c, c.tg); }
    if constexpr (S == 19 && (K & 1)) ld_h<K / 2, K / 2 + 1>(c, p.h1, c.l16, p.hbase);
    if constexpr (S >= 20 && ((K & 1) || K == 4)) { constexpr int i = 4 + 5 * (S - 20) + (K < 4 ? K / 2 : (K + 1) / 2); ld_h<i, i + 1>(c, p.h1, c.l16, p.hbase); }
  }
}
template <int R, int S, int J0, int K, int N> DI void slots(Ctx& c, P1& p) {
  if constexpr (K < N) {
    constexpr int nu = iss_(S) - iss_(S - 1), u0 = iss_(S - 1);
    constexpr int ua = K == 0 ? 0 : ((2 * K + R) * nu) / (2 * N), ub = K == N - 1 ? nu : ((2 * K + 2 + R) * nu) / (2 * N);
    if constexpr (R == 1 && PRIO) {
      if constexpr (K == 0 && S >= 15 && S < 19) __builtin_amdgcn_s_setprio(1);
      if constexpr (S >= 19 && (K == 0 || K == 4)) __builtin_amdgcn_s_setprio(K == 0);
    }
    mm_item<R, J0 + K - 4>(p);
    if constexpr (ub > ua) unit_m0<u0 + ua>(c);
    rd_item<R, (J0 + K) ^ 1>(p);
    if constexpr (ub > ua) { unit_go<R, u0 + ua>(c); issue_range<R, u0 + ua + 1, u0 + ub>(c); }
    slot_work<R, S, K, J0 + K>(c, p);
    SB0();
    slots<R, S, J0, K + 1, N>(c, p);
  }
}
template <int R, int S> DI void step(Ctx& c, P1& p) {
  constexpr int j0 = item0_(R, S), n = item0_(R, S + 1) - j0;
  if constexpr (S == S_POLL + 1) {
    asm volatile("s_waitcnt vmcnt(%1)\n\ts_barrier" : "+v"(p.poll) : "n"(vm_(R, S)) : "memory");
    PIN1(p.g2);
    flag_spin(c, p.poll);
  } else if constexpr ((S == 14 && R == 0) || S == 21) wait_bar<vm_(R, S)>();
  else wait_bar_v<vm_(R, S)>();
  if constexpr (S < 2 || S >= 6) STAMP(S < 2 ? S + 1 : S - 3);
  if constexpr (S == S_FLAG && R == 0) flag_publish(c);
  if constexpr (S == S_POLL) asm volatile("buffer_load_dwordx2 %0, %1, %2, 0 offen sc1" : "=v"(p.poll) : "v"(c.vflag), "s"(c.sw) : "memory");
  SB0();
  slots<R, S, j0, 0, n>(c, p);
}
template <int R, int S, int E> DI void steps(Ctx& c, P1& p) { if constexpr (S < E) { step<R, S>(c, p); steps<R, S + 1, E>(c, p); } }

struct P2 { Seg s[2]; u32 ob[2]; bf16x8 f[8]; f32x16 c[2]; u32x4 h2[14], of[8]; };
template <int J> DI void rd2(P2& q) {
  if constexpr (J < 56) {
    constexpr u32 o = spos(w2s_(J / 4)) + ((J / 2) & 1) * 2048;
    q.f[J % 8] = tr_frag(sa(q.s[J & 1], o), sa(q.s[J & 1], o + 1024));
  } else if constexpr (J < 72) {
    constexpr int i = (J - 56) / 2, hh = i >> 1;
    constexpr u32 o = (28 * hh - 4 * (hh & 1) + 16 * (i & 1)) * 128;
    q.f[J % 8] = tr_frag(q.ob[J & 1] + o, q.ob[J & 1] + o + 1024);
  }
}
template <int J> DI void mm2(P2& q, const P1& p) {
  q.c[J & 1] = mfma(q.f[J % 8], as_frag(J < 28 ? p.h1[J / 2] : J < 56 ? q.h2[(J - 28) / 2] : q.of[(J - 56) / 2]), q.c[J & 1]);
}
template <int A, int B, class F> DI void run2(P2& q, const P1& p, F&& extra) {
  if constexpr (A < B) {
    mm2<A>(q, p); rd2<(A + 4) ^ 1>(q);
    extra(std::integral_constant<int, A>{});
    SB0();
    run2<A + 1, B>(q, p, extra);
  } else { PIN2(q.c[0], q.c[1]); }
}

template <int R> DI void ffn_wg(Ctx& c, P1& p, u64 epoch) {
  const u32 l = c.lane;
  const int tg2 = c.tg & 1, ks = c.tg >> 1;
  issue_range<R, 0, PRO>(c);
  p.TG = 2 * (R == 1 ? c.kh : 1 - c.kh) + tg2;
  p.hbase = WS_H + (u32)(((c.t * 4 + p.TG) * 112 + 56 * c.kh + 2 * ks) * 1024);
  c.tag = epoch + 1;
  asm volatile("" : "+s"(c.tag), "+s"(p.hbase), "+v"(c.vflag), "+v"(c.vtouch), "+v"(c.awp) :: "memory");
  p.g1 = p.u1 = p.a0 = p.a1 = p.g2 = p.u2 = (f32x16){};
  steps<R, 0, 22>(c, p);
  mm_range<R, F2_(R) + 52, F2_(R) + 56>(p);
  SB0();
  STAMP(19);
  h_store<0>(c, p.y, p.u2, 1);
  h_store<1>(c, p.y, p.u2, 1);
  if constexpr (R == 1) {
    chk<0, 4>(p.kv, [&] { ld_1k<0, 4>(c, p.kv, WS_KVR + (u32)c.hd * 4096); });
    STAMP(22);
    o_publish(c, p.kv, p.ph, p.gq);
  }
  P2 q;
  const u32 qq = (l & 15) >> 2, pp_ = l & 3, blk = (l >> 4) & 1, h = l >> 5;
  const u32 b0 = (4 * h + qq) * 128 + ((qq >> 1) << 6) + 32 * blk + 8 * pp_;
  q.s[0] = mkseg(b0 + ks * 4096); q.s[1] = mkseg((b0 ^ 64) + ks * 4096);
  q.ob[0] = b0 + ks * (112 * 128); q.ob[1] = (b0 ^ 64) + ks * (112 * 128);
  PIN2(q.ob[0], q.ob[1]);
  q.c[0] = q.c[1] = (f32x16){};
  rd2<1>(q); rd2<0>(q); rd2<3>(q); rd2<2>(q);
  auto none = [](auto) {};
  const u32 obase = WS_O + (u32)(((c.t * 4 + p.TG) * 16 + 8 * c.kh + 4 * ks) * 2048);
  const u32 l16h = c.l16 + 4096;
  chk<0, 14>(p.h1, [&] { ld_h<0, 14>(c, p.h1, c.l16, p.hbase); });
  STAMP(20);
  constexpr int JC = R ? 8 : 24;
  run2<0, JC>(q, p, none);
  wait_bar_v<2 + 2 * R>();
  STAMP(21);
  if (TOUCH2) touch(c, c.oh + 4096);
  run2<JC, 28>(q, p, [&](auto J) { if constexpr (TOUCH2 && R == 1 && J.value == 16) touch(c, c.oo); });
  STAMP(23);
  if constexpr (R == 0) __builtin_amdgcn_s_sleep(PSLEEP);
  ld_h<0, 14>(c, q.h2, l16h, p.hbase);
  STAMP(24);
  chk<0, 7>(q.h2, [&] { ld_h<0, 7>(c, q.h2, l16h, p.hbase); });
  STAMP(25);
  run2<28, 42>(q, p, [&](auto J) { if constexpr (J.value >= 34) ld_1k<J.value - 34, J.value - 33>(c, q.of, obase); });
  chk<7, 14>(q.h2, [&] { ld_h<7, 14>(c, q.h2, l16h, p.hbase); });
  STAMP(26);
  run2<42, 56>(q, p, none);
  const float om = 1.0f - p.gt;
#pragma unroll
  for (int i = 0; i < 16; ++i) { q.c[0][i] *= om; q.c[1][i] *= om; }
  PIN2(q.c[0], q.c[1]);
  STAMP(27);
  chk<0, 8>(q.of, [&] { ld_1k<0, 8>(c, q.of, obase); });
  STAMP(28);
  u32x4 pp[4];
  const u32 so = c.op ^ (R ? 8 * 16384 : 0);
  run2<56, 72>(q, p, [&](auto J) { if constexpr (R == 1 && J.value >= 68) ld_1k<J.value - 68, J.value - 67>(c, pp, so); });
  f32x16 &c0 = q.c[0], &c1 = q.c[1];
  STAMP(29);
  if constexpr (R == 0) {
    st_dev(c, sanit(pack8<0>(c0)), c.l16, so);
    st_dev(c, sanit(pack8<1>(c0)), c.l16 + 1024, so);
    st_dev(c, sanit(pack8<0>(c1)), c.l16 + 2048, so);
    st_dev(c, sanit(pack8<1>(c1)), c.l16 + 3072, so);
    STAMP(30);
  } else {
    chk<0, 4>(pp, [&] { ld_1k<0, 4>(c, pp, so); });
    STAMP(30);
#pragma unroll
    for (int d = 0; d < 4; ++d) {
      c0[2 * d] += blo(pp[0][d]); c0[2 * d + 1] += bhi(pp[0][d]); c0[8 + 2 * d] += blo(pp[1][d]); c0[9 + 2 * d] += bhi(pp[1][d]);
      c1[2 * d] += blo(pp[2][d]); c1[2 * d + 1] += bhi(pp[2][d]); c1[8 + 2 * d] += blo(pp[3][d]); c1[9 + 2 * d] += bhi(pp[3][d]);
    }
    bar_l();
    const u32 wa = (u32)(ks * 64 + tg2 * 32 + (l & 31)) * 272 + h * 16;
#pragma unroll
    for (int gq = 0; gq < 4; ++gq) {
      f32x4 v0 = {c0[4 * gq], c0[4 * gq + 1], c0[4 * gq + 2], c0[4 * gq + 3]}, v1 = {c1[4 * gq], c1[4 * gq + 1], c1[4 * gq + 2], c1[4 * gq + 3]};
      *(LAS f32x4*)(wa + gq * 32) = v0;
      *(LAS f32x4*)(wa + 128 + gq * 32) = v1;
    }
    bar_l();
#pragma unroll
    for (int it = 0; it < 2; ++it) {
      const u32 tok = c.tg * 16 + it * 8 + (l >> 3), ra = tok * 272 + (l & 7) * 32;
      f32x4 x0 = *(LAS f32x4*)(ra), x1 = *(LAS f32x4*)(ra + 16), y0 = *(LAS f32x4*)(ra + 64 * 272), y1 = *(LAS f32x4*)(ra + 64 * 272 + 16);
      x0 += y0; x1 += y1;
      u32x4 o = {pk(x0[0], x0[1]), pk(x0[2], x0[3]), pk(x1[0], x1[1]), pk(x1[2], x1[3])};
      *(u32x4*)(c.out + (size_t)(c.t * 128 + c.kh * 64 + tok) * 896 + c.jj * 128 + (l & 7) * 16) = o;
    }
    if (__builtin_amdgcn_workgroup_id_x() == 0 && c.w == 4) {
      u32x2 v = {(u32)c.tag, (u32)(c.tag >> 32)};
      __builtin_amdgcn_raw_buffer_store_b64(v, c.sw, l == 0 ? 0u : OOB, 0, 16);
    }
    STAMP(31);
  }
}

template <int R, int A0, int A, int B, int ST> DI void pipe(P1& p) {
  if constexpr (A - 3 * ST < B) {
    if constexpr (A - 3 * ST >= A0) mm_item<R, A - 3 * ST>(p);
    if constexpr (A < B) rd_item<R, A>(p);
    SB0();
    pipe<R, A0, A + ST, B, ST>(p);
  }
}
constexpr u32 sxpos(int ch) { return spos(ch < 2 ? 2 * ch - 2 : 3 + 2 * ch); }
constexpr u32 NB1 = 8, NB2 = 16;
template <int N, int E> DI void spare_x(const Ctx& c) {
  if constexpr (N < E) { set_m0<sxpos(N >> 1) + (N & 1) * UNIT>(c); asm volatile("s_nop 0"); dma<0>((N & 1) ? c.vx2 : c.vx, c.sx, c.rot[N >> 1] * 128); spare_x<N + 1, E>(c); }
}

DI void bulk_mark(const Ctx& c, u32 i) {
  const u32x4 ones = {~0u, ~0u, ~0u, ~0u};
  u32 o;
  if (i < 112) o = WS_P + (c.t * 16 + 8 * c.kh) * 16384 + i * 1024;
  else o = WS_O + ((c.t * 4 + ((i - 112) >> 4)) * 16 + 8 * c.kh) * 2048 + ((i - 112) & 15) * 1024;
  st_dev(c, ones, c.l16, o);
}
template <int R> DI void spare_wg(Ctx& c, P1& p, u64 epoch) {
  const u32 l = c.lane;
  spare_x<0, 4>(c);
  set_m0<spos(2)>(c); asm volatile("s_nop 0"); dma<0>(c.l16, mk(c.gw, 896), 0);
  const u32 okvr = WS_KVR + (u32)(c.t * 4096 + (2 * c.kh + (c.w & 1)) * 1024);
  { const u32x4 ones = {~0u, ~0u, ~0u, ~0u}; st_dev(c, ones, R == 0 || c.tg < 2 ? c.l16 : OOB, R == 0 ? c.okvp : okvr); }
#pragma unroll 1
  for (u32 i = 0; i < NB1; ++i) bulk_mark(c, 176 - 8 * NB1 + i * 8 + c.w);
  spare_x<4, 14>(c);
  issue_range<R, 21, 25>(c);
  c.tag = epoch + 1;
  asm volatile("" : "+s"(c.tag) :: "memory");
  wait_bar<4>();
#pragma unroll
  for (int s = 0; s < 28; ++s) p.xf[s] = as_frag(lds_rd128((p.ax ^ (u32)(32 * (s & 3))) + sxpos(s >> 2)));
  bar_l();
  STAMP(1);
  issue_range<R, 25, 29>(c);
  issue_range<R, 30, 31>(c);
  issue_range<R, 33, 34>(c);
  issue_range<R, 36, 37>(c);
  f32x16 ga = {};
#pragma unroll
  for (int s = 14 * R; s < 14 * R + 14; ++s) ga = mfma(as_frag(lds_rd128(spos(2) + c.rot[s >> 2] * 128 + (s & 3) * 32 + (l >> 5) * 16)), p.xf[s], ga);
  PIN1(ga);
  p.a0 = p.a1 = (f32x16){};
  float g1 = 0.f;
  if constexpr (R == 0) *(LAS float*)(SCR + c.tg * 4096 + l * 4) = ga[0];
  wait_bar<7>();
  if constexpr (R == 0) flag_publish(c);
  if constexpr (R == 1) {
    g1 = __builtin_amdgcn_rcpf(1.0f + __builtin_amdgcn_exp2f(-LOG2E * (ga[0] + *(LAS float*)(SCR + c.tg * 4096 + l * 4))));
    __builtin_amdgcn_raw_buffer_store_b32(f2u(g1), c.sw, l < 32 ? l * 4 : OOB, WS_G + (u32)(c.kh * 2048 + c.t * 128 + c.tg * 32) * 4, 16);
  }
  pipe<R, 28, 28, 44, 1>(p);
  wait_bar_v<1>();
  STAMP(2);
  load_wphi(c, p.wp);
  pipe<R, 44, 44, 56, 1>(p);
  if constexpr (R == 0) {
    fm_slices<0, 24>(p);
    phi_image<0>(c, p); phi_image<1>(c, p);
  } else {
    v_handoff<spos(11), 0>(c, p.a1); v_handoff<spos(11), 1>(c, p.a1);
#pragma unroll 1
    for (u32 i = 0; i < NB2; ++i) bulk_mark(c, i * 4 + c.tg);
  }
  bar_l();
  if constexpr (R == 0) {
    if (c.w >= 2) return;
    kv_stage<spos(11), 0>(c, p); kv_stage<spos(11), 1>(c, p); kv_stage<spos(11), 2>(c, p); kv_stage<spos(11), 3>(c, p); kv_stage<spos(11), 4>(c, p);
    kv_stage<spos(11), 6>(c, p);
    STAMP(3);
    flag_spin(c, __builtin_amdgcn_raw_buffer_load_b64(c.sw, c.vflag, 0, 16));
    STAMP(4);
    u32x4 v[16];
    const u32 ro = WS_KVP + (u32)(c.t * 65536 + (2 * c.kh + c.w) * 1024);
    ld_lin<0, 16>(c, v, ro, 4096);
    chk<0, 16>(v, [&] { ld_lin<0, 16>(c, v, ro, 4096); });
    STAMP(5);
    float s[8], s2[8];
    set8(s, v[0]); set8(s2, v[1]);
#pragma unroll
    for (int i = 2; i < 16; i += 2) { add8(s, v[i]); add8(s2, v[i + 1]); }
#pragma unroll
    for (int i = 0; i < 8; ++i) s[i] += s2[i];
    st_dev(c, sanit(pack8<0>(s)), c.l16, okvr);
    STAMP(6);
  } else {
#pragma unroll 1
    for (u32 i = NB2; i < (176 - 8 * NB1) / 4; ++i) bulk_mark(c, i * 4 + c.tg);
    pipe<R, 57, 57, 112, 2>(p); PIN1(p.a0);
    asm volatile("s_waitcnt vmcnt(0)" ::: "memory");
    { u32x2 v = {(u32)c.tag, (u32)(c.tag >> 32)};
      __builtin_amdgcn_raw_buffer_store_b64(v, c.sw, l == 0 ? 0u : OOB, WS_FLAG + (u32)(c.t * 16 + c.hd) * 128 + c.w * 8, 16); }
    STAMP(3);
    fm_slices<0, 24>(p);
    flag_spin(c, __builtin_amdgcn_raw_buffer_load_b64(c.sw, c.vflag, 0, 16));
    u32x4 v[4];
    const u32 kvo = WS_KVR + (u32)c.hd * 4096;
    ld_1k<0, 4>(c, v, kvo);
    chk<0, 4>(v, [&] { __builtin_amdgcn_s_sleep(4); ld_1k<0, 4>(c, v, kvo); });
    o_publish(c, v, p.ph, g1);
    STAMP(7);
  }
}

extern "C" __global__ __launch_bounds__(512, 2) void fused_kernel(const char* x, const char* wqkv, const char* wphi, const char* wo, const char* wg,
                                                                   const char* wu, const char* wd, const char* gw, char* out, char* ws) {
  Ctx c;
  const int bid = __builtin_amdgcn_workgroup_id_x(), xcd = bid & 7, idx = bid >> 3;
  const u32 tid = __builtin_amdgcn_workitem_id_x();
  c.gw = gw; c.out = out; c.ws = ws;
  c.t = 4 * (xcd >> 1) + (idx & 3); c.kh = xcd & 1; c.jj = idx >> 2; c.hd = 8 * c.kh + c.jj; c.j = 7 * c.kh + c.jj;
  c.w = __builtin_amdgcn_readfirstlane(tid >> 6); c.tg = c.w & 3; c.r = c.w >> 2;
  c.lane = tid & 63; c.l16 = c.lane * 16; c.m0w = c.w * 1024;
  const u32 l = c.lane, w = c.w;
  STAMP(0);
  const u64 epoch = *(const __attribute__((address_space(4))) u64*)ws;
  {
    u32 c0 = (u32)(c.jj + 2 * (idx & 3) + (xcd >> 1)) % 7u;
#pragma unroll
    for (int i = 0; i < 7; ++i) { c.rot[i] = c0; c0 = c0 == 6 ? 0 : c0 + 1; }
  }
  const u32 row8 = 8 * w + (l >> 3), sw2 = ((l & 7) ^ (((l >> 4) & 1) << 2)) << 4;
  c.vx = row8 * 896 + (((l & 7) ^ ((4 * w + (l >> 4)) & 7)) << 4); c.vx2 = c.vx + 64 * 896;
  c.vw = row8 * 3584 + sw2;
  c.v2 = row8 * 896 + sw2; c.v2b = c.v2 + 64 * 896;
  c.vq = (16 * c.tg + (l >> 2)) * 2688 + (l & 3) * 16; c.vkv = c.vq + 896 * (1 + c.r);
  const u32 win = 56 * c.hd - 8 * (c.hd & 1);
  c.sx = mk(x + c.t * 114688, 114688);
  c.sq = mk(wqkv + win, 448 * 2688 - win);
  c.sp = mk(wphi + c.hd * 1568, 1568);
  c.sw = mk(ws, WS_END);
  c.oh = WS_H + (u32)(((c.t * 4 + c.tg) * 112 + 8 * c.j + 2 * c.r) * 1024);
  c.oo = WS_O + (u32)(((c.t * 4 + c.tg) * 16 + c.hd) * 2048);
  c.op = WS_P + (u32)((c.t * 16 + c.hd) * 16384 + c.tg * 4096);
  c.okvp = WS_KVP + (u32)((c.hd * 16 + c.t) * 4096 + c.tg * 1024);
  static_assert(WS_P % (16 * 16384) == 0, "the partner's slot is op ^ (8 * 16384)");
  asm volatile("" : "+s"(c.oh), "+s"(c.oo), "+s"(c.op), "+s"(c.okvp));
  c.vflag = flag_off(c, c.jj == 7); c.vtouch = l < 16 ? l * 128 : OOB;
  { const u32 rho = l & 31, f = rho < 26 ? rho : ((rho == 28 || rho == 29) ? rho - 2 : 0);
    c.awp = upos(28) + 5 * 1024 + 2 * (28 * (4 * (l >> 5) - 4 * (c.hd & 1)) + f); }
  P1 p;
  {
    const u32 q = (l & 15) >> 2, pp = l & 3, blk = (l >> 4) & 1, h = l >> 5, r = l & 31;
    p.aw = mkseg((8 * h + q) * 128 + ((64 * c.r) ^ ((q >> 1) << 6)) + 32 * blk + 8 * pp);
    p.aq = mkseg((8 * h + q) * 64 + 32 * blk + 8 * pp);
    p.ax = (32 * c.tg + r) * 128 + ((h ^ ((r >> 1) & 7)) << 4);
  }
  if (c.jj < 7) {
    c.sg = mk(wg + c.j * 256, 448 * 3584 - c.j * 256);
    c.su = mk(wu + c.j * 256, 448 * 3584 - c.j * 256);
    c.sd = mk(wd + c.kh * 896 * 896 + c.jj * 128, 896 * 896 - c.jj * 128);
    c.so = mk(wo + c.kh * 224 * 896 + c.jj * 128, 224 * 896 - c.jj * 128);
    if (c.r == 0) { ffn_wg<0>(c, p, epoch); __builtin_amdgcn_endpgm(); }
    ffn_wg<1>(c, p, epoch);
    __builtin_amdgcn_endpgm();
  }
  {
    u32 lo; u64 pc;
    asm volatile("s_getpc_b64 %0\n.Lpf_here:\n\ts_mov_b32 %1, .Lpf_here-fused_kernel" : "=s"(pc), "=s"(lo));
    asm volatile("s_add_u32 m0, %0, %1\n\ts_nop 0\n\tbuffer_load_dword %2, %3, 0 offen lds"
                 :: "s"(c.m0w), "i"(spos(17)), "v"((w * 64 + l) * 128), "s"(mk((const char*)(pc - lo), CODE_BYTES)) : "memory", "scc");
  }
  if (c.r == 0) { spare_wg<0>(c, p, epoch); __builtin_amdgcn_endpgm(); }
  spare_wg<1>(c, p, epoch);
}

#if defined(__HIP_DEVICE_COMPILE__)
#pragma clang attribute pop
#endif

__global__ void amp_k(const unsigned long long* st){
  unsigned mn=~0u;
  for(int i=threadIdx.x;i<2048;i+=64){ unsigned a=(unsigned)st[i*32]; mn=a<mn?a:mn; }
  for(int o=32;o;o>>=1){ unsigned b=__shfl_xor(mn,o); mn=b<mn?b:mn; }
  unsigned sum=0,mx=0,cnt=0;
  for(int b=threadIdx.x;b<256;b+=64){ bool sp=(b>>5)==7; if(sp!=('a'=='s')) continue; unsigned d=(unsigned)st[(b*8+4)*32+PROBE_K]-mn; if(d<100000){ sum+=d; mx=d>mx?d:mx; cnt++; } }
  for(int o=32;o;o>>=1){ sum+=__shfl_xor(sum,o); cnt+=__shfl_xor(cnt,o); unsigned b=__shfl_xor(mx,o); mx=b>mx?b:mx; }
  unsigned long long m = 'a'=='m' ? (unsigned long long)mx*100 : (unsigned long long)sum*100/(cnt?cnt:1);
  unsigned long long t0=__builtin_amdgcn_s_memrealtime();
  while(__builtin_amdgcn_s_memrealtime()-t0<m) __builtin_amdgcn_s_sleep(4);
}
extern "C" void kernel_launch(void* const* d_in, const int* in_sizes, int n_in, void* d_out, int out_size, void* d_ws, size_t ws_size,
                              hipStream_t stream) {
  (void)hipFuncSetAttribute((const void*)fused_kernel, hipFuncAttributeMaxDynamicSharedMemorySize, LDS_BYTES);
  fused_kernel<<<256, 512, LDS_BYTES, stream>>>((const char*)d_in[0], (const char*)d_in[1], (const char*)d_in[2], (const char*)d_in[3],
                                                (const char*)d_in[5], (const char*)d_in[6], (const char*)d_in[7], (const char*)d_in[8],
                                                (char*)d_out, (char*)d_ws);
  amp_k<<<1,64,0,stream>>>((const unsigned long long*)((char*)d_ws + WS_STAMP));
}
```

```cpp
#define STAMPS 4
#define PROBE_K 31
#include <hip/hip_runtime.h>
#include <hip/hip_bf16.h>
#include <type_traits>
#pragma clang diagnostic ignored "-Wint-to-pointer-cast"

typedef unsigned u32;
typedef unsigned long long u64;
typedef u32 u32x4 __attribute__((ext_vector_type(4)));
typedef u32 u32x2 __attribute__((ext_vector_type(2)));
typedef short s16x4 __attribute__((ext_vector_type(4)));
typedef __bf16 bf16x8 __attribute__((ext_vector_type(8)));
typedef __bf16 bf16x2 __attribute__((ext_vector_type(2)));
typedef float f32x16 __attribute__((ext_vector_type(16)));
typedef float f32x4 __attribute__((ext_vector_type(4)));
typedef float f32x2 __attribute__((ext_vector_type(2)));
typedef __amdgpu_buffer_rsrc_t rsrc_t;
#define DI __device__ __forceinline__
#define LAS __attribute__((address_space(3)))
#define SB0() __builtin_amdgcn_sched_barrier(0)
#define PIN1(a) asm volatile("" : "+v"(a))
#define PIN2(a, b) asm volatile("" : "+v"(a), "+v"(b))

constexpr u32 WS_FLAG = 4096;
constexpr u32 WS_G    = WS_FLAG + 256 * 128;
constexpr u32 WS_KVR  = 65536;
constexpr u32 WS_KVP  = 262144;
constexpr u32 WS_O    = WS_KVP + 256 * 4096;
constexpr u32 WS_P    = WS_O + 64 * 32 * 1024;
constexpr u32 WS_H    = WS_P + 256 * 16384;
constexpr u32 WS_END  = WS_H + 64 * 112 * 1024;
constexpr u32 WS_STAMP = 32u << 20;

constexpr int UNIT = 8192, RING = 18;
constexpr u32 SCR = 0, RBASE = 2 * UNIT;
constexpr u32 TOUCH_LDS = RBASE + UNIT + 4096;
constexpr int LDS_BYTES = 163840;
constexpr u32 OOB = 0x80000000u;
constexpr u32 CODE_BYTES = 40960;
#define ABL 0
#define MARKV 0
#define TOUCH2 1
#define PSLEEP 8
#define PRIO 1
#define PROV 9
constexpr u32 SPIN_MAX = ABL ? 0 : 1u << 16;
constexpr float LOG2E = 1.4426950408889634f;

#if defined(__HIP_DEVICE_COMPILE__)
#pragma clang attribute push(__attribute__((target("no-packed-fp32-ops"))), apply_to = function)
#endif
constexpr u32 spos(int k) { return RBASE + k * UNIT; }
constexpr int w2c_(int i) { constexpr int t[18] = {0, 1, 14, 2, 3, 15, 4, 5, 16, 6, 7, 17, 8, 9, 10, 11, 12, 13}; return t[i]; }
constexpr int w2s_(int c) { constexpr int t[18] = {8, 2, 3, 4, 5, 6, 7, 9, 10, 11, 12, 13, 14, 15, -2, -1, 0, 1}; return t[c]; }
constexpr u32 upos(int n) { return spos(n < 44 ? n % RING : n < 53 ? (n + 1) % RING : w2s_(w2c_(n - 53))); }
constexpr int imin(int a, int b) { return a < b ? a : b; }

struct Ctx {
  const char* gw;
  char *out, *ws;
  int t, kh, jj, hd, j, w, tg, r;
  u32 lane, l16, m0w;
  u32 vx, vx2, vw, vq, vkv, v2, v2b;
  rsrc_t sx, sg, su, sq, sp, sd, so, sw;
  u64 tag;
  u32 rot[7];
  u32 oh, oo, op, okvp;
  u32 vflag, vtouch, awp;
};

DI rsrc_t mk(const void* p, u32 bytes) { return __builtin_amdgcn_make_buffer_rsrc((void*)p, 0, bytes, 0x00020000); }
template <u32 L> DI void set_m0(const Ctx& c) { if (!(ABL & 1)) asm volatile("s_add_u32 m0, %0, %1" :: "s"(c.m0w), "i"(L) : "memory", "scc"); }
template <u32 OFF> DI void dma(u32 voff, rsrc_t rs, u32 soff) {
  if (!(ABL & 1)) asm volatile("buffer_load_dwordx4 %0, %1, %2 offen offset:%3 lds" :: "v"(voff), "s"(rs), "s"(soff), "i"(OFF) : "memory");
}
template <int N> DI void wait_bar() { asm volatile("s_waitcnt vmcnt(%0) lgkmcnt(0)\n\ts_barrier" :: "n"(N) : "memory"); }
template <int N> DI void wait_bar_v() { asm volatile("s_waitcnt vmcnt(%0)\n\ts_barrier" :: "n"(N) : "memory"); }
DI void bar_l() { asm volatile("s_waitcnt lgkmcnt(0)\n\ts_barrier" ::: "memory"); }

constexpr int uq_(int ch) { return ch == 0 ? 28 : 30 + 3 * ((ch - 1) / 2); }
constexpr int uu_(int ch) { return ch == 0 ? 29 : 31 + 3 * ((ch - 1) / 2) + ((ch - 1) & 1); }
constexpr u32 ioff_(int n) { return n >= 39 && n < 53 ? 128 : 0; }
template <int N> DI void unit_m0(const Ctx& c) { set_m0<upos(N) - ioff_(N)>(c); }
template <int R, int N> DI void unit_go(const Ctx& c) {
  if constexpr (N < 21) {
    if constexpr (N % 3 < 2) dma<0>(N % 3 ? c.vx2 : c.vx, c.sx, c.rot[N / 3] * 128);
    else dma<0>(c.vw, c.sg, c.rot[N / 3] * (64 * 3584));
  } else if constexpr (N < 28) dma<0>(c.vkv, c.sq, c.rot[N - 21] * (64 * 2688));
  else if constexpr (N == 28) {
    if constexpr (R == 0) dma<0>(c.vq, c.sq, c.rot[0] * (64 * 2688));
    else dma<0>(c.l16, c.sp, c.w == 5 ? 0u : c.w == 6 ? 1024u : 0x40000000u);
  } else if constexpr (N < 39) {
    if constexpr (N % 3 == 0) dma<0>(c.vq, c.sq, c.rot[2 * ((N - 30) / 3) + 1 + R] * (64 * 2688));
    else dma<0>(c.vw, c.su, c.rot[N == 29 ? 0 : 2 * ((N - 30) / 3) + N % 3] * (64 * 3584));
  } else if constexpr (N < 53) dma<128>(c.vw, N < 46 ? c.sg : c.su, c.rot[(N - 39) % 7] * (64 * 3584));
  else {
    constexpr int k = w2c_(N - 53);
    if constexpr (k < 14) dma<0>(k < 7 ? c.v2 : c.v2b, c.sd, c.rot[k % 7] * (128 * 896));
    else dma<0>((k & 1) ? c.v2b : c.v2, c.so, k < 16 ? 0u : 128u * 896u);
  }
}
template <int R, int A, int B> DI void issue_range(const Ctx& c) {
  if constexpr (A < B) { unit_m0<A>(c); asm volatile("s_nop 0"); unit_go<R, A>(c); issue_range<R, A + 1, B>(c); }
}

struct Seg { u32 b[3]; };
DI Seg mkseg(u32 a) { Seg s; s.b[0] = a; s.b[1] = a + 65536; s.b[2] = a + 131072; asm volatile("" : "+v"(s.b[0]), "+v"(s.b[1]), "+v"(s.b[2])); return s; }
DI u32 sa(const Seg& s, u32 off) { return s.b[off >> 16] + (off & 0xffffu); }
DI f32x16 mfma(bf16x8 a, bf16x8 b, f32x16 c) { return __builtin_amdgcn_mfma_f32_32x32x16_bf16(a, b, c, 0, 0, 0); }
DI bf16x8 as_frag(u32x4 v) { return __builtin_bit_cast(bf16x8, v); }
DI bf16x8 tr_frag(u32 a0, u32 a1) {
  s16x4 lo = __builtin_amdgcn_ds_read_tr16_b64_v4i16((LAS s16x4*)a0);
  s16x4 hi = __builtin_amdgcn_ds_read_tr16_b64_v4i16((LAS s16x4*)a1);
  return __builtin_bit_cast(bf16x8, __builtin_shufflevector(lo, hi, 0, 1, 2, 3, 4, 5, 6, 7));
}
DI u32x4 lds_rd128(u32 a) { return *(LAS u32x4*)a; }
DI void lds_wr128(u32 a, u32x4 v) { *(LAS u32x4*)a = v; }
DI u32 f2u(float f) { return __builtin_bit_cast(u32, f); }
DI float u2f(u32 u) { return __builtin_bit_cast(float, u); }
DI u32 pk(float a, float b) { f32x2 t = {a, b}; return __builtin_bit_cast(u32, __builtin_convertvector(t, bf16x2)); }
template <int S, class V> DI u32x4 pack8(const V& v) {
  u32x4 r = {pk(v[8 * S], v[8 * S + 1]), pk(v[8 * S + 2], v[8 * S + 3]), pk(v[8 * S + 4], v[8 * S + 5]), pk(v[8 * S + 6], v[8 * S + 7])};
  return r;
}
DI float blo(u32 d) { return u2f(d << 16); }
DI float bhi(u32 d) { return u2f(d & 0xffff0000u); }
DI u32 umax(u32 a, u32 b) { return a > b ? a : b; }
DI bool any_(bool p) { return __builtin_amdgcn_ballot_w64(p) != 0; }
DI u32x4 sanit(u32x4 v) { return __builtin_elementwise_min(v, (u32x4){0xfffffffeu, 0xfffffffeu, 0xfffffffeu, 0xfffffffeu}); }
DI void st_dev(const Ctx& c, u32x4 v, u32 voff, u32 soff) {
  if (ABL & 8) { asm volatile("" :: "v"(v)); return; }
  __builtin_amdgcn_raw_buffer_store_b128(v, c.sw, voff, soff, 16);
  asm volatile("s_nop 1" :: "v"(v) : "memory");
}
DI u32x4 ld_dev(const Ctx& c, u32 voff, u32 soff) {
  if (ABL & 16) { u32x4 z = {voff, soff, voff, soff}; asm volatile("" : "+v"(z)); return z; }
  return __builtin_amdgcn_raw_buffer_load_b128(c.sw, voff, soff, 16);
}
DI f32x2 xhalf(float v) { auto p = __builtin_amdgcn_permlane32_swap(f2u(v), f2u(v), false, false); f32x2 r = {u2f(p[0]), u2f(p[1])}; return r; }

DI void stamp(const Ctx& c, int i) {
  SB0();
  u64 tm, tc; asm volatile("s_memrealtime %0\n\ts_memtime %1\n\ts_waitcnt lgkmcnt(0)" : "=s"(tm), "=s"(tc) :: "memory");
  tm = (tm & 0xffffffffull) | (tc << 32);
  { u32x2 v = {(u32)tm, (u32)(tm >> 32)}; __builtin_amdgcn_raw_buffer_store_b64(v, mk(c.ws + WS_STAMP, 1 << 20), c.lane == 0 ? 0u : OOB, ((__builtin_amdgcn_workgroup_id_x() * 8 + c.w) * 32 + i) * 8, 0); }
  SB0();
}
#define STAMP(i) do { if ((i) == 0 || (i) == PROBE_K) stamp(c, i); } while (0)

struct P1 {
  Seg aw, aq; u32 ax;
  bf16x8 xf[28], f[8], af[8], vf[8];
  f32x16 g1, u1, a0, a1, g2, u2, fp, k0;
  u32x4 wp[2], ph[4], h1[14], kv[4];
  float y[16], e[14], sum, mxl, mnl;
  u32x2 poll;
  float gq, gt;
  u32 hbase; int TG;
};

DI void load_wphi(const Ctx& c, u32x4 (&wp)[2]) {
  const u32 a = c.awp;
#pragma unroll
  for (int s = 0; s < 2; ++s)
#pragma unroll
    for (int d = 0; d < 4; ++d) {
      const int e0 = 2 * d, k0 = 16 * s + 8 * (e0 >> 2) + (e0 & 3);
      u32 lo = *(LAS unsigned short*)(a + 56 * k0), hi = *(LAS unsigned short*)(a + 56 * (k0 + 1));
      wp[s][d] = lo | (hi << 16);
    }
}
template <int K> DI void fm_slice(P1& p) {
  if constexpr (K == 0) { p.fp = mfma(as_frag(p.wp[0]), as_frag(pack8<0>(p.a0)), (f32x16){}); }
  else if constexpr (K == 1) { p.fp = mfma(as_frag(p.wp[1]), as_frag(pack8<1>(p.a0)), p.fp); }
  else if constexpr (K == 2) { }
  else if constexpr (K == 3) {
    PIN1(p.fp);
    float mx = p.fp[0];
#pragma unroll
    for (int i = 1; i < 14; ++i) mx = __builtin_fmaxf(mx, p.fp[i]);
    p.mxl = mx; PIN1(p.mxl);
  } else if constexpr (K == 4) {
    float mn = p.fp[0];
#pragma unroll
    for (int i = 1; i < 14; ++i) mn = __builtin_fminf(mn, p.fp[i]);
    p.mnl = mn; PIN1(p.mnl);
  } else if constexpr (K == 5) {
    f32x2 t = xhalf(p.mxl); p.mxl = __builtin_fmaxf(t[0], t[1]) * LOG2E;
    t = xhalf(p.mnl); p.mnl = __builtin_fminf(t[0], t[1]) * LOG2E;
    PIN2(p.mxl, p.mnl);
  } else {
    constexpr int NEG = K >= 15, k = K - 6 - 9 * NEG;
    if constexpr (k < 5) {
#pragma unroll
      for (int i = 3 * k; i < imin(14, 3 * k + 3); ++i) {
        p.e[i] = __builtin_amdgcn_exp2f(NEG ? __builtin_fmaf(p.fp[i], -LOG2E, p.mnl) : __builtin_fmaf(p.fp[i], LOG2E, -p.mxl));
        PIN1(p.e[i]);
      }
      if constexpr (k == 1) p.sum = p.e[0] + p.e[1] + p.e[2];
      if constexpr (k > 1) p.sum += p.e[3 * k - 3] + p.e[3 * k - 2] + p.e[3 * k - 1];
      if constexpr (k > 0) PIN1(p.sum);
    } else if constexpr (k == 5) {
      f32x2 t = xhalf(p.sum + p.e[12] + p.e[13]); p.sum = __builtin_amdgcn_rcpf(t[0] + t[1]); PIN1(p.sum);
    } else if constexpr (k < 8) {
#pragma unroll
      for (int i = 7 * (k - 6); i < 7 * (k - 5); ++i) { p.e[i] *= p.sum; PIN1(p.e[i]); }
    } else {
      u32x4 o1 = {pk(p.e[8], p.e[9]), pk(p.e[10], p.e[11]), pk(p.e[12], p.e[13]), 0u};
      p.ph[2 * NEG] = pack8<0>(p.e); p.ph[2 * NEG + 1] = o1; PIN2(p.ph[2 * NEG], p.ph[2 * NEG + 1]);
    }
  }
}
template <int A, int B> DI void fm_slices(P1& p) { if constexpr (A < B) { fm_slice<A>(p); fm_slices<A + 1, B>(p); } }
template <u32 VB, int S> DI void v_handoff(const Ctx& c, const f32x16& av) {
  u32x4 vf = pack8<S>(av);
  const bool one = (c.lane & 31) == ((c.hd & 1) ? 0u : 28u);
#pragma unroll
  for (int d = 0; d < 4; ++d) vf[d] = one ? 0x3f803f80u : vf[d];
  lds_wr128(VB + c.tg * 2048 + c.l16 + S * 1024, vf);
}
template <int T> DI void phi_image(const Ctx& c, P1& p) {
  const u32 tok = c.lane & 31, h = c.lane >> 5;
  const u32 ft = (((tok >> 1) & 1) << 3) | (tok & 1) | (((tok >> 2) & 3) << 1);
  const u32 wa = SCR + c.tg * 4096 + tok * 128 + ((h ^ ft) << 3);
#pragma unroll
  for (int g = 0; g < 4; ++g) {
    u32x2 v = {p.ph[2 * T + (g >> 1)][2 * (g & 1)], p.ph[2 * T + (g >> 1)][2 * (g & 1) + 1]};
    *(LAS u32x2*)(wa ^ (u32)((8 * T + 2 * g) << 3)) = v;
  }
}
template <u32 VB, int K> DI void kv_stage(const Ctx& c, P1& p) {
  const u32 l = c.lane, h = l >> 5;
  if constexpr (K >= 1 && K <= 4) {
    p.k0 = mfma(p.af[2 * K - 2], p.vf[2 * K - 2], K == 1 ? (f32x16){} : p.k0);
    p.k0 = mfma(p.af[2 * K - 1], p.vf[2 * K - 1], p.k0);
  }
  if constexpr (K < 4) {
    const u32 q = (l & 15) >> 2, pp = l & 3, blk = (l >> 4) & 1;
    const u32 ra = SCR + (4 * h + q) * 128 + (((4 * blk + pp) ^ (q & 1) ^ (h << 1) ^ ((q >> 1) << 3) ^ ((c.tg & 1) << 3)) << 3);
#pragma unroll
    for (int s = 0; s < 2; ++s) {
      p.af[2 * K + s] = tr_frag(ra + K * 4096 + s * 2048, (ra ^ 32u) + K * 4096 + s * 2048 + 1024);
      p.vf[2 * K + s] = as_frag(lds_rd128(VB + c.l16 + K * 2048 + s * 1024));
    }
  }
  if constexpr (K == 6) {
    const u32 vo = c.tg < 2 ? c.l16 : OOB;
    st_dev(c, sanit(pack8<0>(p.k0)), vo, c.okvp + c.tg * 1024);
    st_dev(c, sanit(pack8<1>(p.k0)), vo + 1024, c.okvp + c.tg * 1024);
  }
}
DI void add8(float (&s)[8], u32x4 v) {
#pragma unroll
  for (int d = 0; d < 4; ++d) { s[2 * d] += blo(v[d]); s[2 * d + 1] += bhi(v[d]); }
}
DI void set8(float (&s)[8], u32x4 v) {
#pragma unroll
  for (int d = 0; d < 4; ++d) { s[2 * d] = blo(v[d]); s[2 * d + 1] = bhi(v[d]); }
}
template <int A, int B, int N> DI bool has_mark(const u32x4 (&v)[N]) {
  u32 m = 0;
#pragma unroll
  for (int i = A; i < B; ++i) m = umax(umax(m, v[i][0]), umax(v[i][1], umax(v[i][2], v[i][3])));
  return any_(m == 0xffffffffu);
}
template <int A, int B, int N, class F> DI void chk(u32x4 (&v)[N], F&& reload) {
  if (__builtin_expect(SPIN_MAX && has_mark<A, B>(v), 0)) {
    u32 spin = 0;
    do { asm volatile("" ::: "memory"); reload(); } while (has_mark<A, B>(v) && ++spin < SPIN_MAX);
  }
}
template <int A, int B, int N> DI void ld_lin(const Ctx& c, u32x4 (&v)[N], u32 soff, u32 stride) {
#pragma unroll
  for (int i = A; i < B; ++i) v[i] = ld_dev(c, c.l16, soff + i * stride);
}
template <int A, int B, int N> DI void ld_1k(const Ctx& c, u32x4 (&v)[N], u32 soff) {
#pragma unroll
  for (int i = A; i < B; ++i) v[i] = ld_dev(c, c.l16 + (i & 3) * 1024, soff + (i >> 2) * 4096);
}
template <int A, int B> DI void ld_h(const Ctx& c, u32x4 (&v)[14], u32 voff, u32 soff) {
#pragma unroll
  for (int i = A; i < B; ++i) v[i] = ld_dev(c, voff + (i & 1) * 1024, soff + c.rot[i >> 1] * 8192);
}
DI void o_publish(const Ctx& c, const u32x4 (&kv)[4], const u32x4 (&phq)[4], float g) {
  f32x16 n = {};
#pragma unroll
  for (int i = 0; i < 4; ++i) n = mfma(as_frag(kv[i]), as_frag(phq[i]), n);
  const bool odd = c.hd & 1;
  f32x2 t = xhalf(odd ? n[0] : n[12]);
  float sc = g * __builtin_amdgcn_rcpf((odd ? t[0] : t[1]) + 1e-6f);
#pragma unroll
  for (int i = 0; i < 16; ++i) n[i] *= sc;
  u32x4 f0 = sanit(pack8<0>(n)), f1 = sanit(pack8<1>(n));
  const bool z0 = odd && c.lane < 32, z1 = !odd && c.lane >= 32;
  f0[0] = z0 ? 0 : f0[0]; f0[1] = z0 ? 0 : f0[1];
  f1[2] = z1 ? 0 : f1[2]; f1[3] = z1 ? 0 : f1[3];
  st_dev(c, f0, c.l16, c.oo);
  st_dev(c, f1, c.l16 + 1024, c.oo);
}
DI u32 flag_off(const Ctx& c, bool spare) {
  const u32 i = c.lane & 15;
  u32 red = (c.hd * 16 + 7 + 8 * (i & 1)) << 7, late = ((c.t * 16 + 7 + 2 * (i & 4)) << 7) + 32 + (i & 3) * 8;
  u32 lo = (spare ? i * 16 + c.t : c.t * 16 + i) << 7;
  asm volatile("" : "+v"(red), "+v"(late), "+v"(lo));
  return WS_FLAG + (!(c.lane & 16) ? lo : (spare || (i & 8)) ? red : late);
}
DI bool flag_bad(const Ctx& c, u32x2 f) { return any_(f[0] != (u32)c.tag || f[1] != (u32)(c.tag >> 32)); }
DI void flag_spin(const Ctx& c, u32x2 f) {
  if (__builtin_expect(SPIN_MAX && flag_bad(c, f), 0)) {
    u32 spin = 0;
    do {
      __builtin_amdgcn_s_sleep(2);
      asm volatile("" ::: "memory");
      f = __builtin_amdgcn_raw_buffer_load_b64(c.sw, c.vflag, 0, 16);
    } while (flag_bad(c, f) && ++spin < SPIN_MAX);
  }
  asm volatile("" ::: "memory");
}
DI void flag_publish(const Ctx& c) {
  u32x2 v = {(u32)c.tag, (u32)(c.tag >> 32)};
  __builtin_amdgcn_raw_buffer_store_b64(v, c.sw, c.lane + c.w == 0 ? 0u : OOB, WS_FLAG + (u32)(c.t * 16 + c.hd) * 128, 16);
}
DI float ld_g(const Ctx& c, int tgroup) {
  return u2f(__builtin_amdgcn_raw_buffer_load_b32(c.sw, (c.lane & 31) * 4, WS_G + (u32)(c.kh * 2048 + c.t * 128 + tgroup * 32) * 4, 16));
}
template <int TG> DI void mark_tg(const Ctx& c, u32 voff, u32 soff) {
  if (ABL & 8) return;
  const u32x4 ones = {~0u, ~0u, ~0u, ~0u};
  asm volatile("s_cmp_lg_u32 %0, %1\n\ts_cbranch_scc1 1f\n\tbuffer_store_dwordx4 %2, %3, %4, %5 offen sc1\n\ts_nop 1\n1:"
               :: "s"(c.tg), "i"(TG), "v"(ones), "v"(voff), "s"(c.sw), "s"(soff) : "memory", "scc");
}
DI void touch(const Ctx& c, u32 soff) {
  if (ABL & 8) return;
  asm volatile("s_mov_b32 m0, %0\n\ts_nop 0\n\tbuffer_load_dword %1, %2, %3 offen sc1 lds"
               :: "i"(TOUCH_LDS), "v"(c.vtouch), "s"(c.sw), "s"(soff) : "memory");
}

constexpr int PRO = PROV;
constexpr int need_(int s) { return s < 0 ? 0 : s < 7 ? 3 * (s + 1) : s < 10 ? 23 + 2 * (s - 7) : s == 10 ? 28 : s < 15 ? 30 + 3 * (s - 11) : 41 + 2 * (s - 15); }
constexpr int iss_(int s) {
  constexpr int t[6] = {56, 59, 62, 65, 67, 71};
  return s < 0 ? PRO : s >= 16 ? t[s - 16] : imin(18 + need_(s - 2) - (s >= 11 ? 1 : 0), PRO + 3 * (s + 1));
}
static_assert(iss_(10) <= 44 && iss_(15) == 53, "ring schedule");
constexpr int S_FLAG = 13, S_POLL = 18;
constexpr int nmark_(int r, int s) { return s == 6 ? 1 - r : (s >= 7 && s <= 9) ? 1 : s == 10 ? 1 - r : s == 11 ? r : 0; }
constexpr int oth_(int r, int s) {
  return nmark_(r, s) + (s == S_FLAG ? 1 - r : s == 14 ? 2 - 2 * r : 0) + (s == 15 ? 2 : s == S_POLL ? 2 : s == 19 ? 5 + r : s == 20 ? 5 : s == 21 ? 5 + 4 * r : 0);
}
constexpr int tot_(int r, int s) { return iss_(s) - iss_(s - 1) + oth_(r, s); }
constexpr int vm_(int r, int s) {
  int sp = -1; while (iss_(sp) < need_(s)) ++sp;
  int n = iss_(sp) - need_(s);
  for (int k = sp + 1; k < s; ++k) n += tot_(r, k);
  if (s == S_FLAG) n = imin(n, tot_(r, s - 1));
  if (s == 21) n = imin(n, tot_(r, 20) + 2);
  if (s == S_POLL + 1) n = imin(n, tot_(r, S_POLL) - 1);
  return n;
}
constexpr int F2_(int r) { return r ? 112 : 84; }
constexpr int item0_(int r, int s) { return s < 7 ? 4 * s : s < 11 ? 28 + 8 * (s - 7) : s == 11 ? 56 : s < 15 ? 56 + (1 + r) * (8 * (s - 12) + 4) : F2_(r) + 8 * (s - 15); }

template <int R, int J> DI void rd_item(P1& p) {
  constexpr bool att = (J >= 28 && J < 56) || (R == 1 && J >= 56 && J < 112 && (J & 1));
  if constexpr (att) {
    constexpr int k = J < 56 ? J - 28 : (J - 56) / 2, ch = k / 4;
    constexpr u32 o = (J < 56 ? upos(21 + ch) + R * 4096 : upos(uq_(ch)) + (ch > 0 && !(ch & 1) ? 4096 : 0)) + (k % 4) * 1024;
    p.f[J % 8] = tr_frag(sa(p.aq, o), sa(p.aq, o + 256));
  } else {
    constexpr int k = J < 28 ? J : J < F2_(R) ? (J - 56) / (1 + R) : J - F2_(R);
    constexpr u32 o = upos(J < 28 ? 3 * (k / 4) + 2 : J < F2_(R) ? uu_(k / 4) : 39 + k / 4) + (k % 4) * 2048;
    p.f[J % 8] = tr_frag(sa(p.aw, o), sa(p.aw, o + 512));
  }
}
template <int R, int J> DI void mm_item(P1& p) {
  if constexpr (J < 0) {}
  else if constexpr (J < 28) p.g1 = mfma(p.f[J % 8], p.xf[J], p.g1);
  else if constexpr (J < 56) { if constexpr (R == 0) p.a0 = mfma(p.f[J % 8], p.xf[J - 28], p.a0); else p.a1 = mfma(p.xf[J - 28], p.f[J % 8], p.a1); }
  else if constexpr (J < F2_(R)) {
    constexpr int k = (J - 56) / (1 + R);
    if constexpr (R == 1 && (J & 1)) p.a0 = mfma(p.f[J % 8], p.xf[k], p.a0); else p.u1 = mfma(p.f[J % 8], p.xf[k], p.u1);
  } else { constexpr int k = J - F2_(R); if constexpr (k < 28) p.g2 = mfma(p.f[J % 8], p.xf[k], p.g2); else p.u2 = mfma(p.f[J % 8], p.xf[k - 28], p.u2); }
}
template <int R, int A, int B> DI void mm_range(P1& p) { if constexpr (A < B) { mm_item<R, A>(p); mm_range<R, A + 1, B>(p); } }

constexpr float GK1 = -2.0f * 0.7978845608028654f * LOG2E, GK2 = GK1 * 0.044715f;
template <int OP> DI void gelu_op(float (&y)[16], const f32x16& g) {
  constexpr int st = OP % 4, i = OP / 4 - st;
  if constexpr (i >= 0 && i < 16) {
    if constexpr (st == 0) y[i] = g[i] * __builtin_fmaf(g[i] * g[i], GK2, GK1);
    else if constexpr (st == 1) y[i] = __builtin_amdgcn_exp2f(y[i]);
    else if constexpr (st == 2) y[i] = __builtin_amdgcn_rcpf(1.0f + y[i]);
    else y[i] = g[i] * y[i];
    PIN1(y[i]);
  }
}
constexpr int gcost(int op) { return op / 4 - op % 4 < 0 || op / 4 - op % 4 > 15 ? 0 : op % 4 == 0 ? 3 : op % 4 == 1 ? 2 : op % 4 == 2 ? 3 : 1; }
constexpr int gcum(int k, int n) { if (k >= n) return 76; int op = 0, cst = 0; while (cst * n < k * 144) cst += gcost(op++); return op; }
template <int A, int B> DI void gelu_ops(float (&y)[16], const f32x16& g) { if constexpr (A < B) { gelu_op<A>(y, g); gelu_ops<A + 1, B>(y, g); } }
template <int S> DI void h_store(const Ctx& c, const float (&y)[16], const f32x16& u, int blk) {
  float h[8];
#pragma unroll
  for (int i = 0; i < 8; ++i) h[i] = y[8 * S + i] * u[8 * S + i];
  st_dev(c, sanit(pack8<0>(h)), c.l16 + S * 1024, c.oh + blk * 4096);
}
template <int R, int S, int K, int J> DI void slot_work(Ctx& c, P1& p) {
  if constexpr (S < 7) p.xf[J] = as_frag(lds_rd128((p.ax + upos(3 * S)) ^ (u32)(32 * K)));
  if constexpr (nmark_(R, S) > 0) {
    if constexpr (S == 6) mark_tg<K>(c, c.l16, c.okvp);
    else if constexpr (S == 10) mark_tg<K>(c, c.l16 + 1024, c.oh + 4096);
    else if constexpr ((K & 1) == R) mark_tg<K / 2>(c, S == 8 || S == 11 ? c.l16 + 1024 : c.l16, S >= 9 ? c.oh + 4096 : c.oh);
  }
  if constexpr (R == 0) {
    if constexpr (J >= 33 && J < 60) gelu_ops<gcum(J - 33, 27), gcum(J - 32, 27)>(p.y, p.g1);
    if constexpr (J == 56) load_wphi(c, p.wp);
    if constexpr (J >= 60 && J < 76) fm_slices<(3 * (J - 60) + 1) / 2, J == 75 ? 24 : (3 * (J - 59) + 1) / 2>(p);
    if constexpr (J == 70) phi_image<0>(c, p);
    if constexpr (J == 75) phi_image<1>(c, p);
    if constexpr (J >= 76 && J < 83) kv_stage<spos(8), J - 76>(c, p);
  } else {
    constexpr int G = J < 56 ? J - 33 : J - 96 + 23;
    if constexpr ((J >= 33 && J < 56) || (J >= 96 && J < 112)) gelu_ops<gcum(G, 39), gcum(G + 1, 39)>(p.y, p.g1);
    if constexpr (J == 61 || J == 62) v_handoff<spos(8), J - 61>(c, p.a1);
    if constexpr (J == 57) load_wphi(c, p.wp);
    if constexpr (J == 60) PIN2(p.wp[0], p.wp[1]);
  }
  if constexpr (S >= 15) {
    constexpr int g = J - F2_(R);
    if constexpr (g == 5 || g == 6) h_store<g - 5>(c, p.y, p.u1, 0);
    if constexpr (R == 1 && g >= 7 && g < 31) fm_slice<g - 7>(p);
    if constexpr (g >= 33) gelu_ops<gcum(g - 33, 23), gcum(g - 32, 23)>(p.y, p.g2);
    if constexpr (g == 26) touch(c, c.oh);
    if constexpr (R == 1 && S == 21 && !(K & 1)) ld_1k<K / 2, K / 2 + 1>(c, p.kv, WS_KVR + (u32)c.hd * 4096);
    if constexpr (S == 19 && K == 0) { p.gt = ld_g(c, p.TG); if constexpr (R == 1) p.gq = ld_g(c, c.tg); }
    if constexpr (S == 19 && (K & 1)) ld_h<K / 2, K / 2 + 1>(c, p.h1, c.l16, p.hbase);
    if constexpr (S >= 20 && ((K & 1) || K == 4)) { constexpr int i = 4 + 5 * (S - 20) + (K < 4 ? K / 2 : (K + 1) / 2); ld_h<i, i + 1>(c, p.h1, c.l16, p.hbase); }
  }
}
template <int R, int S, int J0, int K, int N> DI void slots(Ctx& c, P1& p) {
  if constexpr (K < N) {
    constexpr int nu = iss_(S) - iss_(S - 1), u0 = iss_(S - 1);
    constexpr int ua = K == 0 ? 0 : ((2 * K + R) * nu) / (2 * N), ub = K == N - 1 ? nu : ((2 * K + 2 + R) * nu) / (2 * N);
    if constexpr (R == 1 && PRIO) {
      if constexpr (K == 0 && S >= 15 && S < 19) __builtin_amdgcn_s_setprio(1);
      if constexpr (S >= 19 && (K == 0 || K == 4)) __builtin_amdgcn_s_setprio(K == 0);
    }
    mm_item<R, J0 + K - 4>(p);
    if constexpr (ub > ua) unit_m0<u0 + ua>(c);
    rd_item<R, (J0 + K) ^ 1>(p);
    if constexpr (ub > ua) { unit_go<R, u0 + ua>(c); issue_range<R, u0 + ua + 1, u0 + ub>(c); }
    slot_work<R, S, K, J0 + K>(c, p);
    SB0();
    slots<R, S, J0, K + 1, N>(c, p);
  }
}
template <int R, int S> DI void step(Ctx& c, P1& p) {
  constexpr int j0 = item0_(R, S), n = item0_(R, S + 1) - j0;
  if constexpr (S == S_POLL + 1) {
    asm volatile("s_waitcnt vmcnt(%1)\n\ts_barrier" : "+v"(p.poll) : "n"(vm_(R, S)) : "memory");
    PIN1(p.g2);
    flag_spin(c, p.poll);
  } else if constexpr ((S == 14 && R == 0) || S == 21) wait_bar<vm_(R, S)>();
  else wait_bar_v<vm_(R, S)>();
  if constexpr (S < 2 || S >= 6) STAMP(S < 2 ? S + 1 : S - 3);
  if constexpr (S == S_FLAG && R == 0) flag_publish(c);
  if constexpr (S == S_POLL) asm volatile("buffer_load_dwordx2 %0, %1, %2, 0 offen sc1" : "=v"(p.poll) : "v"(c.vflag), "s"(c.sw) : "memory");
  SB0();
  slots<R, S, j0, 0, n>(c, p);
}
template <int R, int S, int E> DI void steps(Ctx& c, P1& p) { if constexpr (S < E) { step<R, S>(c, p); steps<R, S + 1, E>(c, p); } }

struct P2 { Seg s[2]; u32 ob[2]; bf16x8 f[8]; f32x16 c[2]; u32x4 h2[14], of[8]; };
template <int J> DI void rd2(P2& q) {
  if constexpr (J < 56) {
    constexpr u32 o = spos(w2s_(J / 4)) + ((J / 2) & 1) * 2048;
    q.f[J % 8] = tr_frag(sa(q.s[J & 1], o), sa(q.s[J & 1], o + 1024));
  } else if constexpr (J < 72) {
    constexpr int i = (J - 56) / 2, hh = i >> 1;
    constexpr u32 o = (28 * hh - 4 * (hh & 1) + 16 * (i & 1)) * 128;
    q.f[J % 8] = tr_frag(q.ob[J & 1] + o, q.ob[J & 1] + o + 1024);
  }
}
template <int J> DI void mm2(P2& q, const P1& p) {
  q.c[J & 1] = mfma(q.f[J % 8], as_frag(J < 28 ? p.h1[J / 2] : J < 56 ? q.h2[(J - 28) / 2] : q.of[(J - 56) / 2]), q.c[J & 1]);
}
template <int A, int B, class F> DI void run2(P2& q, const P1& p, F&& extra) {
  if constexpr (A < B) {
    mm2<A>(q, p); rd2<(A + 4) ^ 1>(q);
    extra(std::integral_constant<int, A>{});
    SB0();
    run2<A + 1, B>(q, p, extra);
  } else { PIN2(q.c[0], q.c[1]); }
}

template <int R> DI void ffn_wg(Ctx& c, P1& p, u64 epoch) {
  const u32 l = c.lane;
  const int tg2 = c.tg & 1, ks = c.tg >> 1;
  issue_range<R, 0, PRO>(c);
  p.TG = 2 * (R == 1 ? c.kh : 1 - c.kh) + tg2;
  p.hbase = WS_H + (u32)(((c.t * 4 + p.TG) * 112 + 56 * c.kh + 2 * ks) * 1024);
  c.tag = epoch + 1;
  asm volatile("" : "+s"(c.tag), "+s"(p.hbase), "+v"(c.vflag), "+v"(c.vtouch), "+v"(c.awp) :: "memory");
  p.g1 = p.u1 = p.a0 = p.a1 = p.g2 = p.u2 = (f32x16){};
  steps<R, 0, 22>(c, p);
  mm_range<R, F2_(R) + 52, F2_(R) + 56>(p);
  SB0();
  STAMP(19);
  h_store<0>(c, p.y, p.u2, 1);
  h_store<1>(c, p.y, p.u2, 1);
  if constexpr (R == 1) {
    chk<0, 4>(p.kv, [&] { ld_1k<0, 4>(c, p.kv, WS_KVR + (u32)c.hd * 4096); });
    STAMP(22);
    o_publish(c, p.kv, p.ph, p.gq);
  }
  P2 q;
  const u32 qq = (l & 15) >> 2, pp_ = l & 3, blk = (l >> 4) & 1, h = l >> 5;
  const u32 b0 = (4 * h + qq) * 128 + ((qq >> 1) << 6) + 32 * blk + 8 * pp_;
  q.s[0] = mkseg(b0 + ks * 4096); q.s[1] = mkseg((b0 ^ 64) + ks * 4096);
  q.ob[0] = b0 + ks * (112 * 128); q.ob[1] = (b0 ^ 64) + ks * (112 * 128);
  PIN2(q.ob[0], q.ob[1]);
  q.c[0] = q.c[1] = (f32x16){};
  rd2<1>(q); rd2<0>(q); rd2<3>(q); rd2<2>(q);
  auto none = [](auto) {};
  const u32 obase = WS_O + (u32)(((c.t * 4 + p.TG) * 16 + 8 * c.kh + 4 * ks) * 2048);
  const u32 l16h = c.l16 + 4096;
  chk<0, 14>(p.h1, [&] { ld_h<0, 14>(c, p.h1, c.l16, p.hbase); });
  STAMP(20);
  constexpr int JC = R ? 8 : 24;
  run2<0, JC>(q, p, none);
  wait_bar_v<2 + 2 * R>();
  STAMP(21);
  if (TOUCH2) touch(c, c.oh + 4096);
  run2<JC, 28>(q, p, [&](auto J) { if constexpr (TOUCH2 && R == 1 && J.value == 16) touch(c, c.oo); });
  STAMP(23);
  if constexpr (R == 0) __builtin_amdgcn_s_sleep(PSLEEP);
  ld_h<0, 14>(c, q.h2, l16h, p.hbase);
  STAMP(24);
  chk<0, 7>(q.h2, [&] { ld_h<0, 7>(c, q.h2, l16h, p.hbase); });
  STAMP(25);
  run2<28, 42>(q, p, [&](auto J) { if constexpr (J.value >= 34) ld_1k<J.value - 34, J.value - 33>(c, q.of, obase); });
  chk<7, 14>(q.h2, [&] { ld_h<7, 14>(c, q.h2, l16h, p.hbase); });
  STAMP(26);
  run2<42, 56>(q, p, none);
  const float om = 1.0f - p.gt;
#pragma unroll
  for (int i = 0; i < 16; ++i) { q.c[0][i] *= om; q.c[1][i] *= om; }
  PIN2(q.c[0], q.c[1]);
  STAMP(27);
  chk<0, 8>(q.of, [&] { ld_1k<0, 8>(c, q.of, obase); });
  STAMP(28);
  u32x4 pp[4];
  const u32 so = c.op ^ (R ? 8 * 16384 : 0);
  run2<56, 72>(q, p, [&](auto J) { if constexpr (R == 1 && J.value >= 68) ld_1k<J.value - 68, J.value - 67>(c, pp, so); });
  f32x16 &c0 = q.c[0], &c1 = q.c[1];
  STAMP(29);
  if constexpr (R == 0) {
    st_dev(c, sanit(pack8<0>(c0)), c.l16, so);
    st_dev(c, sanit(pack8<1>(c0)), c.l16 + 1024, so);
    st_dev(c, sanit(pack8<0>(c1)), c.l16 + 2048, so);
    st_dev(c, sanit(pack8<1>(c1)), c.l16 + 3072, so);
    STAMP(30);
  } else {
    chk<0, 4>(pp, [&] { ld_1k<0, 4>(c, pp, so); });
    STAMP(30);
#pragma unroll
    for (int d = 0; d < 4; ++d) {
      c0[2 * d] += blo(pp[0][d]); c0[2 * d + 1] += bhi(pp[0][d]); c0[8 + 2 * d] += blo(pp[1][d]); c0[9 + 2 * d] += bhi(pp[1][d]);
      c1[2 * d] += blo(pp[2][d]); c1[2 * d + 1] += bhi(pp[2][d]); c1[8 + 2 * d] += blo(pp[3][d]); c1[9 + 2 * d] += bhi(pp[3][d]);
    }
    bar_l();
    const u32 wa = (u32)(ks * 64 + tg2 * 32 + (l & 31)) * 272 + h * 16;
#pragma unroll
    for (int gq = 0; gq < 4; ++gq) {
      f32x4 v0 = {c0[4 * gq], c0[4 * gq + 1], c0[4 * gq + 2], c0[4 * gq + 3]}, v1 = {c1[4 * gq], c1[4 * gq + 1], c1[4 * gq + 2], c1[4 * gq + 3]};
      *(LAS f32x4*)(wa + gq * 32) = v0;
      *(LAS f32x4*)(wa + 128 + gq * 32) = v1;
    }
    bar_l();
#pragma unroll
    for (int it = 0; it < 2; ++it) {
      const u32 tok = c.tg * 16 + it * 8 + (l >> 3), ra = tok * 272 + (l & 7) * 32;
      f32x4 x0 = *(LAS f32x4*)(ra), x1 = *(LAS f32x4*)(ra + 16), y0 = *(LAS f32x4*)(ra + 64 * 272), y1 = *(LAS f32x4*)(ra + 64 * 272 + 16);
      x0 += y0; x1 += y1;
      u32x4 o = {pk(x0[0], x0[1]), pk(x0[2], x0[3]), pk(x1[0], x1[1]), pk(x1[2], x1[3])};
      *(u32x4*)(c.out + (size_t)(c.t * 128 + c.kh * 64 + tok) * 896 + c.jj * 128 + (l & 7) * 16) = o;
    }
    if (__builtin_amdgcn_workgroup_id_x() == 0 && c.w == 4) {
      u32x2 v = {(u32)c.tag, (u32)(c.tag >> 32)};
      __builtin_amdgcn_raw_buffer_store_b64(v, c.sw, l == 0 ? 0u : OOB, 0, 16);
    }
    STAMP(31);
  }
}

template <int R, int A0, int A, int B, int ST> DI void pipe(P1& p) {
  if constexpr (A - 3 * ST < B) {
    if constexpr (A - 3 * ST >= A0) mm_item<R, A - 3 * ST>(p);
    if constexpr (A < B) rd_item<R, A>(p);
    SB0();
    pipe<R, A0, A + ST, B, ST>(p);
  }
}
constexpr u32 sxpos(int ch) { return spos(ch < 2 ? 2 * ch - 2 : 3 + 2 * ch); }
constexpr u32 NB1 = 8, NB2 = 16;
template <int N, int E> DI void spare_x(const Ctx& c) {
  if constexpr (N < E) { set_m0<sxpos(N >> 1) + (N & 1) * UNIT>(c); asm volatile("s_nop 0"); dma<0>((N & 1) ? c.vx2 : c.vx, c.sx, c.rot[N >> 1] * 128); spare_x<N + 1, E>(c); }
}

DI void bulk_mark(const Ctx& c, u32 i) {
  const u32x4 ones = {~0u, ~0u, ~0u, ~0u};
  u32 o;
  if (i < 112) o = WS_P + (c.t * 16 + 8 * c.kh) * 16384 + i * 1024;
  else o = WS_O + ((c.t * 4 + ((i - 112) >> 4)) * 16 + 8 * c.kh) * 2048 + ((i - 112) & 15) * 1024;
  st_dev(c, ones, c.l16, o);
}
template <int R> DI void spare_wg(Ctx& c, P1& p, u64 epoch) {
  const u32 l = c.lane;
  spare_x<0, 4>(c);
  set_m0<spos(2)>(c); asm volatile("s_nop 0"); dma<0>(c.l16, mk(c.gw, 896), 0);
  const u32 okvr = WS_KVR + (u32)(c.t * 4096 + (2 * c.kh + (c.w & 1)) * 1024);
  { const u32x4 ones = {~0u, ~0u, ~0u, ~0u}; st_dev(c, ones, R == 0 || c.tg < 2 ? c.l16 : OOB, R == 0 ? c.okvp : okvr); }
#pragma unroll 1
  for (u32 i = 0; i < NB1; ++i) bulk_mark(c, 176 - 8 * NB1 + i * 8 + c.w);
  spare_x<4, 14>(c);
  issue_range<R, 21, 25>(c);
  c.tag = epoch + 1;
  asm volatile("" : "+s"(c.tag) :: "memory");
  wait_bar<4>();
#pragma unroll
  for (int s = 0; s < 28; ++s) p.xf[s] = as_frag(lds_rd128((p.ax ^ (u32)(32 * (s & 3))) + sxpos(s >> 2)));
  bar_l();
  STAMP(1);
  issue_range<R, 25, 29>(c);
  issue_range<R, 30, 31>(c);
  issue_range<R, 33, 34>(c);
  issue_range<R, 36, 37>(c);
  f32x16 ga = {};
#pragma unroll
  for (int s = 14 * R; s < 14 * R + 14; ++s) ga = mfma(as_frag(lds_rd128(spos(2) + c.rot[s >> 2] * 128 + (s & 3) * 32 + (l >> 5) * 16)), p.xf[s], ga);
  PIN1(ga);
  p.a0 = p.a1 = (f32x16){};
  float g1 = 0.f;
  if constexpr (R == 0) *(LAS float*)(SCR + c.tg * 4096 + l * 4) = ga[0];
  wait_bar<7>();
  if constexpr (R == 0) flag_publish(c);
  if constexpr (R == 1) {
    g1 = __builtin_amdgcn_rcpf(1.0f + __builtin_amdgcn_exp2f(-LOG2E * (ga[0] + *(LAS float*)(SCR + c.tg * 4096 + l * 4))));
    __builtin_amdgcn_raw_buffer_store_b32(f2u(g1), c.sw, l < 32 ? l * 4 : OOB, WS_G + (u32)(c.kh * 2048 + c.t * 128 + c.tg * 32) * 4, 16);
  }
  pipe<R, 28, 28, 44, 1>(p);
  wait_bar_v<1>();
  STAMP(2);
  load_wphi(c, p.wp);
  pipe<R, 44, 44, 56, 1>(p);
  if constexpr (R == 0) {
    fm_slices<0, 24>(p);
    phi_image<0>(c, p); phi_image<1>(c, p);
  } else {
    v_handoff<spos(11), 0>(c, p.a1); v_handoff<spos(11), 1>(c, p.a1);
#pragma unroll 1
    for (u32 i = 0; i < NB2; ++i) bulk_mark(c, i * 4 + c.tg);
  }
  bar_l();
  if constexpr (R == 0) {
    if (c.w >= 2) return;
    kv_stage<spos(11), 0>(c, p); kv_stage<spos(11), 1>(c, p); kv_stage<spos(11), 2>(c, p); kv_stage<spos(11), 3>(c, p); kv_stage<spos(11), 4>(c, p);
    kv_stage<spos(11), 6>(c, p);
    STAMP(3);
    flag_spin(c, __builtin_amdgcn_raw_buffer_load_b64(c.sw, c.vflag, 0, 16));
    STAMP(4);
    u32x4 v[16];
    const u32 ro = WS_KVP + (u32)(c.t * 65536 + (2 * c.kh + c.w) * 1024);
    ld_lin<0, 16>(c, v, ro, 4096);
    chk<0, 16>(v, [&] { ld_lin<0, 16>(c, v, ro, 4096); });
    STAMP(5);
    float s[8], s2[8];
    set8(s, v[0]); set8(s2, v[1]);
#pragma unroll
    for (int i = 2; i < 16; i += 2) { add8(s, v[i]); add8(s2, v[i + 1]); }
#pragma unroll
    for (int i = 0; i < 8; ++i) s[i] += s2[i];
    st_dev(c, sanit(pack8<0>(s)), c.l16, okvr);
    STAMP(6);
  } else {
#pragma unroll 1
    for (u32 i = NB2; i < (176 - 8 * NB1) / 4; ++i) bulk_mark(c, i * 4 + c.tg);
    pipe<R, 57, 57, 112, 2>(p); PIN1(p.a0);
    asm volatile("s_waitcnt vmcnt(0)" ::: "memory");
    { u32x2 v = {(u32)c.tag, (u32)(c.tag >> 32)};
      __builtin_amdgcn_raw_buffer_store_b64(v, c.sw, l == 0 ? 0u : OOB, WS_FLAG + (u32)(c.t * 16 + c.hd) * 128 + c.w * 8, 16); }
    STAMP(3);
    fm_slices<0, 24>(p);
    flag_spin(c, __builtin_amdgcn_raw_buffer_load_b64(c.sw, c.vflag, 0, 16));
    u32x4 v[4];
    const u32 kvo = WS_KVR + (u32)c.hd * 4096;
    ld_1k<0, 4>(c, v, kvo);
    chk<0, 4>(v, [&] { __builtin_amdgcn_s_sleep(4); ld_1k<0, 4>(c, v, kvo); });
    o_publish(c, v, p.ph, g1);
    STAMP(7);
  }
}

extern "C" __global__ __launch_bounds__(512, 2) void fused_kernel(const char* x, const char* wqkv, const char* wphi, const char* wo, const char* wg,
                                                                   const char* wu, const char* wd, const char* gw, char* out, char* ws) {
  Ctx c;
  const int bid = __builtin_amdgcn_workgroup_id_x(), xcd = bid & 7, idx = bid >> 3;
  const u32 tid = __builtin_amdgcn_workitem_id_x();
  c.gw = gw; c.out = out; c.ws = ws;
  c.t = 4 * (xcd >> 1) + (idx & 3); c.kh = xcd & 1; c.jj = idx >> 2; c.hd = 8 * c.kh + c.jj; c.j = 7 * c.kh + c.jj;
  c.w = __builtin_amdgcn_readfirstlane(tid >> 6); c.tg = c.w & 3; c.r = c.w >> 2;
  c.lane = tid & 63; c.l16 = c.lane * 16; c.m0w = c.w * 1024;
  const u32 l = c.lane, w = c.w;
  STAMP(0);
  const u64 epoch = *(const __attribute__((address_space(4))) u64*)ws;
  {
    u32 c0 = (u32)(c.jj + 2 * (idx & 3) + (xcd >> 1)) % 7u;
#pragma unroll
    for (int i = 0; i < 7; ++i) { c.rot[i] = c0; c0 = c0 == 6 ? 0 : c0 + 1; }
  }
  const u32 row8 = 8 * w + (l >> 3), sw2 = ((l & 7) ^ (((l >> 4) & 1) << 2)) << 4;
  c.vx = row8 * 896 + (((l & 7) ^ ((4 * w + (l >> 4)) & 7)) << 4); c.vx2 = c.vx + 64 * 896;
  c.vw = row8 * 3584 + sw2;
  c.v2 = row8 * 896 + sw2; c.v2b = c.v2 + 64 * 896;
  c.vq = (16 * c.tg + (l >> 2)) * 2688 + (l & 3) * 16; c.vkv = c.vq + 896 * (1 + c.r);
  const u32 win = 56 * c.hd - 8 * (c.hd & 1);
  c.sx = mk(x + c.t * 114688, 114688);
  c.sq = mk(wqkv + win, 448 * 2688 - win);
  c.sp = mk(wphi + c.hd * 1568, 1568);
  c.sw = mk(ws, WS_END);
  c.oh = WS_H + (u32)(((c.t * 4 + c.tg) * 112 + 8 * c.j + 2 * c.r) * 1024);
  c.oo = WS_O + (u32)(((c.t * 4 + c.tg) * 16 + c.hd) * 2048);
  c.op = WS_P + (u32)((c.t * 16 + c.hd) * 16384 + c.tg * 4096);
  c.okvp = WS_KVP + (u32)((c.hd * 16 + c.t) * 4096 + c.tg * 1024);
  static_assert(WS_P % (16 * 16384) == 0, "the partner's slot is op ^ (8 * 16384)");
  asm volatile("" : "+s"(c.oh), "+s"(c.oo), "+s"(c.op), "+s"(c.okvp));
  c.vflag = flag_off(c, c.jj == 7); c.vtouch = l < 16 ? l * 128 : OOB;
  { const u32 rho = l & 31, f = rho < 26 ? rho : ((rho == 28 || rho == 29) ? rho - 2 : 0);
    c.awp = upos(28) + 5 * 1024 + 2 * (28 * (4 * (l >> 5) - 4 * (c.hd & 1)) + f); }
  P1 p;
  {
    const u32 q = (l & 15) >> 2, pp = l & 3, blk = (l >> 4) & 1, h = l >> 5, r = l & 31;
    p.aw = mkseg((8 * h + q) * 128 + ((64 * c.r) ^ ((q >> 1) << 6)) + 32 * blk + 8 * pp);
    p.aq = mkseg((8 * h + q) * 64 + 32 * blk + 8 * pp);
    p.ax = (32 * c.tg + r) * 128 + ((h ^ ((r >> 1) & 7)) << 4);
  }
  if (c.jj < 7) {
    c.sg = mk(wg + c.j * 256, 448 * 3584 - c.j * 256);
    c.su = mk(wu + c.j * 256, 448 * 3584 - c.j * 256);
    c.sd = mk(wd + c.kh * 896 * 896 + c.jj * 128, 896 * 896 - c.jj * 128);
    c.so = mk(wo + c.kh * 224 * 896 + c.jj * 128, 224 * 896 - c.jj * 128);
    if (c.r == 0) { ffn_wg<0>(c, p, epoch); __builtin_amdgcn_endpgm(); }
    ffn_wg<1>(c, p, epoch);
    __builtin_amdgcn_endpgm();
  }
  {
    u32 lo; u64 pc;
    asm volatile("s_getpc_b64 %0\n.Lpf_here:\n\ts_mov_b32 %1, .Lpf_here-fused_kernel" : "=s"(pc), "=s"(lo));
    asm volatile("s_add_u32 m0, %0, %1\n\ts_nop 0\n\tbuffer_load_dword %2, %3, 0 offen lds"
                 :: "s"(c.m0w), "i"(spos(17)), "v"((w * 64 + l) * 128), "s"(mk((const char*)(pc - lo), CODE_BYTES)) : "memory", "scc");
  }
  if (c.r == 0) { spare_wg<0>(c, p, epoch); __builtin_amdgcn_endpgm(); }
  spare_wg<1>(c, p, epoch);
}

#if defined(__HIP_DEVICE_COMPILE__)
#pragma clang attribute pop
#endif

__global__ void amp_k(const unsigned long long* st){
  unsigned tnow=(unsigned)__builtin_amdgcn_s_memrealtime(); asm volatile("":"+s"(tnow)::"memory");
  unsigned mn=~0u;
  for(int i=threadIdx.x;i<2048;i+=64){ unsigned a=(unsigned)st[i*32]; mn=a<mn?a:mn; }
  for(int o=32;o;o>>=1){ unsigned b=__shfl_xor(mn,o); mn=b<mn?b:mn; }
  unsigned sum=0,mx=0,cnt=0;
  for(int b=threadIdx.x;b<256;b+=64){ bool sp=(b>>5)==7; if(sp!=('e'=='s')) continue; unsigned d=(unsigned)st[(b*8+4)*32+PROBE_K]-mn; if(d<100000){ sum+=d; mx=d>mx?d:mx; cnt++; } }
  for(int o=32;o;o>>=1){ sum+=__shfl_xor(sum,o); cnt+=__shfl_xor(cnt,o); unsigned b=__shfl_xor(mx,o); mx=b>mx?b:mx; }
  unsigned long long m = 'e'=='z' ? 0 : 'e'=='e' ? 0ull : 'e'=='m' ? (unsigned long long)mx*100 : (unsigned long long)sum*100/(cnt?cnt:1);
  unsigned long long t0=__builtin_amdgcn_s_memrealtime();
  while(__builtin_amdgcn_s_memrealtime()-t0<m) __builtin_amdgcn_s_sleep(4);
}
__global__ __launch_bounds__(512,2) void pre_k(unsigned long long* st){ unsigned long long t=__builtin_amdgcn_s_memrealtime(); asm volatile("v_mov_b32 v250, 0":::"v250"); if(threadIdx.x==0) st[70000+blockIdx.x]=t; }
extern "C" void kernel_launch(void* const* d_in, const int* in_sizes, int n_in, void* d_out, int out_size, void* d_ws, size_t ws_size,
                              hipStream_t stream) {
  (void)hipFuncSetAttribute((const void*)fused_kernel, hipFuncAttributeMaxDynamicSharedMemorySize, LDS_BYTES);
  (void)hipFuncSetAttribute((const void*)pre_k, hipFuncAttributeMaxDynamicSharedMemorySize, LDS_BYTES);
  pre_k<<<256,512,163840,stream>>>((unsigned long long*)((char*)d_ws + WS_STAMP));
  fused_kernel<<<256, 512, LDS_BYTES, stream>>>((const char*)d_in[0], (const char*)d_in[1], (const char*)d_in[2], (const char*)d_in[3],
                                                (const char*)d_in[5], (const char*)d_in[6], (const char*)d_in[7], (const char*)d_in[8],
                                                (char*)d_out, (char*)d_ws);
  amp_k<<<1,64,0,stream>>>((const unsigned long long*)((char*)d_ws + WS_STAMP));
}
```

```cpp
#include <hip/hip_runtime.h>
#include <hip/hip_bf16.h>
#include <type_traits>
#pragma clang diagnostic ignored "-Wint-to-pointer-cast"

typedef unsigned u32;
typedef unsigned long long u64;
typedef u32 u32x4 __attribute__((ext_vector_type(4)));
typedef u32 u32x2 __attribute__((ext_vector_type(2)));
typedef short s16x4 __attribute__((ext_vector_type(4)));
typedef __bf16 bf16x8 __attribute__((ext_vector_type(8)));
typedef __bf16 bf16x2 __attribute__((ext_vector_type(2)));
typedef float f32x16 __attribute__((ext_vector_type(16)));
typedef float f32x4 __attribute__((ext_vector_type(4)));
typedef float f32x2 __attribute__((ext_vector_type(2)));
typedef __amdgpu_buffer_rsrc_t rsrc_t;
#define DI __device__ __forceinline__
#define LAS __attribute__((address_space(3)))
#define SB0() __builtin_amdgcn_sched_barrier(0)
#define PIN1(a) asm volatile("" : "+v"(a))
#define PIN2(a, b) asm volatile("" : "+v"(a), "+v"(b))

constexpr u32 WS_FLAG = 4096;
constexpr u32 WS_G    = WS_FLAG + 256 * 128;
constexpr u32 WS_KVR  = 65536;
constexpr u32 WS_KVP  = 262144;
constexpr u32 WS_O    = WS_KVP + 256 * 4096;
constexpr u32 WS_P    = WS_O + 64 * 32 * 1024;
constexpr u32 WS_H    = WS_P + 256 * 16384;
constexpr u32 WS_WA   = WS_H + 64 * 112 * 1024;
constexpr u32 WS_END  = WS_WA + 16 * 3 * 28672;

constexpr int UNIT = 8192, RING = 18;
constexpr u32 SCR = 0, RBASE = 2 * UNIT;
constexpr int LDS_BYTES = 163840;
constexpr u32 OOB = 0x80000000u;
constexpr u32 CODE_BYTES = 40960;
#define ABL 0
#define MARKV 0
#define TOUCH2 0
#define PSLEEP 4
#define PRIO 1
#define XP 24
#define OPOS 192
#define KVPOS 2
#define PF 0
#define H1Q 6
#define POLL2 166
#define YP 10
#define XQ 14
#define POLL0 6
#define TOUCH1 0
#define PROV 6
constexpr u32 SPIN_MAX = ABL ? 0 : 1u << 16;
constexpr float LOG2E = 1.4426950408889634f;

#if defined(__HIP_DEVICE_COMPILE__)
#pragma clang attribute push(__attribute__((target("no-packed-fp32-ops"))), apply_to = function)
#endif
constexpr u32 spos(int k) { return RBASE + k * UNIT; }
constexpr int w2c_(int i) { constexpr int t[18] = {16, 17, 0, 1, 2, 3, 4, 5, 6, 7, 8, 9, 10, 11, 12, 13, 14, 15}; return t[i]; }
constexpr int w2s_(int c) { constexpr int t[18] = {2, 3, 4, 5, 6, 7, 8, 9, 10, 12, 11, 13, 14, 15, -2, -1, 0, 1}; return t[c]; }
constexpr int uslot(int n) {
  constexpr int t[22] = {-1, 14, 15, 16, 17, 0, 1, 2, 3, 4, 5, 6, 7, 8, 9, 10, 11, 13, 14, 15, 16, 17};
  return n < 32 ? n % RING : n < 54 ? t[n - 32] : w2s_(w2c_(n - 54));
}
constexpr u32 upos(int n) { return spos(uslot(n)); }
constexpr u32 VBUF = RBASE + 12 * UNIT;
constexpr int imin(int a, int b) { return a < b ? a : b; }

struct Ctx {
  const char* gw;
  char *out, *ws;
  int t, kh, jj, hd, j, w, tg, r;
  u32 lane, l16, m0w;
  u32 vx, vx2, vw, vq, vkv, v2, v2b;
  rsrc_t sx, sg, su, sq, sp, sd, so, sw;
  u64 tag;
  u32 rot[7];
  u32 oh, oo, op, okvp;
  u32 oflag, og;
  u32 vrp, vflag0;
  u32 vflag, vtouch, awp, vone;
};

DI rsrc_t mk(const void* p, u32 bytes) { return __builtin_amdgcn_make_buffer_rsrc((void*)p, 0, bytes, 0x00020000); }
template <u32 L> DI void set_m0(const Ctx& c) { if (!(ABL & 1)) asm volatile("s_add_u32 m0, %0, %1" :: "s"(c.m0w), "i"(L) : "memory", "scc"); }
DI void dma_dev(u32 voff, rsrc_t rs, u32 soff) {
  if (!(ABL & 1)) asm volatile("buffer_load_dwordx4 %0, %1, %2 offen sc1 lds" :: "v"(voff), "s"(rs), "s"(soff) : "memory");
}
template <u32 OFF> DI void dma(u32 voff, rsrc_t rs, u32 soff) {
  if (!(ABL & 1)) asm volatile("buffer_load_dwordx4 %0, %1, %2 offen offset:%3 lds" :: "v"(voff), "s"(rs), "s"(soff), "i"(OFF) : "memory");
}
template <int N> DI void wait_bar() { asm volatile("s_waitcnt vmcnt(%0) lgkmcnt(0)\n\ts_barrier" :: "n"(N) : "memory"); }
template <int N> DI void wait_bar_v() { asm volatile("s_waitcnt vmcnt(%0)\n\ts_barrier" :: "n"(N) : "memory"); }
DI void bar_l() { asm volatile("s_waitcnt lgkmcnt(0)\n\ts_barrier" ::: "memory"); }

constexpr u32 ioff_(int n) { return n >= 36 && n < 54 && !(n < 47 && n % 3 == 0) ? 128 : 0; }
template <int N> DI void unit_m0(const Ctx& c) { set_m0<upos(N) - ioff_(N)>(c); }
template <int R, int N> DI void unit_go(const Ctx& c) {
  if constexpr (N < 21) {
    if constexpr (N % 3 < 2) dma<0>(N % 3 ? c.vx2 : c.vx, c.sx, c.rot[N / 3] * 128);
    else dma<0>(c.vw, c.sg, c.rot[N / 3] * (64 * 3584));
  } else if constexpr (N >= 25 && N < 32) dma_dev(c.vkv, c.sw, c.rot[N - 25] * 4096);
  else if constexpr (N == 32) dma<0>(c.l16, c.sp, c.w == 5 ? 0u : c.w == 6 ? 1024u : 0x40000000u);
  else if constexpr (N < 36) dma<0>(c.vw, c.su, c.rot[N < 25 ? N - 21 : N - 29] * (64 * 3584));
  else if constexpr (N < 47) {
    if constexpr (N % 3 == 0) dma_dev(c.vq, c.sw, N == 45 && R == 1 ? 0x40000000u : c.rot[(2 * (N - 36) / 3 + R) % 7] * 4096);
    else dma<128>(c.vw, c.sg, c.rot[2 * ((N - 36) / 3) + N % 3 - 1] * (64 * 3584));
  } else if constexpr (N < 54) dma<128>(c.vw, c.su, c.rot[N - 47] * (64 * 3584));
  else {
    constexpr int k = w2c_(N - 54);
    if constexpr (k < 14) dma<0>(k < 7 ? c.v2 : c.v2b, c.sd, c.rot[k % 7] * (128 * 896));
    else dma<0>((k & 1) ? c.v2b : c.v2, c.so, k < 16 ? 0u : 128u * 896u);
  }
}
template <int N, int H> DI void unit_half(const Ctx& c) {
  set_m0<upos(N) + H * 4096>(c);
  constexpr int k = w2c_(N - 54);
  const u32 so = (k < 14 ? c.rot[k % 7] * (128 * 896) : k < 16 ? 0u : 128u * 896u) + H * (32 * 896);
  asm volatile("s_nop 0");
  dma<0>((k < 14 ? k >= 7 : (k & 1)) ? c.v2b : c.v2, k < 14 ? c.sd : c.so, so);
}
template <int R, int A, int B> DI void issue_range(const Ctx& c) {
  if constexpr (A < B) { unit_m0<A>(c); asm volatile("s_nop 0"); unit_go<R, A>(c); issue_range<R, A + 1, B>(c); }
}

struct Seg { u32 b[3]; };
DI Seg mkseg(u32 a) { Seg s; s.b[0] = a; s.b[1] = a + 65536; s.b[2] = a + 131072; asm volatile("" : "+v"(s.b[0]), "+v"(s.b[1]), "+v"(s.b[2])); return s; }
DI u32 sa(const Seg& s, u32 off) { return s.b[off >> 16] + (off & 0xffffu); }
DI f32x16 mfma(bf16x8 a, bf16x8 b, f32x16 c) { return __builtin_amdgcn_mfma_f32_32x32x16_bf16(a, b, c, 0, 0, 0); }
DI bf16x8 as_frag(u32x4 v) { return __builtin_bit_cast(bf16x8, v); }
DI bf16x8 tr_frag(u32 a0, u32 a1) {
  s16x4 lo = __builtin_amdgcn_ds_read_tr16_b64_v4i16((LAS s16x4*)a0);
  s16x4 hi = __builtin_amdgcn_ds_read_tr16_b64_v4i16((LAS s16x4*)a1);
  return __builtin_bit_cast(bf16x8, __builtin_shufflevector(lo, hi, 0, 1, 2, 3, 4, 5, 6, 7));
}
DI u32x4 lds_rd128(u32 a) { return *(LAS u32x4*)a; }
DI void lds_wr128(u32 a, u32x4 v) { *(LAS u32x4*)a = v; }
DI u32 f2u(float f) { return __builtin_bit_cast(u32, f); }
DI float u2f(u32 u) { return __builtin_bit_cast(float, u); }
DI u32 pk(float a, float b) { f32x2 t = {a, b}; return __builtin_bit_cast(u32, __builtin_convertvector(t, bf16x2)); }
template <int S, class V> DI u32x4 pack8(const V& v) {
  u32x4 r = {pk(v[8 * S], v[8 * S + 1]), pk(v[8 * S + 2], v[8 * S + 3]), pk(v[8 * S + 4], v[8 * S + 5]), pk(v[8 * S + 6], v[8 * S + 7])};
  return r;
}
DI float blo(u32 d) { return u2f(d << 16); }
DI float bhi(u32 d) { return u2f(d & 0xffff0000u); }
DI u32 umax(u32 a, u32 b) { return a > b ? a : b; }
DI bool any_(bool p) { return __builtin_amdgcn_ballot_w64(p) != 0; }
DI u32x4 sanit(u32x4 v) { return __builtin_elementwise_min(v, (u32x4){0xfffffffeu, 0xfffffffeu, 0xfffffffeu, 0xfffffffeu}); }
DI void st_dev(const Ctx& c, u32x4 v, u32 voff, u32 soff) {
  if (ABL & 8) { asm volatile("" :: "v"(v)); return; }
  __builtin_amdgcn_raw_buffer_store_b128(v, c.sw, voff, soff, 16);
  asm volatile("s_nop 1" :: "v"(v) : "memory");
}
DI u32x4 ld_dev(const Ctx& c, u32 voff, u32 soff) {
  if (ABL & 16) { u32x4 z = {voff, soff, voff, soff}; asm volatile("" : "+v"(z)); return z; }
  return __builtin_amdgcn_raw_buffer_load_b128(c.sw, voff, soff, 16);
}
DI f32x2 xhalf(float v) { auto p = __builtin_amdgcn_permlane32_swap(f2u(v), f2u(v), false, false); f32x2 r = {u2f(p[0]), u2f(p[1])}; return r; }

#define STAMP(i)

struct P1 {
  Seg aw, aq; u32 ax, ka, kb, kc;
  bf16x8 xf[28], f[8], af[8], vf[8];
  f32x16 g1, u1, a0, a1, g2, u2, fp, k0, c[2];
  Seg s2[2]; u32 ob[2];
  u32x4 h2[14], of[8];
  u32x4 wp[2], ph[4], h1[14], kv[4];
  u32 wr[16];
  float y[16], e[14], sum, mxl, mnl;
  u32x2 poll;
  float gq, gt;
  u32 hbase; int TG;
};

DI void wphi_rd(const Ctx& c, P1& p) {
#pragma unroll
  for (int i = 0; i < 16; ++i) p.wr[i] = *(LAS unsigned short*)(c.awp + 56 * (16 * (i >> 3) + 8 * ((i >> 2) & 1) + (i & 3)));
}
DI void wphi_pack(P1& p) {
#pragma unroll
  for (int i = 0; i < 8; ++i) p.wp[i >> 2][i & 3] = p.wr[2 * i] | (p.wr[2 * i + 1] << 16);
  PIN2(p.wp[0], p.wp[1]);
}
template <int K> DI void fm_slice(P1& p) {
  if constexpr (K == 0) { p.fp = mfma(as_frag(p.wp[0]), as_frag(pack8<0>(p.a0)), (f32x16){}); }
  else if constexpr (K == 1) { p.fp = mfma(as_frag(p.wp[1]), as_frag(pack8<1>(p.a0)), p.fp); }
  else if constexpr (K == 2) { }
  else if constexpr (K == 3) {
    PIN1(p.fp);
    float mx = p.fp[0];
#pragma unroll
    for (int i = 1; i < 14; ++i) mx = __builtin_fmaxf(mx, p.fp[i]);
    p.mxl = mx; PIN1(p.mxl);
  } else if constexpr (K == 4) {
    float mn = p.fp[0];
#pragma unroll
    for (int i = 1; i < 14; ++i) mn = __builtin_fminf(mn, p.fp[i]);
    p.mnl = mn; PIN1(p.mnl);
  } else if constexpr (K == 5) {
    f32x2 t = xhalf(p.mxl); p.mxl = __builtin_fmaxf(t[0], t[1]) * LOG2E;
    t = xhalf(p.mnl); p.mnl = __builtin_fminf(t[0], t[1]) * LOG2E;
    PIN2(p.mxl, p.mnl);
  } else {
    constexpr int NEG = K >= 15, k = K - 6 - 9 * NEG;
    if constexpr (k < 5) {
#pragma unroll
      for (int i = 3 * k; i < imin(14, 3 * k + 3); ++i) {
        p.e[i] = __builtin_amdgcn_exp2f(NEG ? __builtin_fmaf(p.fp[i], -LOG2E, p.mnl) : __builtin_fmaf(p.fp[i], LOG2E, -p.mxl));
        PIN1(p.e[i]);
      }
      if constexpr (k == 1) p.sum = p.e[0] + p.e[1] + p.e[2];
      if constexpr (k > 1) p.sum += p.e[3 * k - 3] + p.e[3 * k - 2] + p.e[3 * k - 1];
      if constexpr (k > 0) PIN1(p.sum);
    } else if constexpr (k == 5) {
      f32x2 t = xhalf(p.sum + p.e[12] + p.e[13]); p.sum = __builtin_amdgcn_rcpf(t[0] + t[1]); PIN1(p.sum);
    } else if constexpr (k < 8) {
#pragma unroll
      for (int i = 7 * (k - 6); i < 7 * (k - 5); ++i) { p.e[i] *= p.sum; PIN1(p.e[i]); }
    } else {
      u32x4 o1 = {pk(p.e[8], p.e[9]), pk(p.e[10], p.e[11]), pk(p.e[12], p.e[13]), 0u};
      p.ph[2 * NEG] = pack8<0>(p.e); p.ph[2 * NEG + 1] = o1; PIN2(p.ph[2 * NEG], p.ph[2 * NEG + 1]);
    }
  }
}
template <int A, int B> DI void fm_slices(P1& p) { if constexpr (A < B) { fm_slice<A>(p); fm_slices<A + 1, B>(p); } }
template <u32 VB, int S> DI void v_handoff(const Ctx& c, const f32x16& av) {
  u32x4 vf = pack8<S>(av);
  const bool one = (c.lane & 31) == ((c.hd & 1) ? 0u : 28u);
#pragma unroll
  for (int d = 0; d < 4; ++d) vf[d] = one ? 0x3f803f80u : vf[d];
  lds_wr128(VB + c.tg * 2048 + c.l16 + S * 1024, vf);
}
template <int T> DI void phi_image(const Ctx& c, P1& p) {
  const u32 tok = c.lane & 31, h = c.lane >> 5;
  const u32 ft = (((tok >> 1) & 1) << 3) | (tok & 1) | (((tok >> 2) & 3) << 1);
  const u32 wa = SCR + c.tg * 4096 + tok * 128 + ((h ^ ft) << 3);
#pragma unroll
  for (int g = 0; g < 4; ++g) {
    u32x2 v = {p.ph[2 * T + (g >> 1)][2 * (g & 1)], p.ph[2 * T + (g >> 1)][2 * (g & 1) + 1]};
    *(LAS u32x2*)(wa ^ (u32)((8 * T + 2 * g) << 3)) = v;
  }
}
template <u32 VB, int K> DI void kv_stage(const Ctx& c, P1& p) {
  if constexpr (K >= 2 && K <= 5) {
    p.k0 = mfma(p.af[2 * K - 4], p.vf[2 * K - 4], K == 2 ? (f32x16){} : p.k0);
    p.k0 = mfma(p.af[2 * K - 3], p.vf[2 * K - 3], p.k0);
  }
  if constexpr (K == 0) {
    const u32 l = c.lane, h = l >> 5, q = (l & 15) >> 2, pp = l & 3, blk = (l >> 4) & 1;
    p.ka = SCR + (4 * h + q) * 128 + (((4 * blk + pp) ^ (q & 1) ^ (h << 1) ^ ((q >> 1) << 3) ^ ((c.tg & 1) << 3)) << 3);
    p.kb = p.ka ^ 32u; p.kc = VB + c.l16;
    asm volatile("" : "+v"(p.ka), "+v"(p.kb), "+v"(p.kc));
  }
  if constexpr (K < 4) {
#pragma unroll
    for (int s = 0; s < 2; ++s) {
      p.af[2 * K + s] = tr_frag(p.ka + K * 4096 + s * 2048, p.kb + K * 4096 + s * 2048 + 1024);
      p.vf[2 * K + s] = as_frag(lds_rd128(p.kc + K * 2048 + s * 1024));
    }
  }
  if constexpr (K == 7) {
    const u32 vo = c.tg < 2 ? c.l16 : OOB;
    st_dev(c, sanit(pack8<0>(p.k0)), vo, c.okvp + c.tg * 1024);
    st_dev(c, sanit(pack8<1>(p.k0)), vo + 1024, c.okvp + c.tg * 1024);
  }
}
DI void add8(float (&s)[8], u32x4 v) {
#pragma unroll
  for (int d = 0; d < 4; ++d) { s[2 * d] += blo(v[d]); s[2 * d + 1] += bhi(v[d]); }
}
DI void set8(float (&s)[8], u32x4 v) {
#pragma unroll
  for (int d = 0; d < 4; ++d) { s[2 * d] = blo(v[d]); s[2 * d + 1] = bhi(v[d]); }
}
template <int A, int B, int N> DI bool has_mark(const u32x4 (&v)[N]) {
  u32 m = 0;
#pragma unroll
  for (int i = A; i < B; ++i) m = umax(umax(m, v[i][0]), umax(v[i][1], umax(v[i][2], v[i][3])));
  return any_(m == 0xffffffffu);
}
template <int A, int B, int N, class F> DI void chk(u32x4 (&v)[N], F&& reload) {
  if (__builtin_expect(SPIN_MAX && has_mark<A, B>(v), 0)) {
    u32 spin = 0;
    do { asm volatile("" ::: "memory"); reload(); } while (has_mark<A, B>(v) && ++spin < SPIN_MAX);
  }
}
template <int A, int B, int N> DI void ld_lin(const Ctx& c, u32x4 (&v)[N], u32 soff, u32 stride) {
#pragma unroll
  for (int i = A; i < B; ++i) v[i] = ld_dev(c, c.l16, soff + i * stride);
}
template <int A, int B, int N> DI void ld_1k(const Ctx& c, u32x4 (&v)[N], u32 soff) {
#pragma unroll
  for (int i = A; i < B; ++i) v[i] = ld_dev(c, c.l16 + (i & 3) * 1024, soff + (i >> 2) * 4096);
}
template <int A, int B> DI void ld_h(const Ctx& c, u32x4 (&v)[14], u32 voff, u32 soff) {
#pragma unroll
  for (int i = A; i < B; ++i) v[i] = ld_dev(c, voff + (i & 1) * 1024, soff + c.rot[i >> 1] * 8192);
}
DI void o_publish(const Ctx& c, const u32x4 (&kv)[4], const u32x4 (&phq)[4], float g) {
  f32x16 n = {};
#pragma unroll
  for (int i = 0; i < 4; ++i) n = mfma(as_frag(kv[i]), as_frag(phq[i]), n);
  const bool odd = c.hd & 1;
  f32x2 t = xhalf(odd ? n[0] : n[12]);
  float sc = g * __builtin_amdgcn_rcpf((odd ? t[0] : t[1]) + 1e-6f);
#pragma unroll
  for (int i = 0; i < 16; ++i) n[i] *= sc;
  u32x4 f0 = sanit(pack8<0>(n)), f1 = sanit(pack8<1>(n));
  const bool z0 = odd && c.lane < 32, z1 = !odd && c.lane >= 32;
  f0[0] = z0 ? 0 : f0[0]; f0[1] = z0 ? 0 : f0[1];
  f1[2] = z1 ? 0 : f1[2]; f1[3] = z1 ? 0 : f1[3];
  st_dev(c, f0, c.l16, c.oo);
  st_dev(c, f1, c.l16 + 1024, c.oo);
}
DI u32 flag_off(const Ctx& c, bool spare) {
  const u32 i = c.lane & 15;
  u32 red = (c.hd * 16 + 7 + 8 * (i & 1)) << 7, sp = ((c.t * 16 + 7 + 8 * c.kh) << 7) + 8;
  u32 lo = spare ? (i * 16 + c.t) << 7 : ((c.t * 16 + (i < 7 ? 8 * c.kh + i : c.hd ^ 8)) << 7) + 8;
  asm volatile("" : "+v"(red), "+v"(sp), "+v"(lo));
  return WS_FLAG + ((c.lane & 16) ? red : (spare || i < 8) ? lo : sp);
}
DI bool flag_bad(const Ctx& c, u32x2 f) { return any_(f[0] != (u32)c.tag || f[1] != (u32)(c.tag >> 32)); }
DI void flag_spin(const Ctx& c, u32x2 f, u32 voff) {
  if (__builtin_expect(SPIN_MAX && flag_bad(c, f), 0)) {
    u32 spin = 0;
    do {
      __builtin_amdgcn_s_sleep(2);
      asm volatile("" ::: "memory");
      f = __builtin_amdgcn_raw_buffer_load_b64(c.sw, voff, 0, 16);
    } while (flag_bad(c, f) && ++spin < SPIN_MAX);
  }
  asm volatile("" ::: "memory");
}
DI void flag_publish(const Ctx& c, u32 word) {
  u32x2 v = {(u32)c.tag, (u32)(c.tag >> 32)};
  __builtin_amdgcn_raw_buffer_store_b64(v, c.sw, c.vone + word * 8, c.oflag, 16);
}
DI float ld_g(const Ctx& c, int tgroup) {
  return u2f(__builtin_amdgcn_raw_buffer_load_b32(c.sw, (c.lane & 31) * 4, c.og + tgroup * 128, 16));
}
template <u32 TOUCH_LDS> DI void touch(const Ctx& c, u32 soff) {
  if (ABL & 8) return;
  asm volatile("s_mov_b32 m0, %0\n\ts_nop 0\n\tbuffer_load_dword %1, %2, %3 offen sc1 lds"
               :: "i"(TOUCH_LDS), "v"(c.vtouch), "s"(c.sw), "s"(soff) : "memory");
}

template <u32 L> DI void pf_rows(u32 voff, rsrc_t rs, u32 soff) {
  if (ABL & 1) return;
  asm volatile("s_mov_b32 m0, %0\n\ts_nop 0\n\tbuffer_load_dword %1, %2, %3 offen lds" :: "i"(L), "v"(voff), "s"(rs), "s"(soff) : "memory");
}

constexpr int PRO = PROV;
constexpr int need_(int s) {
  return s < 0 ? 0 : s < 7 ? 3 * (s + 1) : s < 12 ? 23 + 2 * (s - 7) : s < 14 ? 34 + 2 * (s - 12) : s < 18 ? 39 + 3 * (s - 14) : s < 20 ? 50 + 2 * (s - 18) : 62;
}
constexpr int iss_(int s) {
  constexpr int t[15] = {POLL0 == 5 ? 28 : 25, POLL0 == 5 ? 32 : 30, POLL0 == 5 ? 36 : 35, POLL0 == 5 ? 40 : 39, 43, 46, 48, 49, 51, 53, 56, 59, 62, 65, 68};
  return s < 0 ? PRO : s < 6 ? imin(25, PRO + 3 * (s + 1)) : t[s - 6];
}
constexpr int ustep(int n) {
  if (n == 30 || n == 32) return 16;
  int s = 0; while (s < 20 && need_(s) <= n) ++s; return s;
}
constexpr bool ring_ok() {
  for (int s = -1; s <= 20; ++s)
    for (int n = s < 0 ? 0 : iss_(s - 1); n < iss_(s); ++n) {
      if (uslot(n) == -2 && s < 19) return false;
      for (int m = 0; m < n; ++m) if (uslot(m) == uslot(n) && ustep(m) + 2 > s) return false;
    }
  return true;
}
static_assert(ring_ok(), "ring schedule");
constexpr int S_FLAG0 = 3, S_POLL0 = POLL0, S_FLAG1 = 16, S_POLL = 17;
constexpr int oth_(int r, int s) {
  return s == 0 ? 2 - r : s == S_FLAG0 ? 1 - r : s == S_POLL0 ? 1 : s == 10 ? PF * (1 - r) : s == 11 ? 1 + PF * r : s == 12 ? 1 + PF : s == 14 ? 2 :
         s == 16 ? 3 + TOUCH1 - 3 * r : s == S_POLL ? 1 : s == 18 ? (r ? 2 : 5) : s == 19 || s == 20 ? (r ? 0 : 5) : 0;
}
constexpr int tot_(int r, int s) { return iss_(s) - iss_(s - 1) + oth_(r, s); }
constexpr int vm_(int r, int s) {
  int sp = -1; while (iss_(sp) < need_(s)) ++sp;
  int n = iss_(sp) - need_(s);
  for (int k = sp + 1; k < s; ++k) n += tot_(r, k);
  if (s == S_FLAG0) n = imin(n, tot_(r, 2) + tot_(r, 1) + 3);
  if (s == S_FLAG1) n = imin(n, tot_(r, 15));
  if (PF && s == 14) n = imin(n, tot_(r, 13));
  if (s == S_POLL0 + 1 || s == S_POLL + 1) n = imin(n, tot_(r, s - 1) - 1);
  return n;
}
constexpr int P2_(int r) { return r ? 168 : 140; }
constexpr int item0_(int r, int s) { return s < 7 ? 4 * s : (!r || s < 15) ? 8 * s - 28 : s < 18 ? 84 + 16 * (s - 14) : 8 * s; }
template <int R, int J> DI void rd_item(P1& p) {
  constexpr int U2 = P2_(R) - 28;
  if constexpr (J >= P2_(R)) {
    constexpr int k = J - P2_(R);
    if constexpr (k < 56) {
      constexpr u32 o = spos(w2s_(k / 4)) + ((k / 2) & 1) * 2048;
      p.f[J % 8] = tr_frag(sa(p.s2[k & 1], o), sa(p.s2[k & 1], o + 1024));
    } else if constexpr (k < 72) {
      constexpr int i = (k - 56) / 2, hh = i >> 1;
      constexpr u32 o = (28 * hh - 4 * (hh & 1) + 16 * (i & 1)) * 128;
      p.f[J % 8] = tr_frag(p.ob[k & 1] + o, p.ob[k & 1] + o + 1024);
    }
  } else if constexpr ((J >= 44 && J < 72) || (R == 1 && J >= 84 && J < U2 && (J & 1))) {
    constexpr int k = J < 72 ? J - 44 : (J - 84) / 2, ch = k / 4;
    constexpr u32 o = (J < 72 ? upos(25 + ch) + R * 4096 : upos(36 + 3 * (ch / 2)) + (ch & 1) * 4096) + (k % 4) * 1024;
    p.f[J % 8] = tr_frag(sa(p.aq, o), sa(p.aq, o + 256));
  } else {
    constexpr int k = J < 84 ? J : J < U2 ? (J - 84) / (1 + R) : J - U2, ch = k / 4;
    constexpr u32 o = upos(J < 28 ? 3 * ch + 2 : J < 44 ? 14 + ch : J < 84 ? 15 + ch : J < U2 ? 37 + 3 * (ch / 2) + (ch & 1) : 47 + ch) + (k % 4) * 2048;
    p.f[J % 8] = tr_frag(sa(p.aw, o), sa(p.aw, o + 512));
  }
}
template <int R, int J> DI void mm_item(P1& p) {
  constexpr int U2 = P2_(R) - 28;
  if constexpr (J < 0) {}
  else if constexpr (J >= P2_(R)) {
    constexpr int k = J - P2_(R);
    p.c[k & 1] = mfma(p.f[J % 8], as_frag(k < 28 ? p.h1[(k / 2) % 14] : k < 56 ? p.h2[(k / 2) % 14] : p.of[(k / 2 + 4) % 8]), p.c[k & 1]);
  } else if constexpr (J >= 44 && J < 72) {
    if constexpr (R == 1) p.a1 = mfma(p.xf[J - 44], p.f[J % 8], p.a1); else p.a0 = mfma(p.f[J % 8], p.xf[J - 44], p.a0);
  } else {
    f32x16& acc = J < 28 ? p.g1 : J < 84 ? p.u1 : J >= U2 ? p.u2 : (R == 1 && (J & 1)) ? p.a0 : p.g2;
    acc = mfma(p.f[J % 8], p.xf[J < 84 ? J % 28 : J < U2 ? (J - 84) / (1 + R) : J - U2], acc);
  }
}
template <int R, int A, int B> DI void mm_range(P1& p) { if constexpr (A < B) { mm_item<R, A>(p); mm_range<R, A + 1, B>(p); } }

constexpr float GK1 = -2.0f * 0.7978845608028654f * LOG2E, GK2 = GK1 * 0.044715f;
template <int OP> DI void gelu_op(float (&y)[16], const f32x16& g) {
  constexpr int st = OP % 4, i = OP / 4 - st;
  if constexpr (i >= 0 && i < 16) {
    if constexpr (st == 0) y[i] = g[i] * __builtin_fmaf(g[i] * g[i], GK2, GK1);
    else if constexpr (st == 1) y[i] = __builtin_amdgcn_exp2f(y[i]);
    else if constexpr (st == 2) y[i] = __builtin_amdgcn_rcpf(1.0f + y[i]);
    else y[i] = g[i] * y[i];
    PIN1(y[i]);
  }
}
constexpr int gcost(int op) { return op / 4 - op % 4 < 0 || op / 4 - op % 4 > 15 ? 0 : op % 4 == 0 ? 3 : op % 4 == 1 ? 2 : op % 4 == 2 ? 3 : 1; }
constexpr int gcum(int k, int n) { if (k >= n) return 76; int op = 0, cst = 0; while (cst * n < k * 144) cst += gcost(op++); return op; }
template <int A, int B> DI void gelu_ops(float (&y)[16], const f32x16& g) { if constexpr (A < B) { gelu_op<A>(y, g); gelu_ops<A + 1, B>(y, g); } }
template <int S> DI void h_store(const Ctx& c, const float (&y)[16], const f32x16& u, int blk) {
  float h[8];
#pragma unroll
  for (int i = 0; i < 8; ++i) h[i] = y[8 * S + i] * u[8 * S + i];
  st_dev(c, pack8<0>(h), c.l16 + S * 1024, c.oh + blk * 4096);
}
template <int R, int S, int K, int J> DI void slot_work(Ctx& c, P1& p) {
  if constexpr (S < 7) p.xf[J] = as_frag(lds_rd128((p.ax + upos(3 * S)) ^ (u32)(32 * K)));
  if constexpr (R == 0) {
    if constexpr (J >= 33 && J < 75) gelu_ops<gcum(J - 33, 42), gcum(J - 32, 42)>(p.y, p.g1);
    if constexpr (J == 70) wphi_rd(c, p);
    if constexpr (J == 76) wphi_pack(p);
    constexpr int F0 = J < 84 ? J - 77 : J < 89 ? 7 + (3 * (J - 84) + 1) / 2 : J < 91 ? 15 : 15 + (9 * (J - 91) + 7) / 8;
    constexpr int F1 = J + 1 < 84 ? J - 76 : J + 1 < 89 ? 7 + (3 * (J - 83) + 1) / 2 : J + 1 < 91 ? 15 : 15 + (9 * (J - 90) + 7) / 8;
    if constexpr (J >= 77 && J < 99) fm_slices<F0, imin(24, F1)>(p);
    if constexpr (J == 92) phi_image<0>(c, p);
    if constexpr (J == 99) phi_image<1>(c, p);
    if constexpr (S == 16) kv_stage<VBUF, K>(c, p);
    if constexpr (S == 18 && (K & 1)) ld_h<K / 2, K / 2 + 1>(c, p.h1, c.l16, p.hbase);
    if constexpr (S >= 19 && ((K & 1) || K == 4)) { constexpr int i = 4 + 5 * (S - 19) + (K < 4 ? K / 2 : (K + 1) / 2); ld_h<i, i + 1>(c, p.h1, c.l16, p.hbase); }
  } else {
    if constexpr (J >= 33 && J < 77) gelu_ops<gcum(J - 33, 44), gcum(J - 32, 44)>(p.y, p.g1);
    if constexpr (J == 77 || J == 78) v_handoff<VBUF, J - 77>(c, p.a1);
    if constexpr (J == 79) wphi_rd(c, p);
    if constexpr (J == 83) wphi_pack(p);
  }
  if constexpr (J == 89 || J == 90) h_store<J - 89>(c, p.y, p.u1, 0);
  if constexpr (TOUCH1 && S == S_FLAG1 && K == 0) touch<upos(45) + 4096>(c, c.oh);
  if constexpr (J >= P2_(R) - 23) gelu_ops<gcum(J - P2_(R) + 23, 27), gcum(J - P2_(R) + 24, 27)>(p.y, p.g2);
  if constexpr ((S == 11 || S == 12) && K == 5 - 4 * R) st_dev(c, (u32x4){~0u, ~0u, ~0u, ~0u}, c.l16 + (2 * R + S - 11) * 1024, c.op);
  if constexpr (PF && S == 10 + R && K == 2) pf_rows<spos(-2)>(c.lane < 28 ? (c.lane + 28 * c.w) * 896 : OOB, c.sd, (c.t & 3) * (224 * 896));
  if constexpr (PF && S == 12 && K == 3) pf_rows<spos(-2)>(c.lane < 7 ? (c.lane + 7 * c.w) * 896 : OOB, c.so, (c.t & 3) * (56 * 896));
  if constexpr (S == 18 && K == 0) { p.gt = ld_g(c, p.TG); if constexpr (R == 1) p.gq = ld_g(c, c.tg); }
}
template <int R, int S, int J0, int K, int N> DI void slots(Ctx& c, P1& p) {
  if constexpr (K < N) {
    constexpr int nu = iss_(S) - iss_(S - 1), u0 = iss_(S - 1);
    constexpr int ua = K == 0 ? 0 : ((2 * K + R) * nu) / (2 * N), ub = K == N - 1 ? nu : ((2 * K + 2 + R) * nu) / (2 * N);
    if constexpr (R == 1 && PRIO && S >= 18 && (K == 0 || K == 4)) __builtin_amdgcn_s_setprio(K == 0);
    mm_item<R, J0 + K - 4>(p);
    if constexpr (ub > ua) unit_m0<u0 + ua>(c);
    rd_item<R, (J0 + K) ^ 1>(p);
    if constexpr (ub > ua) { unit_go<R, u0 + ua>(c); issue_range<R, u0 + ua + 1, u0 + ub>(c); }
    slot_work<R, S, K, J0 + K>(c, p);
    SB0();
    slots<R, S, J0, K + 1, N>(c, p);
  }
}
template <int R, int S> DI void step(Ctx& c, P1& p) {
  if constexpr (S == S_POLL + 1) {
    asm volatile("s_waitcnt vmcnt(%1)\n\ts_barrier" : "+v"(p.poll) : "n"(vm_(R, S)) : "memory");
    PIN2(p.g2, p.a0);
    flag_spin(c, p.poll, c.vflag);
  } else if constexpr (S == S_POLL0 + 1) {
    asm volatile("s_waitcnt vmcnt(%1)\n\ts_barrier" : "+v"(p.poll) : "n"(vm_(R, S)) : "memory");
    PIN1(p.g1);
    flag_spin(c, p.poll, c.vflag0);
  } else if constexpr (S == 0) {
    wait_bar_v<vm_(R, S)>();
    st_dev(c, lds_rd128(spos(17) + c.m0w + c.l16), c.vrp, 0);
    if constexpr (R == 0) st_dev(c, (u32x4){~0u, ~0u, ~0u, ~0u}, c.l16, c.okvp);
  } else if constexpr (S == 16 && R == 0) wait_bar<vm_(R, S)>();
  else wait_bar_v<vm_(R, S)>();
  if constexpr (S < 2 || S >= 6) STAMP(S < 2 ? S + 1 : S - 3);
  if constexpr (S == S_FLAG0 && R == 0) flag_publish(c, 0);
  if constexpr (S == S_FLAG1 && R == 0) flag_publish(c, 1);
  if constexpr (S == S_POLL || S == S_POLL0) asm volatile("buffer_load_dwordx2 %0, %1, %2, 0 offen sc1" : "=v"(p.poll) : "v"(S == S_POLL ? c.vflag : c.vflag0), "s"(c.sw) : "memory");
  SB0();
  slots<R, S, item0_(R, S), 0, item0_(R, S + 1) - item0_(R, S)>(c, p);
}
template <int R, int S, int E> DI void steps(Ctx& c, P1& p) { if constexpr (S < E) { step<R, S>(c, p); steps<R, S + 1, E>(c, p); } }
template <int R, int A, int B, int RE, class F> DI void run(P1& p, F&& extra) {
  if constexpr (A < B) {
    mm_item<R, A - 4>(p);
    if constexpr (A < RE) rd_item<R, A ^ 1>(p);
    extra(std::integral_constant<int, A>{});
    SB0();
    run<R, A + 1, B, RE>(p, extra);
  } else { PIN2(p.c[0], p.c[1]); }
}
template <int R, int K, class F> DI void fmq(P1& p, F&& extra) {
  if constexpr (K < 24) { fm_slice<K>(p); extra(std::integral_constant<int, K>{}); SB0(); fmq<R, K + 1>(p, extra); }
}

template <int R> DI void ffn_wg(Ctx& c, P1& p) {
  const u32 l = c.lane;
  const int tg2 = c.tg & 1, ks = c.tg >> 1;
  p.TG = 2 * (R == 1 ? c.kh : 1 - c.kh) + tg2;
  p.hbase = WS_H + (u32)(((c.t * 4 + p.TG) * 112 + 56 * c.kh + 2 * ks) * 1024);
  asm volatile("" : "+s"(p.hbase));
  PIN1(c.awp);
  p.g1 = p.u1 = p.a0 = p.a1 = p.g2 = p.u2 = p.c[0] = p.c[1] = (f32x16){};
  steps<R, 0, 21>(c, p);
  STAMP(18);
  const u32 qq = (l & 15) >> 2, pp_ = l & 3, blk = (l >> 4) & 1, h = l >> 5;
  const u32 b0 = (4 * h + qq) * 128 + ((qq >> 1) << 6) + 32 * blk + 8 * pp_;
  const u32 bk = b0 ^ (64 * ks);
  p.s2[0] = mkseg(bk + ks * 4096); p.s2[1] = mkseg((bk ^ 64) + ks * 4096);
  p.ob[0] = bk + ks * (112 * 128); p.ob[1] = (bk ^ 64) + ks * (112 * 128);
  PIN2(p.ob[0], p.ob[1]);
  auto none = [](auto) {};
  constexpr int B = P2_(R);
  auto epi2 = [&](auto J) {
    if constexpr (J.value < B + 4) gelu_ops<gcum(J.value - B + 23, 27), gcum(J.value - B + 24, 27)>(p.y, p.g2);
    if constexpr (J.value == B + 5 || J.value == B + 6) h_store<J.value - B - 5>(c, p.y, p.u2, 1);
  };
  const u32 obase = WS_O + (u32)(((c.t * 4 + p.TG) * 16 + 8 * c.kh + 4 * ks) * 2048);
  const u32 l16h = c.l16 + 4096, vflag2 = c.vflag + ((l & 31) < 7 ? 8 : 0);
  if constexpr (R == 0) {
    run<R, 140, 140 + XP, 999>(p, epi2);
    STAMP(19);
    wait_bar_v<0>();
    STAMP(20);
    flag_publish(c, 2);
    if (TOUCH2) touch<spos(17)>(c, c.oh + 4096);
    run<R, 140 + XP, 172, 999>(p, [&](auto J) { if constexpr (J.value >= 164) unit_half<68 + (J.value - 164) / 2, J.value & 1>(c); });
    STAMP(21);
    __builtin_amdgcn_s_sleep(PSLEEP);
    flag_spin(c, __builtin_amdgcn_raw_buffer_load_b64(c.sw, vflag2, 0, 16), vflag2);
    STAMP(22);
    ld_h<0, 6>(c, p.h2, l16h, p.hbase);
    asm volatile("s_barrier" ::: "memory");
    STAMP(23);
    run<R, 172, 200, 999>(p, [&](auto J) {
      if constexpr (J.value < 180) ld_h<J.value - 166, J.value - 165>(c, p.h2, l16h, p.hbase);
      if constexpr (J.value >= OPOS && J.value < OPOS + 8) ld_1k<J.value - OPOS, J.value - OPOS + 1>(c, p.of, obase);
    });
  } else {
    __builtin_amdgcn_s_setprio(1);
    run<R, 168, 172, 999>(p, epi2);
    fm_slices<0, 2>(p);
    SB0();
    h_store<0>(c, p.y, p.u2, 1); h_store<1>(c, p.y, p.u2, 1);
    SB0();
    STAMP(19);
    fmq<R, 3>(p, [&](auto K) {
      if constexpr (K.value == XQ) {
        wait_bar_v<0>();
        if (TOUCH2) touch<spos(17)>(c, c.oh + 4096);
        ld_1k<0, 4>(c, p.kv, WS_KVR + (u32)c.hd * 4096);
      }
      if constexpr (K.value > XQ && K.value <= XQ + H1Q) ld_h<K.value - XQ - 1, K.value - XQ>(c, p.h1, c.l16, p.hbase);
    });
    STAMP(21);
    chk<0, 4>(p.kv, [&] { ld_1k<0, 4>(c, p.kv, WS_KVR + (u32)c.hd * 4096); });
    o_publish(c, p.kv, p.ph, p.gq);
    __builtin_amdgcn_s_setprio(0);
    STAMP(22);
    u32x2 f2;
    run<R, 172, 172 + YP, 999>(p, [&](auto J) {
      if constexpr (J.value - 172 + H1Q < 14) ld_h<J.value - 172 + H1Q, J.value - 171 + H1Q>(c, p.h1, c.l16, p.hbase);
      if constexpr (J.value == 171 + YP) f2 = __builtin_amdgcn_raw_buffer_load_b64(c.sw, vflag2, 0, 16);
    });
    asm volatile("s_barrier" ::: "memory");
    STAMP(23);
    run<R, 172 + YP, 186, 999>(p, none);
    flag_spin(c, f2, vflag2);
    run<R, 186, 228, 999>(p, [&](auto J) {
      if constexpr (J.value < 200) ld_h<J.value - 186, J.value - 185>(c, p.h2, l16h, p.hbase);
      if constexpr (J.value >= 204 && J.value < 220 && !(J.value & 1)) ld_1k<(J.value - 204) / 2, (J.value - 202) / 2>(c, p.of, obase);
    });
  }
  const float om = 1.0f - p.gt;
#pragma unroll
  for (int i = 0; i < 16; ++i) { p.c[0][i] *= om; p.c[1][i] *= om; }
  PIN2(p.c[0], p.c[1]);
  STAMP(27);
  chk<0, 8>(p.of, [&] { ld_1k<0, 8>(c, p.of, obase); });
  STAMP(28);
  run<R, B + 60, B + 76, 999>(p, none);
  f32x16 &c0 = p.c[0], &c1 = p.c[1];
  STAMP(29);
  if constexpr (R == 0) {
    st_dev(c, sanit(pack8<0>(c0)), c.l16, c.op + ks * 2048);
    st_dev(c, sanit(pack8<1>(c0)), c.l16 + 1024, c.op + ks * 2048);
    st_dev(c, sanit(pack8<0>(c1)), c.l16, c.op + 2048 - ks * 2048);
    st_dev(c, sanit(pack8<1>(c1)), c.l16 + 1024, c.op + 2048 - ks * 2048);
    STAMP(30);
  } else {
    u32x4 pp[4];
    const u32 so = ((c.op ^ (8 * 16384)) & ~8192u) + ks * 2048;
    auto ld_p = [&] {
#pragma unroll
      for (int i = 0; i < 4; ++i) pp[i] = ld_dev(c, c.l16 + (i & 1) * 1024, so + (i >> 1) * 8192);
    };
    ld_p();
    f32x16 &keep = c0, &give = c1;
    const u32 xa = spos(16) + tg2 * 4096 + c.l16;
#pragma unroll
    for (int g = 0; g < 4; ++g) *(LAS f32x4*)(xa + ks * 8192 + g * 1024) = (f32x4){give[4 * g], give[4 * g + 1], give[4 * g + 2], give[4 * g + 3]};
    bar_l();
#pragma unroll
    for (int g = 0; g < 4; ++g) {
      f32x4 v = *(LAS f32x4*)(xa + (1 - ks) * 8192 + g * 1024);
#pragma unroll
      for (int i = 0; i < 4; ++i) keep[4 * g + i] += v[i];
    }
    PIN1(keep);
    chk<0, 4>(pp, ld_p);
    STAMP(30);
#pragma unroll
    for (int d = 0; d < 4; ++d) {
      keep[2 * d] += blo(pp[0][d]) + blo(pp[2][d]); keep[2 * d + 1] += bhi(pp[0][d]) + bhi(pp[2][d]);
      keep[8 + 2 * d] += blo(pp[1][d]) + blo(pp[3][d]); keep[9 + 2 * d] += bhi(pp[1][d]) + bhi(pp[3][d]);
    }
    char* orow = c.out + (size_t)(c.t * 128 + c.kh * 64 + tg2 * 32 + (l & 31)) * 896 + c.jj * 128 + ks * 64 + h * 16;
#pragma unroll
    for (int s = 0; s < 2; ++s) {
      u32x4 v = s ? pack8<1>(keep) : pack8<0>(keep);
      auto a = __builtin_amdgcn_permlane32_swap(v[0], v[2], false, false);
      auto b = __builtin_amdgcn_permlane32_swap(v[1], v[3], false, false);
      *(u32x4*)(orow + s * 32) = (u32x4){a[0], b[0], a[1], b[1]};
    }
    if (__builtin_amdgcn_workgroup_id_x() == 0 && c.w == 4) {
      u32x2 v = {(u32)c.tag, (u32)(c.tag >> 32)};
      __builtin_amdgcn_raw_buffer_store_b64(v, c.sw, l == 0 ? 0u : OOB, 0, 16);
    }
    STAMP(31);
  }
}

template <int R, int A0, int A, int B, int ST> DI void pipe(P1& p) {
  if constexpr (A - 3 * ST < B) {
    if constexpr (A - 3 * ST >= A0) mm_item<R, A - 3 * ST>(p);
    if constexpr (A < B) rd_item<R, A>(p);
    SB0();
    pipe<R, A0, A + ST, B, ST>(p);
  }
}
constexpr u32 sxpos(int ch) { return spos(2 * ch - 2); }
template <int N, int E> DI void spare_x(const Ctx& c) {
  if constexpr (N < E) { set_m0<sxpos(N >> 1) + (N & 1) * UNIT>(c); asm volatile("s_nop 0"); dma<0>((N & 1) ? c.vx2 : c.vx, c.sx, c.rot[N >> 1] * 128); spare_x<N + 1, E>(c); }
}
template <int R> DI void spare_wg(Ctx& c, P1& p) {
  const u32 l = c.lane;
  set_m0<spos(12)>(c); asm volatile("s_nop 0"); dma<0>(c.l16, mk(c.gw, 896), c.w == 0 ? 0u : 0x40000000u);
  const u32 okvr = WS_KVR + (u32)(c.t * 4096 + (2 * c.kh + (c.w & 1)) * 1024);
  const u32x4 ones = {~0u, ~0u, ~0u, ~0u};
  st_dev(c, ones, R == 0 || c.tg < 2 ? c.l16 : OOB, R == 0 ? c.okvp : okvr);
  asm volatile("s_waitcnt vmcnt(6)" ::: "memory");
  st_dev(c, lds_rd128(spos(17) + c.m0w + c.l16), c.vrp, 0);
  spare_x<4, 14>(c);
  wait_bar<0>();
  if constexpr (R == 0) flag_publish(c, 0);
  {
    const u32 mo = WS_O + ((c.t * 4 + c.tg) * 16 + 8 * c.kh + 4 * R) * 2048;
#pragma unroll
    for (int i = 0; i < 8; ++i) st_dev(c, ones, c.l16 + (i & 3) * 1024, mo + (i >> 2) * 4096);
  }
#pragma unroll
  for (int s = 0; s < 28; ++s) p.xf[s] = as_frag(lds_rd128((p.ax ^ (u32)(32 * (s & 3))) + sxpos(s >> 2)));
  STAMP(1);
  f32x16 ga = {};
#pragma unroll
  for (int s = 14 * R; s < 14 * R + 14; ++s) ga = mfma(as_frag(lds_rd128(spos(12) + c.rot[s >> 2] * 128 + (s & 3) * 32 + (l >> 5) * 16)), p.xf[s], ga);
  PIN1(ga);
  flag_spin(c, __builtin_amdgcn_raw_buffer_load_b64(c.sw, c.vflag0, 0, 16), c.vflag0);
  bar_l();
  issue_range<R, 25, 33>(c);
  issue_range<R, 36, 37>(c); issue_range<R, 39, 40>(c); issue_range<R, 42, 43>(c);
  p.a0 = p.a1 = (f32x16){};
  float g1 = 0.f;
  if constexpr (R == 0) *(LAS float*)(spos(0) + c.tg * 256 + l * 4) = ga[0];
  wait_bar<7>();
  STAMP(2);
  if constexpr (R == 1) {
    g1 = __builtin_amdgcn_rcpf(1.0f + __builtin_amdgcn_exp2f(-LOG2E * (ga[0] + *(LAS float*)(spos(0) + c.tg * 256 + l * 4))));
    __builtin_amdgcn_raw_buffer_store_b32(f2u(g1), c.sw, l < 32 ? l * 4 : OOB, c.og + c.tg * 128, 16);
  }
  pipe<R, 44, 44, 60, 1>(p);
  wait_bar_v<0>();
  if constexpr (R == 0) flag_publish(c, 1);
  issue_range<R, 45, 46>(c);
  wphi_rd(c, p);
  pipe<R, 60, 60, 72, 1>(p);
  wphi_pack(p);
  wait_bar_v<0>();
  if constexpr (R == 0) { fm_slices<0, 24>(p); phi_image<0>(c, p); phi_image<1>(c, p); }
  else { v_handoff<spos(1), 0>(c, p.a1); v_handoff<spos(1), 1>(c, p.a1); pipe<R, 85, 85, 140, 2>(p); PIN1(p.a0); }
  bar_l();
  if constexpr (R == 0) {
    if (c.w >= 2) return;
    kv_stage<spos(1), 0>(c, p); kv_stage<spos(1), 1>(c, p); kv_stage<spos(1), 2>(c, p); kv_stage<spos(1), 3>(c, p);
    kv_stage<spos(1), 4>(c, p); kv_stage<spos(1), 5>(c, p); kv_stage<spos(1), 7>(c, p);
    STAMP(3);
    flag_spin(c, __builtin_amdgcn_raw_buffer_load_b64(c.sw, c.vflag, 0, 16), c.vflag);
    STAMP(4);
    u32x4 v[16];
    const u32 ro = WS_KVP + (u32)(c.t * 65536 + (2 * c.kh + c.w) * 1024);
    ld_lin<0, 16>(c, v, ro, 4096);
    chk<0, 16>(v, [&] { ld_lin<0, 16>(c, v, ro, 4096); });
    STAMP(5);
    float s[8], s2[8];
    set8(s, v[0]); set8(s2, v[1]);
#pragma unroll
    for (int i = 2; i < 16; i += 2) { add8(s, v[i]); add8(s2, v[i + 1]); }
#pragma unroll
    for (int i = 0; i < 8; ++i) s[i] += s2[i];
    st_dev(c, sanit(pack8<0>(s)), c.l16, okvr);
    STAMP(6);
  } else {
    fm_slices<0, 24>(p);
    flag_spin(c, __builtin_amdgcn_raw_buffer_load_b64(c.sw, c.vflag, 0, 16), c.vflag);
    u32x4 v[4];
    const u32 kvo = WS_KVR + (u32)c.hd * 4096;
    ld_1k<0, 4>(c, v, kvo);
    chk<0, 4>(v, [&] { __builtin_amdgcn_s_sleep(8); ld_1k<0, 4>(c, v, kvo); });
    o_publish(c, v, p.ph, g1);
    STAMP(7);
  }
}

extern "C" __global__ __launch_bounds__(512, 2) void fused_kernel(const char* x, const char* wqkv, const char* wphi, const char* wo, const char* wg,
                                                                   const char* wu, const char* wd, const char* gw, char* out, char* ws) {
  Ctx c;
  const int bid = __builtin_amdgcn_workgroup_id_x(), xcd = bid & 7, idx = bid >> 3;
  const u32 tid = __builtin_amdgcn_workitem_id_x();
  c.gw = gw; c.out = out; c.ws = ws;
  c.t = 4 * (xcd >> 1) + (idx & 3); c.kh = xcd & 1; c.jj = idx >> 2; c.hd = 8 * c.kh + c.jj; c.j = 7 * c.kh + c.jj;
  c.w = __builtin_amdgcn_readfirstlane(tid >> 6); c.tg = c.w & 3; c.r = c.w >> 2;
  c.lane = tid & 63; c.l16 = c.lane * 16; c.m0w = c.w * 1024;
  const u32 l = c.lane, w = c.w;
  STAMP(0);
  const u64 rt = 0x2106543210654321ull, r0 = c.jj + 2 * (idx & 3) + (xcd >> 1);
  const u32 rs = (u32)(rt >> (4 * (r0 > 9 ? r0 - 7 : r0)));
#pragma unroll
  for (int i = 0; i < 7; ++i) c.rot[i] = (rs >> (4 * i)) & 7;
  const u32 row8 = 8 * w + (l >> 3), sw2 = ((l & 7) ^ (((l >> 4) & 1) << 2)) << 4;
  c.vx = row8 * 896 + (((l & 7) ^ ((4 * w + (l >> 4)) & 7)) << 4); c.vx2 = c.vx + 64 * 896;
  c.vw = row8 * 3584 + sw2;
  c.sx = mk(x + c.t * 114688, 114688);
  c.sg = mk(wg + c.j * 256, 448 * 3584 - c.j * 256);
  {
    const u32 win = 56 * c.hd - 8 * (c.hd & 1), i = (w & 1) * 64 + l;
    const bool ok = i < 112 && w < 6;
    set_m0<spos(17)>(c); asm volatile("s_nop 0");
    dma<0>(ok ? (i >> 2) * 2688 + (i & 3) * 16 : OOB, mk(wqkv, 448 * 2688), c.t * (28 * 2688) + (w >> 1) * 896 + win);
    c.vrp = ok ? WS_WA + c.hd * 86016 + (w >> 1) * 28672 + c.t * 1792 + i * 16 : OOB;
  }
  if (c.jj < 7) issue_range<0, 0, PRO>(c);
  else {
    u32 lo; u64 pc;
    asm volatile("s_getpc_b64 %0\n.Lpf_here:\n\ts_mov_b32 %1, .Lpf_here-fused_kernel" : "=s"(pc), "=s"(lo));
    asm volatile("s_mov_b32 m0, %0\n\ts_nop 0\n\tbuffer_load_dword %1, %2, 0 offen lds"
                 :: "i"(spos(13)), "v"((w * 64 + l) * 128), "s"(mk((const char*)(pc - lo), CODE_BYTES)) : "memory");
    spare_x<0, 4>(c);
  }
  SB0();
  c.tag = *(const __attribute__((address_space(4))) u64*)ws + 1;
  c.v2 = row8 * 896 + sw2; c.v2b = c.v2 + 64 * 896;
  c.vq = WS_WA + c.hd * 86016 + c.tg * 1024 + c.l16; c.vkv = c.vq + 28672 * (1 + c.r);
  c.vflag0 = WS_FLAG + (((l & 15) * 16 + c.hd) << 7);
  c.sp = mk(wphi + c.hd * 1568, 1568);
  c.sw = mk(ws, WS_END);
  c.oh = WS_H + (u32)(((c.t * 4 + c.tg) * 112 + 8 * c.j + 2 * c.r) * 1024);
  c.oo = WS_O + (u32)(((c.t * 4 + c.tg) * 16 + c.hd) * 2048);
  c.op = WS_P + (u32)((c.t * 16 + c.hd) * 16384 + c.tg * 4096);
  c.okvp = WS_KVP + (u32)((c.hd * 16 + c.t) * 4096 + c.tg * 1024);
  c.oflag = WS_FLAG + (u32)(c.t * 16 + c.hd) * 128;
  c.og = WS_G + (u32)(c.kh * 2048 + c.t * 128) * 4;
  static_assert(WS_P % (16 * 16384) == 0, "the partner's slot is op ^ (8 * 16384)");
  c.vflag = flag_off(c, c.jj == 7); c.vtouch = l < 16 ? l * 128 : OOB; c.vone = tid == 0 ? 0u : OOB;
  { const u32 rho = l & 31, f = rho < 26 ? rho : ((rho == 28 || rho == 29) ? rho - 2 : 0);
    c.awp = upos(32) + 5 * 1024 + 2 * (28 * (4 * (l >> 5) - 4 * (c.hd & 1)) + f); }
  asm volatile("" : "+s"(c.tag), "+s"(c.oh), "+s"(c.oo), "+s"(c.op), "+s"(c.okvp), "+s"(c.oflag), "+s"(c.og) :: "memory");
  asm volatile("" : "+v"(c.vflag), "+v"(c.vflag0), "+v"(c.vtouch), "+v"(c.vone), "+v"(c.vrp));
  P1 p;
  {
    const u32 q = (l & 15) >> 2, pp = l & 3, blk = (l >> 4) & 1, h = l >> 5, r = l & 31;
    p.aw = mkseg((8 * h + q) * 128 + ((64 * c.r) ^ ((q >> 1) << 6)) + 32 * blk + 8 * pp);
    p.aq = mkseg((8 * h + q) * 64 + 32 * blk + 8 * pp);
    p.ax = (32 * c.tg + r) * 128 + ((h ^ ((r >> 1) & 7)) << 4);
  }
  if (c.jj < 7) {
    c.su = mk(wu + c.j * 256, 448 * 3584 - c.j * 256);
    c.sd = mk(wd + c.kh * 896 * 896 + c.jj * 128, 896 * 896 - c.jj * 128);
    c.so = mk(wo + c.kh * 224 * 896 + c.jj * 128, 224 * 896 - c.jj * 128);
    if (c.r == 0) { ffn_wg<0>(c, p); __builtin_amdgcn_endpgm(); }
    ffn_wg<1>(c, p);
    __builtin_amdgcn_endpgm();
  }
  if (c.r == 0) { spare_wg<0>(c, p); __builtin_amdgcn_endpgm(); }
  spare_wg<1>(c, p);
}

#if defined(__HIP_DEVICE_COMPILE__)
#pragma clang attribute pop
#endif

extern "C" void kernel_launch(void* const* d_in, const int* in_sizes, int n_in, void* d_out, int out_size, void* d_ws, size_t ws_size,
                              hipStream_t stream) {
  (void)hipFuncSetAttribute((const void*)fused_kernel, hipFuncAttributeMaxDynamicSharedMemorySize, LDS_BYTES);
  fused_kernel<<<256, 512, LDS_BYTES, stream>>>((const char*)d_in[0], (const char*)d_in[1], (const char*)d_in[2], (const char*)d_in[3],
                                                (const char*)d_in[5], (const char*)d_in[6], (const char*)d_in[7], (const char*)d_in[8],
                                                (char*)d_out, (char*)d_ws);
}
```

```cpp
#define STAMPS 4
#define PROBE_K 1
#include <hip/hip_runtime.h>
#include <hip/hip_bf16.h>
#include <type_traits>
#pragma clang diagnostic ignored "-Wint-to-pointer-cast"

typedef unsigned u32;
typedef unsigned long long u64;
typedef u32 u32x4 __attribute__((ext_vector_type(4)));
typedef u32 u32x2 __attribute__((ext_vector_type(2)));
typedef short s16x4 __attribute__((ext_vector_type(4)));
typedef __bf16 bf16x8 __attribute__((ext_vector_type(8)));
typedef __bf16 bf16x2 __attribute__((ext_vector_type(2)));
typedef float f32x16 __attribute__((ext_vector_type(16)));
typedef float f32x4 __attribute__((ext_vector_type(4)));
typedef float f32x2 __attribute__((ext_vector_type(2)));
typedef __amdgpu_buffer_rsrc_t rsrc_t;
#define DI __device__ __forceinline__
#define LAS __attribute__((address_space(3)))
#define SB0() __builtin_amdgcn_sched_barrier(0)
#define PIN1(a) asm volatile("" : "+v"(a))
#define PIN2(a, b) asm volatile("" : "+v"(a), "+v"(b))

constexpr u32 WS_FLAG = 4096;
constexpr u32 WS_G    = WS_FLAG + 256 * 128;
constexpr u32 WS_KVR  = 65536;
constexpr u32 WS_KVP  = 262144;
constexpr u32 WS_O    = WS_KVP + 256 * 4096;
constexpr u32 WS_P    = WS_O + 64 * 32 * 1024;
constexpr u32 WS_H    = WS_P + 256 * 16384;
constexpr u32 WS_WA   = WS_H + 64 * 112 * 1024;
constexpr u32 WS_END  = WS_WA + 16 * 3 * 28672;
constexpr u32 WS_STAMP = 32u << 20;

constexpr int UNIT = 8192, RING = 18;
constexpr u32 SCR = 0, RBASE = 2 * UNIT;
constexpr int LDS_BYTES = 163840;
constexpr u32 OOB = 0x80000000u;
constexpr u32 CODE_BYTES = 40960;
#define ABL 0
#define MARKV 0
#define TOUCH2 0
#define PSLEEP 4
#define PRIO 1
#define XP 24
#define OPOS 192
#define KVPOS 2
#define PF 0
#define H1Q 6
#define POLL2 166
#define YP 10
#define XQ 14
#define POLL0 6
#define TOUCH1 0
#define PROV 6
constexpr u32 SPIN_MAX = ABL ? 0 : 1u << 16;
constexpr float LOG2E = 1.4426950408889634f;

#if defined(__HIP_DEVICE_COMPILE__)
#pragma clang attribute push(__attribute__((target("no-packed-fp32-ops"))), apply_to = function)
#endif
constexpr u32 spos(int k) { return RBASE + k * UNIT; }
constexpr int w2c_(int i) { constexpr int t[18] = {16, 17, 0, 1, 2, 3, 4, 5, 6, 7, 8, 9, 10, 11, 12, 13, 14, 15}; return t[i]; }
constexpr int w2s_(int c) { constexpr int t[18] = {2, 3, 4, 5, 6, 7, 8, 9, 10, 12, 11, 13, 14, 15, -2, -1, 0, 1}; return t[c]; }
constexpr int uslot(int n) {
  constexpr int t[22] = {-1, 14, 15, 16, 17, 0, 1, 2, 3, 4, 5, 6, 7, 8, 9, 10, 11, 13, 14, 15, 16, 17};
  return n < 32 ? n % RING : n < 54 ? t[n - 32] : w2s_(w2c_(n - 54));
}
constexpr u32 upos(int n) { return spos(uslot(n)); }
constexpr u32 VBUF = RBASE + 12 * UNIT;
constexpr int imin(int a, int b) { return a < b ? a : b; }

struct Ctx {
  const char* gw;
  char *out, *ws;
  int t, kh, jj, hd, j, w, tg, r;
  u32 lane, l16, m0w;
  u32 vx, vx2, vw, vq, vkv, v2, v2b;
  rsrc_t sx, sg, su, sq, sp, sd, so, sw;
  u64 tag;
  u32 rot[7];
  u32 oh, oo, op, okvp;
  u32 oflag, og;
  u32 vrp, vflag0;
  u32 vflag, vtouch, awp, vone;
};

DI rsrc_t mk(const void* p, u32 bytes) { return __builtin_amdgcn_make_buffer_rsrc((void*)p, 0, bytes, 0x00020000); }
template <u32 L> DI void set_m0(const Ctx& c) { if (!(ABL & 1)) asm volatile("s_add_u32 m0, %0, %1" :: "s"(c.m0w), "i"(L) : "memory", "scc"); }
DI void dma_dev(u32 voff, rsrc_t rs, u32 soff) {
  if (!(ABL & 1)) asm volatile("buffer_load_dwordx4 %0, %1, %2 offen sc1 lds" :: "v"(voff), "s"(rs), "s"(soff) : "memory");
}
template <u32 OFF> DI void dma(u32 voff, rsrc_t rs, u32 soff) {
  if (!(ABL & 1)) asm volatile("buffer_load_dwordx4 %0, %1, %2 offen offset:%3 lds" :: "v"(voff), "s"(rs), "s"(soff), "i"(OFF) : "memory");
}
template <int N> DI void wait_bar() { asm volatile("s_waitcnt vmcnt(%0) lgkmcnt(0)\n\ts_barrier" :: "n"(N) : "memory"); }
template <int N> DI void wait_bar_v() { asm volatile("s_waitcnt vmcnt(%0)\n\ts_barrier" :: "n"(N) : "memory"); }
DI void bar_l() { asm volatile("s_waitcnt lgkmcnt(0)\n\ts_barrier" ::: "memory"); }

constexpr u32 ioff_(int n) { return n >= 36 && n < 54 && !(n < 47 && n % 3 == 0) ? 128 : 0; }
template <int N> DI void unit_m0(const Ctx& c) { set_m0<upos(N) - ioff_(N)>(c); }
template <int R, int N> DI void unit_go(const Ctx& c) {
  if constexpr (N < 21) {
    if constexpr (N % 3 < 2) dma<0>(N % 3 ? c.vx2 : c.vx, c.sx, c.rot[N / 3] * 128);
    else dma<0>(c.vw, c.sg, c.rot[N / 3] * (64 * 3584));
  } else if constexpr (N >= 25 && N < 32) dma_dev(c.vkv, c.sw, c.rot[N - 25] * 4096);
  else if constexpr (N == 32) dma<0>(c.l16, c.sp, c.w == 5 ? 0u : c.w == 6 ? 1024u : 0x40000000u);
  else if constexpr (N < 36) dma<0>(c.vw, c.su, c.rot[N < 25 ? N - 21 : N - 29] * (64 * 3584));
  else if constexpr (N < 47) {
    if constexpr (N % 3 == 0) dma_dev(c.vq, c.sw, N == 45 && R == 1 ? 0x40000000u : c.rot[(2 * (N - 36) / 3 + R) % 7] * 4096);
    else dma<128>(c.vw, c.sg, c.rot[2 * ((N - 36) / 3) + N % 3 - 1] * (64 * 3584));
  } else if constexpr (N < 54) dma<128>(c.vw, c.su, c.rot[N - 47] * (64 * 3584));
  else {
    constexpr int k = w2c_(N - 54);
    if constexpr (k < 14) dma<0>(k < 7 ? c.v2 : c.v2b, c.sd, c.rot[k % 7] * (128 * 896));
    else dma<0>((k & 1) ? c.v2b : c.v2, c.so, k < 16 ? 0u : 128u * 896u);
  }
}
template <int N, int H> DI void unit_half(const Ctx& c) {
  set_m0<upos(N) + H * 4096>(c);
  constexpr int k = w2c_(N - 54);
  const u32 so = (k < 14 ? c.rot[k % 7] * (128 * 896) : k < 16 ? 0u : 128u * 896u) + H * (32 * 896);
  asm volatile("s_nop 0");
  dma<0>((k < 14 ? k >= 7 : (k & 1)) ? c.v2b : c.v2, k < 14 ? c.sd : c.so, so);
}
template <int R, int A, int B> DI void issue_range(const Ctx& c) {
  if constexpr (A < B) { unit_m0<A>(c); asm volatile("s_nop 0"); unit_go<R, A>(c); issue_range<R, A + 1, B>(c); }
}

struct Seg { u32 b[3]; };
DI Seg mkseg(u32 a) { Seg s; s.b[0] = a; s.b[1] = a + 65536; s.b[2] = a + 131072; asm volatile("" : "+v"(s.b[0]), "+v"(s.b[1]), "+v"(s.b[2])); return s; }
DI u32 sa(const Seg& s, u32 off) { return s.b[off >> 16] + (off & 0xffffu); }
DI f32x16 mfma(bf16x8 a, bf16x8 b, f32x16 c) { return __builtin_amdgcn_mfma_f32_32x32x16_bf16(a, b, c, 0, 0, 0); }
DI bf16x8 as_frag(u32x4 v) { return __builtin_bit_cast(bf16x8, v); }
DI bf16x8 tr_frag(u32 a0, u32 a1) {
  s16x4 lo = __builtin_amdgcn_ds_read_tr16_b64_v4i16((LAS s16x4*)a0);
  s16x4 hi = __builtin_amdgcn_ds_read_tr16_b64_v4i16((LAS s16x4*)a1);
  return __builtin_bit_cast(bf16x8, __builtin_shufflevector(lo, hi, 0, 1, 2, 3, 4, 5, 6, 7));
}
DI u32x4 lds_rd128(u32 a) { return *(LAS u32x4*)a; }
DI void lds_wr128(u32 a, u32x4 v) { *(LAS u32x4*)a = v; }
DI u32 f2u(float f) { return __builtin_bit_cast(u32, f); }
DI float u2f(u32 u) { return __builtin_bit_cast(float, u); }
DI u32 pk(float a, float b) { f32x2 t = {a, b}; return __builtin_bit_cast(u32, __builtin_convertvector(t, bf16x2)); }
template <int S, class V> DI u32x4 pack8(const V& v) {
  u32x4 r = {pk(v[8 * S], v[8 * S + 1]), pk(v[8 * S + 2], v[8 * S + 3]), pk(v[8 * S + 4], v[8 * S + 5]), pk(v[8 * S + 6], v[8 * S + 7])};
  return r;
}
DI float blo(u32 d) { return u2f(d << 16); }
DI float bhi(u32 d) { return u2f(d & 0xffff0000u); }
DI u32 umax(u32 a, u32 b) { return a > b ? a : b; }
DI bool any_(bool p) { return __builtin_amdgcn_ballot_w64(p) != 0; }
DI u32x4 sanit(u32x4 v) { return __builtin_elementwise_min(v, (u32x4){0xfffffffeu, 0xfffffffeu, 0xfffffffeu, 0xfffffffeu}); }
DI void st_dev(const Ctx& c, u32x4 v, u32 voff, u32 soff) {
  if (ABL & 8) { asm volatile("" :: "v"(v)); return; }
  __builtin_amdgcn_raw_buffer_store_b128(v, c.sw, voff, soff, 16);
  asm volatile("s_nop 1" :: "v"(v) : "memory");
}
DI u32x4 ld_dev(const Ctx& c, u32 voff, u32 soff) {
  if (ABL & 16) { u32x4 z = {voff, soff, voff, soff}; asm volatile("" : "+v"(z)); return z; }
  return __builtin_amdgcn_raw_buffer_load_b128(c.sw, voff, soff, 16);
}
DI f32x2 xhalf(float v) { auto p = __builtin_amdgcn_permlane32_swap(f2u(v), f2u(v), false, false); f32x2 r = {u2f(p[0]), u2f(p[1])}; return r; }

DI void stamp(const Ctx& c, int i) {
  SB0();
  u64 tm, tc; asm volatile("s_memrealtime %0\n\ts_memtime %1\n\ts_waitcnt lgkmcnt(0)" : "=s"(tm), "=s"(tc) :: "memory");
  tm = (tm & 0xffffffffull) | (tc << 32);
  { u32x2 v = {(u32)tm, (u32)(tm >> 32)}; __builtin_amdgcn_raw_buffer_store_b64(v, mk(c.ws + WS_STAMP, 1 << 20), c.lane == 0 ? 0u : OOB, ((__builtin_amdgcn_workgroup_id_x() * 8 + c.w) * 32 + i) * 8, 0); }
  SB0();
}
#define STAMP(i) do { if ((i) == 0 || (i) == PROBE_K) stamp(c, i); } while (0)

struct P1 {
  Seg aw, aq; u32 ax, ka, kb, kc;
  bf16x8 xf[28], f[8], af[8], vf[8];
  f32x16 g1, u1, a0, a1, g2, u2, fp, k0, c[2];
  Seg s2[2]; u32 ob[2];
  u32x4 h2[14], of[8];
  u32x4 wp[2], ph[4], h1[14], kv[4];
  u32 wr[16];
  float y[16], e[14], sum, mxl, mnl;
  u32x2 poll;
  float gq, gt;
  u32 hbase; int TG;
};

DI void wphi_rd(const Ctx& c, P1& p) {
#pragma unroll
  for (int i = 0; i < 16; ++i) p.wr[i] = *(LAS unsigned short*)(c.awp + 56 * (16 * (i >> 3) + 8 * ((i >> 2) & 1) + (i & 3)));
}
DI void wphi_pack(P1& p) {
#pragma unroll
  for (int i = 0; i < 8; ++i) p.wp[i >> 2][i & 3] = p.wr[2 * i] | (p.wr[2 * i + 1] << 16);
  PIN2(p.wp[0], p.wp[1]);
}
template <int K> DI void fm_slice(P1& p) {
  if constexpr (K == 0) { p.fp = mfma(as_frag(p.wp[0]), as_frag(pack8<0>(p.a0)), (f32x16){}); }
  else if constexpr (K == 1) { p.fp = mfma(as_frag(p.wp[1]), as_frag(pack8<1>(p.a0)), p.fp); }
  else if constexpr (K == 2) { }
  else if constexpr (K == 3) {
    PIN1(p.fp);
    float mx = p.fp[0];
#pragma unroll
    for (int i = 1; i < 14; ++i) mx = __builtin_fmaxf(mx, p.fp[i]);
    p.mxl = mx; PIN1(p.mxl);
  } else if constexpr (K == 4) {
    float mn = p.fp[0];
#pragma unroll
    for (int i = 1; i < 14; ++i) mn = __builtin_fminf(mn, p.fp[i]);
    p.mnl = mn; PIN1(p.mnl);
  } else if constexpr (K == 5) {
    f32x2 t = xhalf(p.mxl); p.mxl = __builtin_fmaxf(t[0], t[1]) * LOG2E;
    t = xhalf(p.mnl); p.mnl = __builtin_fminf(t[0], t[1]) * LOG2E;
    PIN2(p.mxl, p.mnl);
  } else {
    constexpr int NEG = K >= 15, k = K - 6 - 9 * NEG;
    if constexpr (k < 5) {
#pragma unroll
      for (int i = 3 * k; i < imin(14, 3 * k + 3); ++i) {
        p.e[i] = __builtin_amdgcn_exp2f(NEG ? __builtin_fmaf(p.fp[i], -LOG2E, p.mnl) : __builtin_fmaf(p.fp[i], LOG2E, -p.mxl));
        PIN1(p.e[i]);
      }
      if constexpr (k == 1) p.sum = p.e[0] + p.e[1] + p.e[2];
      if constexpr (k > 1) p.sum += p.e[3 * k - 3] + p.e[3 * k - 2] + p.e[3 * k - 1];
      if constexpr (k > 0) PIN1(p.sum);
    } else if constexpr (k == 5) {
      f32x2 t = xhalf(p.sum + p.e[12] + p.e[13]); p.sum = __builtin_amdgcn_rcpf(t[0] + t[1]); PIN1(p.sum);
    } else if constexpr (k < 8) {
#pragma unroll
      for (int i = 7 * (k - 6); i < 7 * (k - 5); ++i) { p.e[i] *= p.sum; PIN1(p.e[i]); }
    } else {
      u32x4 o1 = {pk(p.e[8], p.e[9]), pk(p.e[10], p.e[11]), pk(p.e[12], p.e[13]), 0u};
      p.ph[2 * NEG] = pack8<0>(p.e); p.ph[2 * NEG + 1] = o1; PIN2(p.ph[2 * NEG], p.ph[2 * NEG + 1]);
    }
  }
}
template <int A, int B> DI void fm_slices(P1& p) { if constexpr (A < B) { fm_slice<A>(p); fm_slices<A + 1, B>(p); } }
template <u32 VB, int S> DI void v_handoff(const Ctx& c, const f32x16& av) {
  u32x4 vf = pack8<S>(av);
  const bool one = (c.lane & 31) == ((c.hd & 1) ? 0u : 28u);
#pragma unroll
  for (int d = 0; d < 4; ++d) vf[d] = one ? 0x3f803f80u : vf[d];
  lds_wr128(VB + c.tg * 2048 + c.l16 + S * 1024, vf);
}
template <int T> DI void phi_image(const Ctx& c, P1& p) {
  const u32 tok = c.lane & 31, h = c.lane >> 5;
  const u32 ft = (((tok >> 1) & 1) << 3) | (tok & 1) | (((tok >> 2) & 3) << 1);
  const u32 wa = SCR + c.tg * 4096 + tok * 128 + ((h ^ ft) << 3);
#pragma unroll
  for (int g = 0; g < 4; ++g) {
    u32x2 v = {p.ph[2 * T + (g >> 1)][2 * (g & 1)], p.ph[2 * T + (g >> 1)][2 * (g & 1) + 1]};
    *(LAS u32x2*)(wa ^ (u32)((8 * T + 2 * g) << 3)) = v;
  }
}
template <u32 VB, int K> DI void kv_stage(const Ctx& c, P1& p) {
  if constexpr (K >= 2 && K <= 5) {
    p.k0 = mfma(p.af[2 * K - 4], p.vf[2 * K - 4], K == 2 ? (f32x16){} : p.k0);
    p.k0 = mfma(p.af[2 * K - 3], p.vf[2 * K - 3], p.k0);
  }
  if constexpr (K == 0) {
    const u32 l = c.lane, h = l >> 5, q = (l & 15) >> 2, pp = l & 3, blk = (l >> 4) & 1;
    p.ka = SCR + (4 * h + q) * 128 + (((4 * blk + pp) ^ (q & 1) ^ (h << 1) ^ ((q >> 1) << 3) ^ ((c.tg & 1) << 3)) << 3);
    p.kb = p.ka ^ 32u; p.kc = VB + c.l16;
    asm volatile("" : "+v"(p.ka), "+v"(p.kb), "+v"(p.kc));
  }
  if constexpr (K < 4) {
#pragma unroll
    for (int s = 0; s < 2; ++s) {
      p.af[2 * K + s] = tr_frag(p.ka + K * 4096 + s * 2048, p.kb + K * 4096 + s * 2048 + 1024);
      p.vf[2 * K + s] = as_frag(lds_rd128(p.kc + K * 2048 + s * 1024));
    }
  }
  if constexpr (K == 7) {
    const u32 vo = c.tg < 2 ? c.l16 : OOB;
    st_dev(c, sanit(pack8<0>(p.k0)), vo, c.okvp + c.tg * 1024);
    st_dev(c, sanit(pack8<1>(p.k0)), vo + 1024, c.okvp + c.tg * 1024);
  }
}
DI void add8(float (&s)[8], u32x4 v) {
#pragma unroll
  for (int d = 0; d < 4; ++d) { s[2 * d] += blo(v[d]); s[2 * d + 1] += bhi(v[d]); }
}
DI void set8(float (&s)[8], u32x4 v) {
#pragma unroll
  for (int d = 0; d < 4; ++d) { s[2 * d] = blo(v[d]); s[2 * d + 1] = bhi(v[d]); }
}
template <int A, int B, int N> DI bool has_mark(const u32x4 (&v)[N]) {
  u32 m = 0;
#pragma unroll
  for (int i = A; i < B; ++i) m = umax(umax(m, v[i][0]), umax(v[i][1], umax(v[i][2], v[i][3])));
  return any_(m == 0xffffffffu);
}
template <int A, int B, int N, class F> DI void chk(u32x4 (&v)[N], F&& reload) {
  if (__builtin_expect(SPIN_MAX && has_mark<A, B>(v), 0)) {
    u32 spin = 0;
    do { asm volatile("" ::: "memory"); reload(); } while (has_mark<A, B>(v) && ++spin < SPIN_MAX);
  }
}
template <int A, int B, int N> DI void ld_lin(const Ctx& c, u32x4 (&v)[N], u32 soff, u32 stride) {
#pragma unroll
  for (int i = A; i < B; ++i) v[i] = ld_dev(c, c.l16, soff + i * stride);
}
template <int A, int B, int N> DI void ld_1k(const Ctx& c, u32x4 (&v)[N], u32 soff) {
#pragma unroll
  for (int i = A; i < B; ++i) v[i] = ld_dev(c, c.l16 + (i & 3) * 1024, soff + (i >> 2) * 4096);
}
template <int A, int B> DI void ld_h(const Ctx& c, u32x4 (&v)[14], u32 voff, u32 soff) {
#pragma unroll
  for (int i = A; i < B; ++i) v[i] = ld_dev(c, voff + (i & 1) * 1024, soff + c.rot[i >> 1] * 8192);
}
DI void o_publish(const Ctx& c, const u32x4 (&kv)[4], const u32x4 (&phq)[4], float g) {
  f32x16 n = {};
#pragma unroll
  for (int i = 0; i < 4; ++i) n = mfma(as_frag(kv[i]), as_frag(phq[i]), n);
  const bool odd = c.hd & 1;
  f32x2 t = xhalf(odd ? n[0] : n[12]);
  float sc = g * __builtin_amdgcn_rcpf((odd ? t[0] : t[1]) + 1e-6f);
#pragma unroll
  for (int i = 0; i < 16; ++i) n[i] *= sc;
  u32x4 f0 = sanit(pack8<0>(n)), f1 = sanit(pack8<1>(n));
  const bool z0 = odd && c.lane < 32, z1 = !odd && c.lane >= 32;
  f0[0] = z0 ? 0 : f0[0]; f0[1] = z0 ? 0 : f0[1];
  f1[2] = z1 ? 0 : f1[2]; f1[3] = z1 ? 0 : f1[3];
  st_dev(c, f0, c.l16, c.oo);
  st_dev(c, f1, c.l16 + 1024, c.oo);
}
DI u32 flag_off(const Ctx& c, bool spare) {
  const u32 i = c.lane & 15;
  u32 red = (c.hd * 16 + 7 + 8 * (i & 1)) << 7, sp = ((c.t * 16 + 7 + 8 * c.kh) << 7) + 8;
  u32 lo = spare ? (i * 16 + c.t) << 7 : ((c.t * 16 + (i < 7 ? 8 * c.kh + i : c.hd ^ 8)) << 7) + 8;
  asm volatile("" : "+v"(red), "+v"(sp), "+v"(lo));
  return WS_FLAG + ((c.lane & 16) ? red : (spare || i < 8) ? lo : sp);
}
DI bool flag_bad(const Ctx& c, u32x2 f) { return any_(f[0] != (u32)c.tag || f[1] != (u32)(c.tag >> 32)); }
DI void flag_spin(const Ctx& c, u32x2 f, u32 voff) {
  if (__builtin_expect(SPIN_MAX && flag_bad(c, f), 0)) {
    u32 spin = 0;
    do {
      __builtin_amdgcn_s_sleep(2);
      asm volatile("" ::: "memory");
      f = __builtin_amdgcn_raw_buffer_load_b64(c.sw, voff, 0, 16);
    } while (flag_bad(c, f) && ++spin < SPIN_MAX);
  }
  asm volatile("" ::: "memory");
}
DI void flag_publish(const Ctx& c, u32 word) {
  u32x2 v = {(u32)c.tag, (u32)(c.tag >> 32)};
  __builtin_amdgcn_raw_buffer_store_b64(v, c.sw, c.vone + word * 8, c.oflag, 16);
}
DI float ld_g(const Ctx& c, int tgroup) {
  return u2f(__builtin_amdgcn_raw_buffer_load_b32(c.sw, (c.lane & 31) * 4, c.og + tgroup * 128, 16));
}
template <u32 TOUCH_LDS> DI void touch(const Ctx& c, u32 soff) {
  if (ABL & 8) return;
  asm volatile("s_mov_b32 m0, %0\n\ts_nop 0\n\tbuffer_load_dword %1, %2, %3 offen sc1 lds"
               :: "i"(TOUCH_LDS), "v"(c.vtouch), "s"(c.sw), "s"(soff) : "memory");
}

template <u32 L> DI void pf_rows(u32 voff, rsrc_t rs, u32 soff) {
  if (ABL & 1) return;
  asm volatile("s_mov_b32 m0, %0\n\ts_nop 0\n\tbuffer_load_dword %1, %2, %3 offen lds" :: "i"(L), "v"(voff), "s"(rs), "s"(soff) : "memory");
}

constexpr int PRO = PROV;
constexpr int need_(int s) {
  return s < 0 ? 0 : s < 7 ? 3 * (s + 1) : s < 12 ? 23 + 2 * (s - 7) : s < 14 ? 34 + 2 * (s - 12) : s < 18 ? 39 + 3 * (s - 14) : s < 20 ? 50 + 2 * (s - 18) : 62;
}
constexpr int iss_(int s) {
  constexpr int t[15] = {POLL0 == 5 ? 28 : 25, POLL0 == 5 ? 32 : 30, POLL0 == 5 ? 36 : 35, POLL0 == 5 ? 40 : 39, 43, 46, 48, 49, 51, 53, 56, 59, 62, 65, 68};
  return s < 0 ? PRO : s < 6 ? imin(25, PRO + 3 * (s + 1)) : t[s - 6];
}
constexpr int ustep(int n) {
  if (n == 30 || n == 32) return 16;
  int s = 0; while (s < 20 && need_(s) <= n) ++s; return s;
}
constexpr bool ring_ok() {
  for (int s = -1; s <= 20; ++s)
    for (int n = s < 0 ? 0 : iss_(s - 1); n < iss_(s); ++n) {
      if (uslot(n) == -2 && s < 19) return false;
      for (int m = 0; m < n; ++m) if (uslot(m) == uslot(n) && ustep(m) + 2 > s) return false;
    }
  return true;
}
static_assert(ring_ok(), "ring schedule");
constexpr int S_FLAG0 = 3, S_POLL0 = POLL0, S_FLAG1 = 16, S_POLL = 17;
constexpr int oth_(int r, int s) {
  return s == 0 ? 2 - r : s == S_FLAG0 ? 1 - r : s == S_POLL0 ? 1 : s == 10 ? PF * (1 - r) : s == 11 ? 1 + PF * r : s == 12 ? 1 + PF : s == 14 ? 2 :
         s == 16 ? 3 + TOUCH1 - 3 * r : s == S_POLL ? 1 : s == 18 ? (r ? 2 : 5) : s == 19 || s == 20 ? (r ? 0 : 5) : 0;
}
constexpr int tot_(int r, int s) { return iss_(s) - iss_(s - 1) + oth_(r, s); }
constexpr int vm_(int r, int s) {
  int sp = -1; while (iss_(sp) < need_(s)) ++sp;
  int n = iss_(sp) - need_(s);
  for (int k = sp + 1; k < s; ++k) n += tot_(r, k);
  if (s == S_FLAG0) n = imin(n, tot_(r, 2) + tot_(r, 1) + 3);
  if (s == S_FLAG1) n = imin(n, tot_(r, 15));
  if (PF && s == 14) n = imin(n, tot_(r, 13));
  if (s == S_POLL0 + 1 || s == S_POLL + 1) n = imin(n, tot_(r, s - 1) - 1);
  return n;
}
constexpr int P2_(int r) { return r ? 168 : 140; }
constexpr int item0_(int r, int s) { return s < 7 ? 4 * s : (!r || s < 15) ? 8 * s - 28 : s < 18 ? 84 + 16 * (s - 14) : 8 * s; }
template <int R, int J> DI void rd_item(P1& p) {
  constexpr int U2 = P2_(R) - 28;
  if constexpr (J >= P2_(R)) {
    constexpr int k = J - P2_(R);
    if constexpr (k < 56) {
      constexpr u32 o = spos(w2s_(k / 4)) + ((k / 2) & 1) * 2048;
      p.f[J % 8] = tr_frag(sa(p.s2[k & 1], o), sa(p.s2[k & 1], o + 1024));
    } else if constexpr (k < 72) {
      constexpr int i = (k - 56) / 2, hh = i >> 1;
      constexpr u32 o = (28 * hh - 4 * (hh & 1) + 16 * (i & 1)) * 128;
      p.f[J % 8] = tr_frag(p.ob[k & 1] + o, p.ob[k & 1] + o + 1024);
    }
  } else if constexpr ((J >= 44 && J < 72) || (R == 1 && J >= 84 && J < U2 && (J & 1))) {
    constexpr int k = J < 72 ? J - 44 : (J - 84) / 2, ch = k / 4;
    constexpr u32 o = (J < 72 ? upos(25 + ch) + R * 4096 : upos(36 + 3 * (ch / 2)) + (ch & 1) * 4096) + (k % 4) * 1024;
    p.f[J % 8] = tr_frag(sa(p.aq, o), sa(p.aq, o + 256));
  } else {
    constexpr int k = J < 84 ? J : J < U2 ? (J - 84) / (1 + R) : J - U2, ch = k / 4;
    constexpr u32 o = upos(J < 28 ? 3 * ch + 2 : J < 44 ? 14 + ch : J < 84 ? 15 + ch : J < U2 ? 37 + 3 * (ch / 2) + (ch & 1) : 47 + ch) + (k % 4) * 2048;
    p.f[J % 8] = tr_frag(sa(p.aw, o), sa(p.aw, o + 512));
  }
}
template <int R, int J> DI void mm_item(P1& p) {
  constexpr int U2 = P2_(R) - 28;
  if constexpr (J < 0) {}
  else if constexpr (J >= P2_(R)) {
    constexpr int k = J - P2_(R);
    p.c[k & 1] = mfma(p.f[J % 8], as_frag(k < 28 ? p.h1[(k / 2) % 14] : k < 56 ? p.h2[(k / 2) % 14] : p.of[(k / 2 + 4) % 8]), p.c[k & 1]);
  } else if constexpr (J >= 44 && J < 72) {
    if constexpr (R == 1) p.a1 = mfma(p.xf[J - 44], p.f[J % 8], p.a1); else p.a0 = mfma(p.f[J % 8], p.xf[J - 44], p.a0);
  } else {
    f32x16& acc = J < 28 ? p.g1 : J < 84 ? p.u1 : J >= U2 ? p.u2 : (R == 1 && (J & 1)) ? p.a0 : p.g2;
    acc = mfma(p.f[J % 8], p.xf[J < 84 ? J % 28 : J < U2 ? (J - 84) / (1 + R) : J - U2], acc);
  }
}
template <int R, int A, int B> DI void mm_range(P1& p) { if constexpr (A < B) { mm_item<R, A>(p); mm_range<R, A + 1, B>(p); } }

constexpr float GK1 = -2.0f * 0.7978845608028654f * LOG2E, GK2 = GK1 * 0.044715f;
template <int OP> DI void gelu_op(float (&y)[16], const f32x16& g) {
  constexpr int st = OP % 4, i = OP / 4 - st;
  if constexpr (i >= 0 && i < 16) {
    if constexpr (st == 0) y[i] = g[i] * __builtin_fmaf(g[i] * g[i], GK2, GK1);
    else if constexpr (st == 1) y[i] = __builtin_amdgcn_exp2f(y[i]);
    else if constexpr (st == 2) y[i] = __builtin_amdgcn_rcpf(1.0f + y[i]);
    else y[i] = g[i] * y[i];
    PIN1(y[i]);
  }
}
constexpr int gcost(int op) { return op / 4 - op % 4 < 0 || op / 4 - op % 4 > 15 ? 0 : op % 4 == 0 ? 3 : op % 4 == 1 ? 2 : op % 4 == 2 ? 3 : 1; }
constexpr int gcum(int k, int n) { if (k >= n) return 76; int op = 0, cst = 0; while (cst * n < k * 144) cst += gcost(op++); return op; }
template <int A, int B> DI void gelu_ops(float (&y)[16], const f32x16& g) { if constexpr (A < B) { gelu_op<A>(y, g); gelu_ops<A + 1, B>(y, g); } }
template <int S> DI void h_store(const Ctx& c, const float (&y)[16], const f32x16& u, int blk) {
  float h[8];
#pragma unroll
  for (int i = 0; i < 8; ++i) h[i] = y[8 * S + i] * u[8 * S + i];
  st_dev(c, pack8<0>(h), c.l16 + S * 1024, c.oh + blk * 4096);
}
template <int R, int S, int K, int J> DI void slot_work(Ctx& c, P1& p) {
  if constexpr (S < 7) p.xf[J] = as_frag(lds_rd128((p.ax + upos(3 * S)) ^ (u32)(32 * K)));
  if constexpr (R == 0) {
    if constexpr (J >= 33 && J < 75) gelu_ops<gcum(J - 33, 42), gcum(J - 32, 42)>(p.y, p.g1);
    if constexpr (J == 70) wphi_rd(c, p);
    if constexpr (J == 76) wphi_pack(p);
    constexpr int F0 = J < 84 ? J - 77 : J < 89 ? 7 + (3 * (J - 84) + 1) / 2 : J < 91 ? 15 : 15 + (9 * (J - 91) + 7) / 8;
    constexpr int F1 = J + 1 < 84 ? J - 76 : J + 1 < 89 ? 7 + (3 * (J - 83) + 1) / 2 : J + 1 < 91 ? 15 : 15 + (9 * (J - 90) + 7) / 8;
    if constexpr (J >= 77 && J < 99) fm_slices<F0, imin(24, F1)>(p);
    if constexpr (J == 92) phi_image<0>(c, p);
    if constexpr (J == 99) phi_image<1>(c, p);
    if constexpr (S == 16) kv_stage<VBUF, K>(c, p);
    if constexpr (S == 18 && (K & 1)) ld_h<K / 2, K / 2 + 1>(c, p.h1, c.l16, p.hbase);
    if constexpr (S >= 19 && ((K & 1) || K == 4)) { constexpr int i = 4 + 5 * (S - 19) + (K < 4 ? K / 2 : (K + 1) / 2); ld_h<i, i + 1>(c, p.h1, c.l16, p.hbase); }
  } else {
    if constexpr (J >= 33 && J < 77) gelu_ops<gcum(J - 33, 44), gcum(J - 32, 44)>(p.y, p.g1);
    if constexpr (J == 77 || J == 78) v_handoff<VBUF, J - 77>(c, p.a1);
    if constexpr (J == 79) wphi_rd(c, p);
    if constexpr (J == 83) wphi_pack(p);
  }
  if constexpr (J == 89 || J == 90) h_store<J - 89>(c, p.y, p.u1, 0);
  if constexpr (TOUCH1 && S == S_FLAG1 && K == 0) touch<upos(45) + 4096>(c, c.oh);
  if constexpr (J >= P2_(R) - 23) gelu_ops<gcum(J - P2_(R) + 23, 27), gcum(J - P2_(R) + 24, 27)>(p.y, p.g2);
  if constexpr ((S == 11 || S == 12) && K == 5 - 4 * R) st_dev(c, (u32x4){~0u, ~0u, ~0u, ~0u}, c.l16 + (2 * R + S - 11) * 1024, c.op);
  if constexpr (PF && S == 10 + R && K == 2) pf_rows<spos(-2)>(c.lane < 28 ? (c.lane + 28 * c.w) * 896 : OOB, c.sd, (c.t & 3) * (224 * 896));
  if constexpr (PF && S == 12 && K == 3) pf_rows<spos(-2)>(c.lane < 7 ? (c.lane + 7 * c.w) * 896 : OOB, c.so, (c.t & 3) * (56 * 896));
  if constexpr (S == 18 && K == 0) { p.gt = ld_g(c, p.TG); if constexpr (R == 1) p.gq = ld_g(c, c.tg); }
}
template <int R, int S, int J0, int K, int N> DI void slots(Ctx& c, P1& p) {
  if constexpr (K < N) {
    constexpr int nu = iss_(S) - iss_(S - 1), u0 = iss_(S - 1);
    constexpr int ua = K == 0 ? 0 : ((2 * K + R) * nu) / (2 * N), ub = K == N - 1 ? nu : ((2 * K + 2 + R) * nu) / (2 * N);
    if constexpr (R == 1 && PRIO && S >= 18 && (K == 0 || K == 4)) __builtin_amdgcn_s_setprio(K == 0);
    mm_item<R, J0 + K - 4>(p);
    if constexpr (ub > ua) unit_m0<u0 + ua>(c);
    rd_item<R, (J0 + K) ^ 1>(p);
    if constexpr (ub > ua) { unit_go<R, u0 + ua>(c); issue_range<R, u0 + ua + 1, u0 + ub>(c); }
    slot_work<R, S, K, J0 + K>(c, p);
    SB0();
    slots<R, S, J0, K + 1, N>(c, p);
  }
}
template <int R, int S> DI void step(Ctx& c, P1& p) {
  if constexpr (S == S_POLL + 1) {
    asm volatile("s_waitcnt vmcnt(%1)\n\ts_barrier" : "+v"(p.poll) : "n"(vm_(R, S)) : "memory");
    PIN2(p.g2, p.a0);
    flag_spin(c, p.poll, c.vflag);
  } else if constexpr (S == S_POLL0 + 1) {
    asm volatile("s_waitcnt vmcnt(%1)\n\ts_barrier" : "+v"(p.poll) : "n"(vm_(R, S)) : "memory");
    PIN1(p.g1);
    flag_spin(c, p.poll, c.vflag0);
  } else if constexpr (S == 0) {
    wait_bar_v<vm_(R, S)>();
    st_dev(c, lds_rd128(spos(17) + c.m0w + c.l16), c.vrp, 0);
    if constexpr (R == 0) st_dev(c, (u32x4){~0u, ~0u, ~0u, ~0u}, c.l16, c.okvp);
  } else if constexpr (S == 16 && R == 0) wait_bar<vm_(R, S)>();
  else wait_bar_v<vm_(R, S)>();
  if constexpr (S < 2 || S >= 6) STAMP(S < 2 ? S + 1 : S - 3);
  if constexpr (S == S_FLAG0 && R == 0) flag_publish(c, 0);
  if constexpr (S == S_FLAG1 && R == 0) flag_publish(c, 1);
  if constexpr (S == S_POLL || S == S_POLL0) asm volatile("buffer_load_dwordx2 %0, %1, %2, 0 offen sc1" : "=v"(p.poll) : "v"(S == S_POLL ? c.vflag : c.vflag0), "s"(c.sw) : "memory");
  SB0();
  slots<R, S, item0_(R, S), 0, item0_(R, S + 1) - item0_(R, S)>(c, p);
}
template <int R, int S, int E> DI void steps(Ctx& c, P1& p) { if constexpr (S < E) { step<R, S>(c, p); steps<R, S + 1, E>(c, p); } }
template <int R, int A, int B, int RE, class F> DI void run(P1& p, F&& extra) {
  if constexpr (A < B) {
    mm_item<R, A - 4>(p);
    if constexpr (A < RE) rd_item<R, A ^ 1>(p);
    extra(std::integral_constant<int, A>{});
    SB0();
    run<R, A + 1, B, RE>(p, extra);
  } else { PIN2(p.c[0], p.c[1]); }
}
template <int R, int K, class F> DI void fmq(P1& p, F&& extra) {
  if constexpr (K < 24) { fm_slice<K>(p); extra(std::integral_constant<int, K>{}); SB0(); fmq<R, K + 1>(p, extra); }
}

template <int R> DI void ffn_wg(Ctx& c, P1& p) {
  const u32 l = c.lane;
  const int tg2 = c.tg & 1, ks = c.tg >> 1;
  p.TG = 2 * (R == 1 ? c.kh : 1 - c.kh) + tg2;
  p.hbase = WS_H + (u32)(((c.t * 4 + p.TG) * 112 + 56 * c.kh + 2 * ks) * 1024);
  asm volatile("" : "+s"(p.hbase));
  PIN1(c.awp);
  p.g1 = p.u1 = p.a0 = p.a1 = p.g2 = p.u2 = p.c[0] = p.c[1] = (f32x16){};
  steps<R, 0, 21>(c, p);
  STAMP(18);
  const u32 qq = (l & 15) >> 2, pp_ = l & 3, blk = (l >> 4) & 1, h = l >> 5;
  const u32 b0 = (4 * h + qq) * 128 + ((qq >> 1) << 6) + 32 * blk + 8 * pp_;
  const u32 bk = b0 ^ (64 * ks);
  p.s2[0] = mkseg(bk + ks * 4096); p.s2[1] = mkseg((bk ^ 64) + ks * 4096);
  p.ob[0] = bk + ks * (112 * 128); p.ob[1] = (bk ^ 64) + ks * (112 * 128);
  PIN2(p.ob[0], p.ob[1]);
  auto none = [](auto) {};
  constexpr int B = P2_(R);
  auto epi2 = [&](auto J) {
    if constexpr (J.value < B + 4) gelu_ops<gcum(J.value - B + 23, 27), gcum(J.value - B + 24, 27)>(p.y, p.g2);
    if constexpr (J.value == B + 5 || J.value == B + 6) h_store<J.value - B - 5>(c, p.y, p.u2, 1);
  };
  const u32 obase = WS_O + (u32)(((c.t * 4 + p.TG) * 16 + 8 * c.kh + 4 * ks) * 2048);
  const u32 l16h = c.l16 + 4096, vflag2 = c.vflag + ((l & 31) < 7 ? 8 : 0);
  if constexpr (R == 0) {
    run<R, 140, 140 + XP, 999>(p, epi2);
    STAMP(19);
    wait_bar_v<0>();
    STAMP(20);
    flag_publish(c, 2);
    if (TOUCH2) touch<spos(17)>(c, c.oh + 4096);
    run<R, 140 + XP, 172, 999>(p, [&](auto J) { if constexpr (J.value >= 164) unit_half<68 + (J.value - 164) / 2, J.value & 1>(c); });
    STAMP(21);
    __builtin_amdgcn_s_sleep(PSLEEP);
    flag_spin(c, __builtin_amdgcn_raw_buffer_load_b64(c.sw, vflag2, 0, 16), vflag2);
    STAMP(22);
    ld_h<0, 6>(c, p.h2, l16h, p.hbase);
    asm volatile("s_barrier" ::: "memory");
    STAMP(23);
    run<R, 172, 200, 999>(p, [&](auto J) {
      if constexpr (J.value < 180) ld_h<J.value - 166, J.value - 165>(c, p.h2, l16h, p.hbase);
      if constexpr (J.value >= OPOS && J.value < OPOS + 8) ld_1k<J.value - OPOS, J.value - OPOS + 1>(c, p.of, obase);
    });
  } else {
    __builtin_amdgcn_s_setprio(1);
    run<R, 168, 172, 999>(p, epi2);
    fm_slices<0, 2>(p);
    SB0();
    h_store<0>(c, p.y, p.u2, 1); h_store<1>(c, p.y, p.u2, 1);
    SB0();
    STAMP(19);
    fmq<R, 3>(p, [&](auto K) {
      if constexpr (K.value == XQ) {
        wait_bar_v<0>();
        if (TOUCH2) touch<spos(17)>(c, c.oh + 4096);
        ld_1k<0, 4>(c, p.kv, WS_KVR + (u32)c.hd * 4096);
      }
      if constexpr (K.value > XQ && K.value <= XQ + H1Q) ld_h<K.value - XQ - 1, K.value - XQ>(c, p.h1, c.l16, p.hbase);
    });
    STAMP(21);
    chk<0, 4>(p.kv, [&] { ld_1k<0, 4>(c, p.kv, WS_KVR + (u32)c.hd * 4096); });
    o_publish(c, p.kv, p.ph, p.gq);
    __builtin_amdgcn_s_setprio(0);
    STAMP(22);
    u32x2 f2;
    run<R, 172, 172 + YP, 999>(p, [&](auto J) {
      if constexpr (J.value - 172 + H1Q < 14) ld_h<J.value - 172 + H1Q, J.value - 171 + H1Q>(c, p.h1, c.l16, p.hbase);
      if constexpr (J.value == 171 + YP) f2 = __builtin_amdgcn_raw_buffer_load_b64(c.sw, vflag2, 0, 16);
    });
    asm volatile("s_barrier" ::: "memory");
    STAMP(23);
    run<R, 172 + YP, 186, 999>(p, none);
    flag_spin(c, f2, vflag2);
    run<R, 186, 228, 999>(p, [&](auto J) {
      if constexpr (J.value < 200) ld_h<J.value - 186, J.value - 185>(c, p.h2, l16h, p.hbase);
      if constexpr (J.value >= 204 && J.value < 220 && !(J.value & 1)) ld_1k<(J.value - 204) / 2, (J.value - 202) / 2>(c, p.of, obase);
    });
  }
  const float om = 1.0f - p.gt;
#pragma unroll
  for (int i = 0; i < 16; ++i) { p.c[0][i] *= om; p.c[1][i] *= om; }
  PIN2(p.c[0], p.c[1]);
  STAMP(27);
  chk<0, 8>(p.of, [&] { ld_1k<0, 8>(c, p.of, obase); });
  STAMP(28);
  run<R, B + 60, B + 76, 999>(p, none);
  f32x16 &c0 = p.c[0], &c1 = p.c[1];
  STAMP(29);
  if constexpr (R == 0) {
    st_dev(c, sanit(pack8<0>(c0)), c.l16, c.op + ks * 2048);
    st_dev(c, sanit(pack8<1>(c0)), c.l16 + 1024, c.op + ks * 2048);
    st_dev(c, sanit(pack8<0>(c1)), c.l16, c.op + 2048 - ks * 2048);
    st_dev(c, sanit(pack8<1>(c1)), c.l16 + 1024, c.op + 2048 - ks * 2048);
    STAMP(30);
  } else {
    u32x4 pp[4];
    const u32 so = ((c.op ^ (8 * 16384)) & ~8192u) + ks * 2048;
    auto ld_p = [&] {
#pragma unroll
      for (int i = 0; i < 4; ++i) pp[i] = ld_dev(c, c.l16 + (i & 1) * 1024, so + (i >> 1) * 8192);
    };
    ld_p();
    f32x16 &keep = c0, &give = c1;
    const u32 xa = spos(16) + tg2 * 4096 + c.l16;
#pragma unroll
    for (int g = 0; g < 4; ++g) *(LAS f32x4*)(xa + ks * 8192 + g * 1024) = (f32x4){give[4 * g], give[4 * g + 1], give[4 * g + 2], give[4 * g + 3]};
    bar_l();
#pragma unroll
    for (int g = 0; g < 4; ++g) {
      f32x4 v = *(LAS f32x4*)(xa + (1 - ks) * 8192 + g * 1024);
#pragma unroll
      for (int i = 0; i < 4; ++i) keep[4 * g + i] += v[i];
    }
    PIN1(keep);
    chk<0, 4>(pp, ld_p);
    STAMP(30);
#pragma unroll
    for (int d = 0; d < 4; ++d) {
      keep[2 * d] += blo(pp[0][d]) + blo(pp[2][d]); keep[2 * d + 1] += bhi(pp[0][d]) + bhi(pp[2][d]);
      keep[8 + 2 * d] += blo(pp[1][d]) + blo(pp[3][d]); keep[9 + 2 * d] += bhi(pp[1][d]) + bhi(pp[3][d]);
    }
    char* orow = c.out + (size_t)(c.t * 128 + c.kh * 64 + tg2 * 32 + (l & 31)) * 896 + c.jj * 128 + ks * 64 + h * 16;
#pragma unroll
    for (int s = 0; s < 2; ++s) {
      u32x4 v = s ? pack8<1>(keep) : pack8<0>(keep);
      auto a = __builtin_amdgcn_permlane32_swap(v[0], v[2], false, false);
      auto b = __builtin_amdgcn_permlane32_swap(v[1], v[3], false, false);
      *(u32x4*)(orow + s * 32) = (u32x4){a[0], b[0], a[1], b[1]};
    }
    if (__builtin_amdgcn_workgroup_id_x() == 0 && c.w == 4) {
      u32x2 v = {(u32)c.tag, (u32)(c.tag >> 32)};
      __builtin_amdgcn_raw_buffer_store_b64(v, c.sw, l == 0 ? 0u : OOB, 0, 16);
    }
    STAMP(31);
  }
}

template <int R, int A0, int A, int B, int ST> DI void pipe(P1& p) {
  if constexpr (A - 3 * ST < B) {
    if constexpr (A - 3 * ST >= A0) mm_item<R, A - 3 * ST>(p);
    if constexpr (A < B) rd_item<R, A>(p);
    SB0();
    pipe<R, A0, A + ST, B, ST>(p);
  }
}
constexpr u32 sxpos(int ch) { return spos(2 * ch - 2); }
template <int N, int E> DI void spare_x(const Ctx& c) {
  if constexpr (N < E) { set_m0<sxpos(N >> 1) + (N & 1) * UNIT>(c); asm volatile("s_nop 0"); dma<0>((N & 1) ? c.vx2 : c.vx, c.sx, c.rot[N >> 1] * 128); spare_x<N + 1, E>(c); }
}
template <int R> DI void spare_wg(Ctx& c, P1& p) {
  const u32 l = c.lane;
  set_m0<spos(12)>(c); asm volatile("s_nop 0"); dma<0>(c.l16, mk(c.gw, 896), c.w == 0 ? 0u : 0x40000000u);
  const u32 okvr = WS_KVR + (u32)(c.t * 4096 + (2 * c.kh + (c.w & 1)) * 1024);
  const u32x4 ones = {~0u, ~0u, ~0u, ~0u};
  st_dev(c, ones, R == 0 || c.tg < 2 ? c.l16 : OOB, R == 0 ? c.okvp : okvr);
  asm volatile("s_waitcnt vmcnt(6)" ::: "memory");
  st_dev(c, lds_rd128(spos(17) + c.m0w + c.l16), c.vrp, 0);
  spare_x<4, 14>(c);
  wait_bar<0>();
  if constexpr (R == 0) flag_publish(c, 0);
  {
    const u32 mo = WS_O + ((c.t * 4 + c.tg) * 16 + 8 * c.kh + 4 * R) * 2048;
#pragma unroll
    for (int i = 0; i < 8; ++i) st_dev(c, ones, c.l16 + (i & 3) * 1024, mo + (i >> 2) * 4096);
  }
#pragma unroll
  for (int s = 0; s < 28; ++s) p.xf[s] = as_frag(lds_rd128((p.ax ^ (u32)(32 * (s & 3))) + sxpos(s >> 2)));
  STAMP(1);
  f32x16 ga = {};
#pragma unroll
  for (int s = 14 * R; s < 14 * R + 14; ++s) ga = mfma(as_frag(lds_rd128(spos(12) + c.rot[s >> 2] * 128 + (s & 3) * 32 + (l >> 5) * 16)), p.xf[s], ga);
  PIN1(ga);
  flag_spin(c, __builtin_amdgcn_raw_buffer_load_b64(c.sw, c.vflag0, 0, 16), c.vflag0);
  bar_l();
  issue_range<R, 25, 33>(c);
  issue_range<R, 36, 37>(c); issue_range<R, 39, 40>(c); issue_range<R, 42, 43>(c);
  p.a0 = p.a1 = (f32x16){};
  float g1 = 0.f;
  if constexpr (R == 0) *(LAS float*)(spos(0) + c.tg * 256 + l * 4) = ga[0];
  wait_bar<7>();
  STAMP(2);
  if constexpr (R == 1) {
    g1 = __builtin_amdgcn_rcpf(1.0f + __builtin_amdgcn_exp2f(-LOG2E * (ga[0] + *(LAS float*)(spos(0) + c.tg * 256 + l * 4))));
    __builtin_amdgcn_raw_buffer_store_b32(f2u(g1), c.sw, l < 32 ? l * 4 : OOB, c.og + c.tg * 128, 16);
  }
  pipe<R, 44, 44, 60, 1>(p);
  wait_bar_v<0>();
  if constexpr (R == 0) flag_publish(c, 1);
  issue_range<R, 45, 46>(c);
  wphi_rd(c, p);
  pipe<R, 60, 60, 72, 1>(p);
  wphi_pack(p);
  wait_bar_v<0>();
  if constexpr (R == 0) { fm_slices<0, 24>(p); phi_image<0>(c, p); phi_image<1>(c, p); }
  else { v_handoff<spos(1), 0>(c, p.a1); v_handoff<spos(1), 1>(c, p.a1); pipe<R, 85, 85, 140, 2>(p); PIN1(p.a0); }
  bar_l();
  if constexpr (R == 0) {
    if (c.w >= 2) return;
    kv_stage<spos(1), 0>(c, p); kv_stage<spos(1), 1>(c, p); kv_stage<spos(1), 2>(c, p); kv_stage<spos(1), 3>(c, p);
    kv_stage<spos(1), 4>(c, p); kv_stage<spos(1), 5>(c, p); kv_stage<spos(1), 7>(c, p);
    STAMP(3);
    flag_spin(c, __builtin_amdgcn_raw_buffer_load_b64(c.sw, c.vflag, 0, 16), c.vflag);
    STAMP(4);
    u32x4 v[16];
    const u32 ro = WS_KVP + (u32)(c.t * 65536 + (2 * c.kh + c.w) * 1024);
    ld_lin<0, 16>(c, v, ro, 4096);
    chk<0, 16>(v, [&] { ld_lin<0, 16>(c, v, ro, 4096); });
    STAMP(5);
    float s[8], s2[8];
    set8(s, v[0]); set8(s2, v[1]);
#pragma unroll
    for (int i = 2; i < 16; i += 2) { add8(s, v[i]); add8(s2, v[i + 1]); }
#pragma unroll
    for (int i = 0; i < 8; ++i) s[i] += s2[i];
    st_dev(c, sanit(pack8<0>(s)), c.l16, okvr);
    STAMP(6);
  } else {
    fm_slices<0, 24>(p);
    flag_spin(c, __builtin_amdgcn_raw_buffer_load_b64(c.sw, c.vflag, 0, 16), c.vflag);
    u32x4 v[4];
    const u32 kvo = WS_KVR + (u32)c.hd * 4096;
    ld_1k<0, 4>(c, v, kvo);
    chk<0, 4>(v, [&] { __builtin_amdgcn_s_sleep(8); ld_1k<0, 4>(c, v, kvo); });
    o_publish(c, v, p.ph, g1);
    STAMP(7);
  }
}

extern "C" __global__ __launch_bounds__(512, 2) void fused_kernel(const char* x, const char* wqkv, const char* wphi, const char* wo, const char* wg,
                                                                   const char* wu, const char* wd, const char* gw, char* out, char* ws) {
  Ctx c;
  const int bid = __builtin_amdgcn_workgroup_id_x(), xcd = bid & 7, idx = bid >> 3;
  const u32 tid = __builtin_amdgcn_workitem_id_x();
  c.gw = gw; c.out = out; c.ws = ws;
  c.t = 4 * (xcd >> 1) + (idx & 3); c.kh = xcd & 1; c.jj = idx >> 2; c.hd = 8 * c.kh + c.jj; c.j = 7 * c.kh + c.jj;
  c.w = __builtin_amdgcn_readfirstlane(tid >> 6); c.tg = c.w & 3; c.r = c.w >> 2;
  c.lane = tid & 63; c.l16 = c.lane * 16; c.m0w = c.w * 1024;
  const u32 l = c.lane, w = c.w;
  STAMP(0);
  const u64 rt = 0x2106543210654321ull, r0 = c.jj + 2 * (idx & 3) + (xcd >> 1);
  const u32 rs = (u32)(rt >> (4 * (r0 > 9 ? r0 - 7 : r0)));
#pragma unroll
  for (int i = 0; i < 7; ++i) c.rot[i] = (rs >> (4 * i)) & 7;
  const u32 row8 = 8 * w + (l >> 3), sw2 = ((l & 7) ^ (((l >> 4) & 1) << 2)) << 4;
  c.vx = row8 * 896 + (((l & 7) ^ ((4 * w + (l >> 4)) & 7)) << 4); c.vx2 = c.vx + 64 * 896;
  c.vw = row8 * 3584 + sw2;
  c.sx = mk(x + c.t * 114688, 114688);
  c.sg = mk(wg + c.j * 256, 448 * 3584 - c.j * 256);
  {
    const u32 win = 56 * c.hd - 8 * (c.hd & 1), i = (w & 1) * 64 + l;
    const bool ok = i < 112 && w < 6;
    set_m0<spos(17)>(c); asm volatile("s_nop 0");
    dma<0>(ok ? (i >> 2) * 2688 + (i & 3) * 16 : OOB, mk(wqkv, 448 * 2688), c.t * (28 * 2688) + (w >> 1) * 896 + win);
    c.vrp = ok ? WS_WA + c.hd * 86016 + (w >> 1) * 28672 + c.t * 1792 + i * 16 : OOB;
  }
  if (c.jj < 7) issue_range<0, 0, PRO>(c);
  else {
    u32 lo; u64 pc;
    asm volatile("s_getpc_b64 %0\n.Lpf_here:\n\ts_mov_b32 %1, .Lpf_here-fused_kernel" : "=s"(pc), "=s"(lo));
    asm volatile("s_mov_b32 m0, %0\n\ts_nop 0\n\tbuffer_load_dword %1, %2, 0 offen lds"
                 :: "i"(spos(13)), "v"((w * 64 + l) * 128), "s"(mk((const char*)(pc - lo), CODE_BYTES)) : "memory");
    spare_x<0, 4>(c);
  }
  SB0();
  c.tag = *(const __attribute__((address_space(4))) u64*)ws + 1;
  c.v2 = row8 * 896 + sw2; c.v2b = c.v2 + 64 * 896;
  c.vq = WS_WA + c.hd * 86016 + c.tg * 1024 + c.l16; c.vkv = c.vq + 28672 * (1 + c.r);
  c.vflag0 = WS_FLAG + (((l & 15) * 16 + c.hd) << 7);
  c.sp = mk(wphi + c.hd * 1568, 1568);
  c.sw = mk(ws, WS_END);
  c.oh = WS_H + (u32)(((c.t * 4 + c.tg) * 112 + 8 * c.j + 2 * c.r) * 1024);
  c.oo = WS_O + (u32)(((c.t * 4 + c.tg) * 16 + c.hd) * 2048);
  c.op = WS_P + (u32)((c.t * 16 + c.hd) * 16384 + c.tg * 4096);
  c.okvp = WS_KVP + (u32)((c.hd * 16 + c.t) * 4096 + c.tg * 1024);
  c.oflag = WS_FLAG + (u32)(c.t * 16 + c.hd) * 128;
  c.og = WS_G + (u32)(c.kh * 2048 + c.t * 128) * 4;
  static_assert(WS_P % (16 * 16384) == 0, "the partner's slot is op ^ (8 * 16384)");
  c.vflag = flag_off(c, c.jj == 7); c.vtouch = l < 16 ? l * 128 : OOB; c.vone = tid == 0 ? 0u : OOB;
  { const u32 rho = l & 31, f = rho < 26 ? rho : ((rho == 28 || rho == 29) ? rho - 2 : 0);
    c.awp = upos(32) + 5 * 1024 + 2 * (28 * (4 * (l >> 5) - 4 * (c.hd & 1)) + f); }
  asm volatile("" : "+s"(c.tag), "+s"(c.oh), "+s"(c.oo), "+s"(c.op), "+s"(c.okvp), "+s"(c.oflag), "+s"(c.og) :: "memory");
  asm volatile("" : "+v"(c.vflag), "+v"(c.vflag0), "+v"(c.vtouch), "+v"(c.vone), "+v"(c.vrp));
  P1 p;
  {
    const u32 q = (l & 15) >> 2, pp = l & 3, blk = (l >> 4) & 1, h = l >> 5, r = l & 31;
    p.aw = mkseg((8 * h + q) * 128 + ((64 * c.r) ^ ((q >> 1) << 6)) + 32 * blk + 8 * pp);
    p.aq = mkseg((8 * h + q) * 64 + 32 * blk + 8 * pp);
    p.ax = (32 * c.tg + r) * 128 + ((h ^ ((r >> 1) & 7)) << 4);
  }
  if (c.jj < 7) {
    c.su = mk(wu + c.j * 256, 448 * 3584 - c.j * 256);
    c.sd = mk(wd + c.kh * 896 * 896 + c.jj * 128, 896 * 896 - c.jj * 128);
    c.so = mk(wo + c.kh * 224 * 896 + c.jj * 128, 224 * 896 - c.jj * 128);
    if (c.r == 0) { ffn_wg<0>(c, p); __builtin_amdgcn_endpgm(); }
    ffn_wg<1>(c, p);
    __builtin_amdgcn_endpgm();
  }
  if (c.r == 0) { spare_wg<0>(c, p); __builtin_amdgcn_endpgm(); }
  spare_wg<1>(c, p);
}

#if defined(__HIP_DEVICE_COMPILE__)
#pragma clang attribute pop
#endif

__global__ void amp_k(const unsigned long long* st){
  unsigned tnow=(unsigned)__builtin_amdgcn_s_memrealtime(); asm volatile("":"+s"(tnow)::"memory");
  unsigned mn=~0u;
  for(int i=threadIdx.x;i<2048;i+=64){ unsigned a=(unsigned)st[i*32]; mn=a<mn?a:mn; }
  for(int o=32;o;o>>=1){ unsigned b=__shfl_xor(mn,o); mn=b<mn?b:mn; }
  unsigned sum=0,mx=0,cnt=0;
  for(int b=threadIdx.x;b<256;b+=64){ bool sp=(b>>5)==7; if(sp!=('a'=='s')) continue; unsigned d=(unsigned)st[(b*8+4)*32+PROBE_K]-mn; if(d<100000){ sum+=d; mx=d>mx?d:mx; cnt++; } }
  for(int o=32;o;o>>=1){ sum+=__shfl_xor(sum,o); cnt+=__shfl_xor(cnt,o); unsigned b=__shfl_xor(mx,o); mx=b>mx?b:mx; }
  unsigned long long m = 'a'=='z' ? 0 : 'a'=='e' ? (unsigned long long)(tnow-mn-mx)*100 : 'a'=='m' ? (unsigned long long)mx*100 : (unsigned long long)sum*100/(cnt?cnt:1);
  unsigned long long t0=__builtin_amdgcn_s_memrealtime();
  while(__builtin_amdgcn_s_memrealtime()-t0<m) __builtin_amdgcn_s_sleep(4);
}
extern "C" void kernel_launch(void* const* d_in, const int* in_sizes, int n_in, void* d_out, int out_size, void* d_ws, size_t ws_size,
                              hipStream_t stream) {
  (void)hipFuncSetAttribute((const void*)fused_kernel, hipFuncAttributeMaxDynamicSharedMemorySize, LDS_BYTES);
  fused_kernel<<<256, 512, LDS_BYTES, stream>>>((const char*)d_in[0], (const char*)d_in[1], (const char*)d_in[2], (const char*)d_in[3],
                                                (const char*)d_in[5], (const char*)d_in[6], (const char*)d_in[7], (const char*)d_in[8],
                                                (char*)d_out, (char*)d_ws);
  amp_k<<<1,64,0,stream>>>((const unsigned long long*)((char*)d_ws + WS_STAMP));
}
```

```cpp
#include <hip/hip_runtime.h>
#include <hip/hip_bf16.h>
#include <type_traits>
#pragma clang diagnostic ignored "-Wint-to-pointer-cast"

typedef unsigned u32;
typedef unsigned long long u64;
typedef u32 u32x4 __attribute__((ext_vector_type(4)));
typedef u32 u32x2 __attribute__((ext_vector_type(2)));
typedef short s16x4 __attribute__((ext_vector_type(4)));
typedef __bf16 bf16x8 __attribute__((ext_vector_type(8)));
typedef __bf16 bf16x2 __attribute__((ext_vector_type(2)));
typedef float f32x16 __attribute__((ext_vector_type(16)));
typedef float f32x4 __attribute__((ext_vector_type(4)));
typedef float f32x2 __attribute__((ext_vector_type(2)));
typedef __amdgpu_buffer_rsrc_t rsrc_t;
#define DI __device__ __forceinline__
#define LAS __attribute__((address_space(3)))
#define SB0() __builtin_amdgcn_sched_barrier(0)
#define PIN1(a) asm volatile("" : "+v"(a))
#define PIN2(a, b) asm volatile("" : "+v"(a), "+v"(b))

constexpr u32 WS_FLAG = 4096;
constexpr u32 WS_G    = 131072;
constexpr u32 WS_KVR  = 65536;
constexpr u32 WS_KVP  = 262144;
constexpr u32 WS_O    = WS_KVP + 256 * 4096;
constexpr u32 WS_P    = WS_O + 64 * 32 * 1024;
constexpr u32 WS_H    = WS_P + 256 * 16384;
constexpr u32 WS_WA   = WS_H + 64 * 112 * 1024;
constexpr u32 WS_END  = WS_WA + 16 * 3 * 28672;

constexpr int UNIT = 8192, RING = 18;
constexpr u32 SCR = 0, RBASE = 2 * UNIT;
constexpr int LDS_BYTES = 163840;
constexpr u32 OOB = 0x80000000u;
constexpr u32 CODE_BYTES = 40960;
#define ABL 0
#define ORD 1
#define A4 9
#define SWP 1
#define DELAYS 99
#define NOGELU 0
#define NOFM 0
#define I7 33
#define I8 36
#define I9 38
#define I10 41
#define I11 44
#define I12 47
#define I18 62
#define NOMM 0
#define RJ 1
#define RT 2
#define RX 1
#define RK 0
#define XB 0
#define PH 7
#define OL 1
#define OUTAUX 0
#define NB0 10
#define C2 196
#define P2 186
#define NPRE 4
#define NB1 2
#define YR1 199
#define O1 204
#define PROV 9
#define POLL0 5
#define POLL1 17
#define GATE 11
#define PFS 0
#define TOUCH1 0
#define TOUCH2 0
#define XP 24
#define XQ 14
#define H1Q 6
#define H1R 10
#define PSLEEP 4
#define OPOS 184
#define YP 10
#define CHK1 19
#define FLAG0 2
#define I19 65
#define I20 68
constexpr u32 SPIN_MAX = ABL ? 0 : 1u << 16;
constexpr float LOG2E = 1.4426950408889634f;

#if defined(__HIP_DEVICE_COMPILE__)
#pragma clang attribute push(__attribute__((target("no-packed-fp32-ops"))), apply_to = function)
#endif
constexpr u32 spos(int k) { return RBASE + k * UNIT; }
constexpr int w2c_(int i) { constexpr int t[18] = {16, 17, 0, 1, 2, 3, 4, 5, 6, 7, 8, 9, 10, 11, 12, 13, 14, 15}; return t[i]; }
constexpr int w2s_(int c) { constexpr int t[18] = {2, 3, 4, 5, 6, 10, 11, 12, 13, 15, 14, 16, 7, 8, -2, -1, 0, 1}; return t[c]; }
constexpr int uslot(int n) {
  constexpr int t[26] = {-1, 10, 11, 12, 13, 14, 15, 16, 17, 0, 1, 2, 3, 4, 5, 6, 10, 11, 12, 13, 14, 16, 7, 8, 9, 17};
  return n < 28 ? n % RING : n < 54 ? t[n - 28] : w2s_(w2c_(n - 54));
}
constexpr u32 upos(int n) { return spos(uslot(n)); }
constexpr u32 VBUF = RBASE + 15 * UNIT;
constexpr int imin(int a, int b) { return a < b ? a : b; }

struct Ctx {
  const char *gw, *wd, *wo;
  char *out, *ws;
  int t, kh, jj, hd, j, w, tg, r;
  u32 lane, l16, m0w;
  u32 vx, vx2, vw, vq, vkv, v2, v2b;
  rsrc_t sx, sg, su, sq, sp, sd, so, sw;
  u64 tag;
  u32 rot[7];
  u32 oh, oo, op, okvp;
  u32 oflag, og;
  u32 vrp, vflag0;
  u32 vflag, awp, vone;
};

DI rsrc_t mk(const void* p, u32 bytes) { return __builtin_amdgcn_make_buffer_rsrc((void*)p, 0, bytes, 0x00020000); }
template <u32 L> DI void set_m0(const Ctx& c) { if (!(ABL & 1)) asm volatile("s_add_u32 m0, %0, %1" :: "s"(c.m0w), "i"(L) : "memory", "scc"); }
DI void dma_dev(u32 voff, rsrc_t rs, u32 soff) {
  if (!(ABL & 1)) asm volatile("buffer_load_dwordx4 %0, %1, %2 offen sc1 lds" :: "v"(voff), "s"(rs), "s"(soff) : "memory");
}
template <u32 OFF> DI void dma(u32 voff, rsrc_t rs, u32 soff) {
  if (!(ABL & 1)) asm volatile("buffer_load_dwordx4 %0, %1, %2 offen offset:%3 lds" :: "v"(voff), "s"(rs), "s"(soff), "i"(OFF) : "memory");
}
template <int N> DI void wait_bar() { asm volatile("s_waitcnt vmcnt(%0) lgkmcnt(0)\n\ts_barrier" :: "n"(N) : "memory"); }
template <int N> DI void wait_bar_v() { asm volatile("s_waitcnt vmcnt(%0)\n\ts_barrier" :: "n"(N) : "memory"); }
DI void bar_l() { asm volatile("s_waitcnt lgkmcnt(0)\n\ts_barrier" ::: "memory"); }

constexpr u32 ioff_(int n) { return n >= 36 && n < 54 && !(n < 47 && n % 3 == 0) ? 128 : 0; }
template <int N> DI void unit_m0(const Ctx& c) { set_m0<upos(N) - ioff_(N)>(c); }
template <int R, int N> DI void unit_go(const Ctx& c) {
  if constexpr (N < 21) {
    if constexpr (N % 3 < 2) dma<0>(N % 3 ? c.vx2 : c.vx, c.sx, c.rot[N / 3] * 128);
    else dma<0>(c.vw, c.sg, c.rot[N / 3] * (64 * 3584));
  } else if constexpr (N >= 29 && N < 36) dma_dev(c.vkv, c.sw, c.rot[N - 29] * 4096);
  else if constexpr (N == 28) dma<0>(c.l16, c.sp, c.w == 5 ? 0u : c.w == 6 ? 1024u : 0x40000000u);
  else if constexpr (N < 28) dma<0>(c.vw, c.su, c.rot[N - 21] * (64 * 3584));
  else if constexpr (N < 47) {
    if constexpr (N % 3 == 0) dma_dev(c.vq, c.sw, N == 45 && R == 1 ? 0x40000000u : c.rot[(2 * (N - 36) / 3 + R) % 7] * 4096);
    else dma<128>(c.vw, c.sg, c.rot[2 * ((N - 36) / 3) + N % 3 - 1] * (64 * 3584));
  } else if constexpr (N < 54) dma<128>(c.vw, c.su, c.rot[N - 47] * (64 * 3584));
  else {
    constexpr int k = w2c_(N - 54);
    if constexpr (k < 14) dma<0>(k < 7 ? c.v2 : c.v2b, c.sd, c.rot[k % 7] * (128 * 896));
    else dma<0>((k & 1) ? c.v2b : c.v2, c.so, k < 16 ? 0u : 128u * 896u);
  }
}
template <int N, int H> DI void unit_half(const Ctx& c) {
  set_m0<upos(N) + H * 4096>(c);
  constexpr int k = w2c_(N - 54);
  const u32 so = (k < 14 ? c.rot[k % 7] * (128 * 896) : k < 16 ? 0u : 128u * 896u) + H * (32 * 896);
  asm volatile("s_nop 0");
  dma<0>((k < 14 ? k >= 7 : (k & 1)) ? c.v2b : c.v2, k < 14 ? c.sd : c.so, so);
}
template <int R, int A, int B> DI void issue_range(const Ctx& c) {
  if constexpr (A < B) { unit_m0<A>(c); asm volatile("s_nop 0"); unit_go<R, A>(c); issue_range<R, A + 1, B>(c); }
}

struct Seg { u32 b[3]; };
DI Seg mkseg(u32 a) { Seg s; s.b[0] = a; s.b[1] = a + 65536; s.b[2] = a + 131072; asm volatile("" : "+v"(s.b[0]), "+v"(s.b[1]), "+v"(s.b[2])); return s; }
DI u32 sa(const Seg& s, u32 off) { return s.b[off >> 16] + (off & 0xffffu); }
DI f32x16 mfma(bf16x8 a, bf16x8 b, f32x16 c) { if (NOMM) { asm volatile("" : "+v"(c) : "v"(a), "v"(b)); return c; } return __builtin_amdgcn_mfma_f32_32x32x16_bf16(a, b, c, 0, 0, 0); }
DI bf16x8 as_frag(u32x4 v) { return __builtin_bit_cast(bf16x8, v); }
DI bf16x8 tr_frag(u32 a0, u32 a1) {
  if (NOMM > 1) { bf16x8 z; asm volatile("" : "=v"(z)); return z; }
  s16x4 lo = __builtin_amdgcn_ds_read_tr16_b64_v4i16((LAS s16x4*)a0);
  s16x4 hi = __builtin_amdgcn_ds_read_tr16_b64_v4i16((LAS s16x4*)a1);
  return __builtin_bit_cast(bf16x8, __builtin_shufflevector(lo, hi, 0, 1, 2, 3, 4, 5, 6, 7));
}
DI u32x4 lds_rd128(u32 a) { return *(LAS u32x4*)a; }
DI void lds_wr128(u32 a, u32x4 v) { *(LAS u32x4*)a = v; }
DI u32 f2u(float f) { return __builtin_bit_cast(u32, f); }
DI float u2f(u32 u) { return __builtin_bit_cast(float, u); }
DI u32 pk(float a, float b) { f32x2 t = {a, b}; return __builtin_bit_cast(u32, __builtin_convertvector(t, bf16x2)); }
template <int S, class V> DI u32x4 pack8(const V& v) {
  u32x4 r = {pk(v[8 * S], v[8 * S + 1]), pk(v[8 * S + 2], v[8 * S + 3]), pk(v[8 * S + 4], v[8 * S + 5]), pk(v[8 * S + 6], v[8 * S + 7])};
  return r;
}
DI float blo(u32 d) { return u2f(d << 16); }
DI float bhi(u32 d) { return u2f(d & 0xffff0000u); }
DI u32 umax(u32 a, u32 b) { return a > b ? a : b; }
DI bool any_(bool p) { return __builtin_amdgcn_ballot_w64(p) != 0; }
DI u32x4 sanit(u32x4 v) { return __builtin_elementwise_min(v, (u32x4){0xfffffffeu, 0xfffffffeu, 0xfffffffeu, 0xfffffffeu}); }
DI void st_dev(const Ctx& c, u32x4 v, u32 voff, u32 soff) {
  if (ABL & 8) { asm volatile("" :: "v"(v)); return; }
  __builtin_amdgcn_raw_buffer_store_b128(v, c.sw, voff, soff, 16);
  asm volatile("s_nop 1" :: "v"(v) : "memory");
}
DI u32x4 ld_dev(const Ctx& c, u32 voff, u32 soff) {
  if (ABL & 16) { u32x4 z = {voff, soff, voff, soff}; asm volatile("" : "+v"(z)); return z; }
  return __builtin_amdgcn_raw_buffer_load_b128(c.sw, voff, soff, 16);
}
DI f32x2 xhalf(float v) { auto p = __builtin_amdgcn_permlane32_swap(f2u(v), f2u(v), false, false); f32x2 r = {u2f(p[0]), u2f(p[1])}; return r; }

#define STAMP(i)
#define STAMP_END()

struct P1 {
  Seg aw, aq; u32 ax, ka, kb, kc;
  bf16x8 xf[28], f[8], af[8], vf[8];
  f32x16 g1, u1, a0, a1, g2, u2, fp, k0, c[2];
  Seg s2[2]; u32 ob[2];
  u32x4 h2[14], of[8];
  u32x4 wp[2], ph[4], h1[14], kv[4];
  u32 wr[16];
  float y[16], e[14], sum, mxl, mnl;
  u32x2 poll;
  float gq, om;
  u32 hbase;
};

DI void wphi_rd(const Ctx& c, P1& p) {
#pragma unroll
  for (int i = 0; i < 16; ++i) p.wr[i] = *(LAS unsigned short*)(c.awp + 56 * (16 * (i >> 3) + 8 * ((i >> 2) & 1) + (i & 3)));
}
DI void wphi_pack(P1& p) {
#pragma unroll
  for (int i = 0; i < 8; ++i) p.wp[i >> 2][i & 3] = p.wr[2 * i] | (p.wr[2 * i + 1] << 16);
  PIN2(p.wp[0], p.wp[1]);
}
template <int K> DI void fm_slice(P1& p) {
  if constexpr (NOFM) { if constexpr (K == 0) { p.ph[0] = p.ph[2] = pack8<0>(p.a0); p.ph[1] = p.ph[3] = pack8<1>(p.a0); PIN2(p.ph[0], p.ph[1]); PIN2(p.ph[2], p.ph[3]); } }
  else if constexpr (K == 0) { p.fp = mfma(as_frag(p.wp[0]), as_frag(pack8<0>(p.a0)), (f32x16){}); }
  else if constexpr (K == 1) { p.fp = mfma(as_frag(p.wp[1]), as_frag(pack8<1>(p.a0)), p.fp); }
  else if constexpr (K == 2) { }
  else if constexpr (K == 3) {
    PIN1(p.fp);
    float mx = p.fp[0];
#pragma unroll
    for (int i = 1; i < 14; ++i) mx = __builtin_fmaxf(mx, p.fp[i]);
    p.mxl = mx; PIN1(p.mxl);
  } else if constexpr (K == 4) {
    float mn = p.fp[0];
#pragma unroll
    for (int i = 1; i < 14; ++i) mn = __builtin_fminf(mn, p.fp[i]);
    p.mnl = mn; PIN1(p.mnl);
  } else if constexpr (K == 5) {
    f32x2 t = xhalf(p.mxl); p.mxl = __builtin_fmaxf(t[0], t[1]) * LOG2E;
    t = xhalf(p.mnl); p.mnl = __builtin_fminf(t[0], t[1]) * LOG2E;
    PIN2(p.mxl, p.mnl);
  } else {
    constexpr int NEG = K >= 15, k = K - 6 - 9 * NEG;
    if constexpr (k < 5) {
#pragma unroll
      for (int i = 3 * k; i < imin(14, 3 * k + 3); ++i) {
        p.e[i] = __builtin_amdgcn_exp2f(NEG ? __builtin_fmaf(p.fp[i], -LOG2E, p.mnl) : __builtin_fmaf(p.fp[i], LOG2E, -p.mxl));
        PIN1(p.e[i]);
      }
      if constexpr (k == 1) p.sum = p.e[0] + p.e[1] + p.e[2];
      if constexpr (k > 1) p.sum += p.e[3 * k - 3] + p.e[3 * k - 2] + p.e[3 * k - 1];
      if constexpr (k > 0) PIN1(p.sum);
    } else if constexpr (k == 5) {
      f32x2 t = xhalf(p.sum + p.e[12] + p.e[13]); p.sum = __builtin_amdgcn_rcpf(t[0] + t[1]); PIN1(p.sum);
    } else if constexpr (k < 8) {
#pragma unroll
      for (int i = 7 * (k - 6); i < 7 * (k - 5); ++i) { p.e[i] *= p.sum; PIN1(p.e[i]); }
    } else {
      u32x4 o1 = {pk(p.e[8], p.e[9]), pk(p.e[10], p.e[11]), pk(p.e[12], p.e[13]), 0u};
      p.ph[2 * NEG] = pack8<0>(p.e); p.ph[2 * NEG + 1] = o1; PIN2(p.ph[2 * NEG], p.ph[2 * NEG + 1]);
    }
  }
}
template <int A, int B> DI void fm_slices(P1& p) { if constexpr (A < B) { fm_slice<A>(p); fm_slices<A + 1, B>(p); } }
template <u32 VB, int S> DI void v_handoff(const Ctx& c, const f32x16& av) {
  u32x4 vf = pack8<S>(av);
  const bool one = (c.lane & 31) == ((c.hd & 1) ? 0u : 28u);
#pragma unroll
  for (int d = 0; d < 4; ++d) vf[d] = one ? 0x3f803f80u : vf[d];
  lds_wr128(VB + c.tg * 2048 + c.l16 + S * 1024, vf);
}
template <int T> DI void phi_image(const Ctx& c, P1& p) {
  const u32 tok = c.lane & 31, h = c.lane >> 5;
  const u32 ft = (((tok >> 1) & 1) << 3) | (tok & 1) | (((tok >> 2) & 3) << 1);
  const u32 wa = SCR + c.tg * 4096 + tok * 128 + ((h ^ ft) << 3);
#pragma unroll
  for (int g = 0; g < 4; ++g) {
    u32x2 v = {p.ph[2 * T + (g >> 1)][2 * (g & 1)], p.ph[2 * T + (g >> 1)][2 * (g & 1) + 1]};
    *(LAS u32x2*)(wa ^ (u32)((8 * T + 2 * g) << 3)) = v;
  }
}
template <u32 VB, int K> DI void kv_stage(const Ctx& c, P1& p) {
  if constexpr (K >= 2 && K <= 5) {
    p.k0 = mfma(p.af[2 * K - 4], p.vf[2 * K - 4], K == 2 ? (f32x16){} : p.k0);
    p.k0 = mfma(p.af[2 * K - 3], p.vf[2 * K - 3], p.k0);
  }
  if constexpr (K == 0) {
    const u32 l = c.lane, h = l >> 5, q = (l & 15) >> 2, pp = l & 3, blk = (l >> 4) & 1;
    p.ka = SCR + (4 * h + q) * 128 + (((4 * blk + pp) ^ (q & 1) ^ (h << 1) ^ ((q >> 1) << 3) ^ ((c.tg & 1) << 3)) << 3);
    p.kb = p.ka ^ 32u; p.kc = VB + c.l16;
    asm volatile("" : "+v"(p.ka), "+v"(p.kb), "+v"(p.kc));
  }
  if constexpr (K < 4) {
#pragma unroll
    for (int s = 0; s < 2; ++s) {
      p.af[2 * K + s] = tr_frag(p.ka + K * 4096 + s * 2048, p.kb + K * 4096 + s * 2048 + 1024);
      p.vf[2 * K + s] = as_frag(lds_rd128(p.kc + K * 2048 + s * 1024));
    }
  }
  if constexpr (K == 7) {
    const u32 vo = c.tg < 2 ? c.l16 : OOB;
    st_dev(c, sanit(pack8<0>(p.k0)), vo, c.okvp + c.tg * 1024);
    st_dev(c, sanit(pack8<1>(p.k0)), vo + 1024, c.okvp + c.tg * 1024);
  }
}
DI void add8(float (&s)[8], u32x4 v) {
#pragma unroll
  for (int d = 0; d < 4; ++d) { s[2 * d] += blo(v[d]); s[2 * d + 1] += bhi(v[d]); }
}
DI void set8(float (&s)[8], u32x4 v) {
#pragma unroll
  for (int d = 0; d < 4; ++d) { s[2 * d] = blo(v[d]); s[2 * d + 1] = bhi(v[d]); }
}
template <int A, int B, int N> DI bool has_mark(const u32x4 (&v)[N]) {
  u32 m = 0;
#pragma unroll
  for (int i = A; i < B; ++i) m = umax(umax(m, v[i][0]), umax(v[i][1], umax(v[i][2], v[i][3])));
  return any_(m == 0xffffffffu);
}
template <int A, int B, int N, class F> DI void chk(u32x4 (&v)[N], F&& reload) {
  if (__builtin_expect(SPIN_MAX && has_mark<A, B>(v), 0)) {
    u32 spin = 0;
    do { asm volatile("" ::: "memory"); reload(); } while (has_mark<A, B>(v) && ++spin < SPIN_MAX);
  }
}
template <int A, int B, int N> DI void ld_lin(const Ctx& c, u32x4 (&v)[N], u32 soff, u32 stride) {
#pragma unroll
  for (int i = A; i < B; ++i) v[i] = ld_dev(c, c.l16, soff + i * stride);
}
template <int A, int B, int N> DI void ld_1k(const Ctx& c, u32x4 (&v)[N], u32 soff) {
#pragma unroll
  for (int i = A; i < B; ++i) v[i] = ld_dev(c, c.l16 + (i & 3) * 1024, soff + (i >> 2) * 4096);
}
template <int A, int B> DI void ld_h(const Ctx& c, u32x4 (&v)[14], u32 voff, u32 soff) {
#pragma unroll
  for (int i = A; i < B; ++i) v[i] = ld_dev(c, voff + (i & 1) * 1024, soff + c.rot[i >> 1] * 8192);
}
DI void o_publish(const Ctx& c, const u32x4 (&kv)[4], const u32x4 (&phq)[4], float g) {
  f32x16 n = {};
#pragma unroll
  for (int i = 0; i < 4; ++i) n = mfma(as_frag(kv[i]), as_frag(phq[i]), n);
  const bool odd = c.hd & 1;
  f32x2 t = xhalf(odd ? n[0] : n[12]);
  float sc = g * __builtin_amdgcn_rcpf((odd ? t[0] : t[1]) + 1e-6f);
#pragma unroll
  for (int i = 0; i < 16; ++i) n[i] *= sc;
  u32x4 f0 = sanit(pack8<0>(n)), f1 = sanit(pack8<1>(n));
  const bool z0 = odd && c.lane < 32, z1 = !odd && c.lane >= 32;
  f0[0] = z0 ? 0 : f0[0]; f0[1] = z0 ? 0 : f0[1];
  f1[2] = z1 ? 0 : f1[2]; f1[3] = z1 ? 0 : f1[3];
  st_dev(c, f0, c.l16, c.oo);
  st_dev(c, f1, c.l16 + 1024, c.oo);
}
DI u32 flag_off(const Ctx& c, bool spare) {
  const u32 i = c.lane & 15;
  u32 red = (c.hd * 16 + 7 + 8 * (i & 1)) << 7, sp = ((c.t * 16 + 7 + 8 * c.kh) << 7) + 8;
  u32 lo = spare ? (i * 16 + c.t) << 7 : ((c.t * 16 + (i < 7 ? 8 * c.kh + i : c.hd ^ 8)) << 7) + 8;
  asm volatile("" : "+v"(red), "+v"(sp), "+v"(lo));
  return WS_FLAG + ((c.lane & 16) ? red : (spare || i < 8) ? lo : sp);
}
DI bool flag_bad(const Ctx& c, u32x2 f) { return any_(f[0] != (u32)c.tag || f[1] != (u32)(c.tag >> 32)); }
DI void flag_spin(const Ctx& c, u32x2 f, u32 voff) {
  if (__builtin_expect(SPIN_MAX && flag_bad(c, f), 0)) {
    u32 spin = 0;
    do {
      __builtin_amdgcn_s_sleep(2);
      asm volatile("" ::: "memory");
      f = __builtin_amdgcn_raw_buffer_load_b64(c.sw, voff, 0, 16);
    } while (flag_bad(c, f) && ++spin < SPIN_MAX);
  }
  asm volatile("" ::: "memory");
}
DI void flag_publish(const Ctx& c, u32 word) {
  u32x2 v = {(u32)c.tag, (u32)(c.tag >> 32)};
  __builtin_amdgcn_raw_buffer_store_b64(v, c.sw, c.vone + word * 8, c.oflag, 16);
}
DI u32x2 ld_g(const Ctx& c) { return __builtin_amdgcn_raw_buffer_load_b64(c.sw, (c.lane & 31) * 8, c.og, 16); }

template <u32 L, bool DEV> DI void touch(u32 voff, rsrc_t rs, u32 soff) {
  if (ABL & 9) return;
  if constexpr (DEV) asm volatile("s_mov_b32 m0, %0\n\ts_nop 0\n\tbuffer_load_dword %1, %2, %3 offen sc1 lds" :: "i"(L), "v"(voff), "s"(rs), "s"(soff) : "memory");
  else asm volatile("s_mov_b32 m0, %0\n\ts_nop 0\n\tbuffer_load_dword %1, %2, %3 offen lds" :: "i"(L), "v"(voff), "s"(rs), "s"(soff) : "memory");
}

constexpr int PRO = PROV;
constexpr int NS = 21;
constexpr int need_(int s) {
  return s < 0 ? 0 : s < 7 ? 3 * (s + 1) : s < 9 ? 23 + 2 * (s - 7) : s < 12 ? 31 + 2 * (s - 9) : s < 14 ? 36 : s < 18 ? 39 + 3 * (s - 14) : s < 20 ? 50 + 2 * (s - 18) : 62;
}
constexpr int iss_(int s) {
  constexpr int t[14] = {I7, I8, I9, I10, I11, I12, 49, 51, 53, 56, 59, I18, I19, I20};
  return s < 0 ? PRO : s < 7 ? imin(imin(29, 18 + need_(s - 2)), PRO + 3 * (s + 1) + (s >= A4 ? s - A4 + 1 : 0)) : t[s - 7];
}
constexpr int ustep(int n) {
  if (n == 34 || n == 28) return 16;
  if (n >= 25 && n < 28) return n == 25 ? 12 : 13;
  int s = 0; while (s < 20 && need_(s) <= n) ++s; return s;
}
constexpr bool ring_ok() {
  for (int s = -1; s < NS; ++s)
    for (int n = s < 0 ? 0 : iss_(s - 1); n < iss_(s); ++n) {
      if (uslot(n) == -2 && s < 18) return false;
      for (int m = 0; m < n; ++m) if (uslot(m) == uslot(n) && ustep(m) + 2 > s) return false;
    }
  return true;
}
static_assert(ring_ok(), "ring schedule");
constexpr int S_FLAG0 = FLAG0, S_POLL0 = POLL0, S_CHK0 = 7, S_GATE = GATE, S_CHKG = 13, S_FLAG1 = 16, S_POLL = POLL1, S_CHK = CHK1;
constexpr int oth_(int r, int s) {
  return s == S_FLAG0 ? 1 - r : s == S_POLL0 ? 1 : s == 10 ? (GATE == 10) : s == 11 ? 1 - r + (GATE == 11) : s == 12 ? r : s == 14 ? 2 : s == 16 ? 3 - 3 * r + TOUCH1 :
         s == S_POLL ? 1 : s == 18 ? (r || CHK1 == 19 ? 0 : 4) : s == 19 || s == 20 ? (r ? 0 : CHK1 == 19 ? 7 : 5) : 0;
}
constexpr int tot_(int r, int s) { return iss_(s) - iss_(s - 1) + oth_(r, s); }
constexpr int vm_(int r, int s) {
  int sp = -1; while (iss_(sp) < need_(s)) ++sp;
  int n = iss_(sp) - need_(s) + (sp < 0 ? 2 - r : 0);
  for (int k = sp + 1; k < s; ++k) n += tot_(r, k);
  if (s == S_FLAG0) { int m = 0; for (int k = 0; k < s; ++k) m += tot_(r, k); n = imin(n, m); }
  if (s == S_FLAG1) n = imin(n, tot_(r, 15));
  if (s == S_CHK0 || s == S_CHK || s == S_CHKG) {
    int m = -1; for (int k = s == S_CHK0 ? S_POLL0 : s == S_CHKG ? S_GATE : S_POLL; k < s; ++k) m += tot_(r, k);
    n = imin(n, m);
  }
  return n;
}
constexpr int P2_(int r) { return r ? 168 : 140; }
constexpr int item0_(int r, int s) { return s < 7 ? 4 * s : (!r || s < 15) ? 8 * s - 28 : s < 18 ? 84 + 16 * (s - 14) : 8 * s; }
template <int R, int J> DI void rd_item(P1& p) {
  constexpr int U2 = P2_(R) - 28;
  if constexpr (J >= P2_(R)) {
    constexpr int k = J - P2_(R);
    if constexpr (k < 56) {
      constexpr u32 o = spos(w2s_(k / 4)) + ((k / 2) & 1) * 2048;
      p.f[J % 8] = tr_frag(sa(p.s2[k & 1], o), sa(p.s2[k & 1], o + 1024));
    } else if constexpr (k < 72) {
      constexpr int i = (k - 56) / 2, hh = i >> 1;
      constexpr u32 o = (28 * hh - 4 * (hh & 1) + 16 * (i & 1)) * 128;
      p.f[J % 8] = tr_frag(p.ob[k & 1] + o, p.ob[k & 1] + o + 1024);
    }
  } else if constexpr ((J >= 44 && J < 72) || (R == 1 && J >= 84 && J < U2 && (J & 1))) {
    constexpr int k = J < 72 ? J - 44 : (J - 84) / 2, ch = k / 4;
    constexpr u32 o = (J < 72 ? upos(29 + ch) + R * 4096 : upos(36 + 3 * (ch / 2)) + (ch & 1) * 4096) + (k % 4) * 1024;
    p.f[J % 8] = tr_frag(sa(p.aq, o), sa(p.aq, o + 256));
  } else {
    constexpr int k = J < 84 ? J : J < U2 ? (J - 84) / (1 + R) : J - U2, ch = k / 4;
    constexpr u32 o = upos(J < 28 ? 3 * ch + 2 : J < 44 ? 14 + ch : J < 84 ? 7 + ch : J < U2 ? 37 + 3 * (ch / 2) + (ch & 1) : 47 + ch) + (k % 4) * 2048;
    p.f[J % 8] = tr_frag(sa(p.aw, o), sa(p.aw, o + 512));
  }
}
template <int R, int J> DI void mm_item(P1& p) {
  constexpr int U2 = P2_(R) - 28;
  if constexpr (J < 0) {}
  else if constexpr (J >= P2_(R)) {
    constexpr int k = J - P2_(R);
    p.c[k & 1] = mfma(p.f[J % 8], as_frag(k < 28 ? p.h1[(k / 2) % 14] : k < 56 ? p.h2[(k / 2) % 14] : p.of[(k / 2 + 4) % 8]), p.c[k & 1]);
  } else if constexpr (J >= 44 && J < 72) {
    if constexpr (R == 1) p.a1 = mfma(p.xf[J - 44], p.f[J % 8], p.a1); else p.a0 = mfma(p.f[J % 8], p.xf[J - 44], p.a0);
  } else {
    f32x16& acc = J < 28 ? p.g1 : J < 84 ? p.u1 : J >= U2 ? p.u2 : (R == 1 && (J & 1)) ? p.a0 : p.g2;
    acc = mfma(p.f[J % 8], p.xf[J < 84 ? J % 28 : J < U2 ? (J - 84) / (1 + R) : J - U2], acc);
  }
}
template <int R, int A, int B> DI void mm_range(P1& p) { if constexpr (A < B) { mm_item<R, A>(p); mm_range<R, A + 1, B>(p); } }

constexpr float GK1 = -2.0f * 0.7978845608028654f * LOG2E, GK2 = GK1 * 0.044715f;
template <bool SC, int OP> DI void gelu_op(float (&y)[16], const f32x16& g, float om) {
  constexpr int st = OP % 4, i = OP / 4 - st;
  if constexpr (NOGELU && i >= 0 && i < 16) { if constexpr (st == 3) { y[i] = g[i]; PIN1(y[i]); } }
  else if constexpr (i >= 0 && i < 16) {
    if constexpr (st == 0) y[i] = g[i] * __builtin_fmaf(g[i] * g[i], GK2, GK1);
    else if constexpr (st == 1) y[i] = __builtin_amdgcn_exp2f(y[i]);
    else if constexpr (st == 2) y[i] = __builtin_amdgcn_rcpf(1.0f + y[i]);
    else y[i] = SC ? (g[i] * om) * y[i] : g[i] * y[i];
    PIN1(y[i]);
  }
}
constexpr int gcost(int op) { return op / 4 - op % 4 < 0 || op / 4 - op % 4 > 15 ? 0 : op % 4 == 0 ? 3 : op % 4 == 1 ? 2 : op % 4 == 2 ? 3 : 1; }
constexpr int gcum(int k, int n) { if (k >= n) return 76; int op = 0, cst = 0; while (cst * n < k * 144) cst += gcost(op++); return op; }
template <bool SC, int A, int B> DI void gelu_ops(float (&y)[16], const f32x16& g, float om) { if constexpr (A < B) { gelu_op<SC, A>(y, g, om); gelu_ops<SC, A + 1, B>(y, g, om); } }
template <int S> DI void h_store(const Ctx& c, const float (&y)[16], const f32x16& u, int blk) {
  float h[8];
#pragma unroll
  for (int i = 0; i < 8; ++i) h[i] = y[8 * S + i] * u[8 * S + i];
  st_dev(c, pack8<0>(h), c.l16 + S * 1024, c.oh + blk * 4096);
}
template <int A, int B> DI void y_scale(P1& p) {
#pragma unroll
  for (int i = A; i < B; ++i) { p.y[i] *= p.om; PIN1(p.y[i]); }
}
DI void addr2(const Ctx& c, P1& p) {
  const u32 l = c.lane, ks = c.tg >> 1, qq = (l & 15) >> 2;
  const u32 bk = ((4 * (l >> 5) + qq) * 128 + ((qq >> 1) << 6) + 32 * ((l >> 4) & 1) + 8 * (l & 3)) ^ (64 * ks);
  p.s2[0] = mkseg(bk + ks * 4096); p.s2[1] = mkseg((bk ^ 64) + ks * 4096);
  p.ob[0] = bk + ks * (112 * 128); p.ob[1] = (bk ^ 64) + ks * (112 * 128);
  PIN2(p.ob[0], p.ob[1]);
}
template <int R, int S, int K, int J> DI void slot_work(Ctx& c, P1& p) {
  if constexpr (S < 7) p.xf[J] = as_frag(lds_rd128((p.ax + upos(3 * S)) ^ (u32)(32 * K)));
  if constexpr (R == 0) {
    if constexpr (J >= 33 && J < 75) gelu_ops<false, gcum(J - 33, 42), gcum(J - 32, 42)>(p.y, p.g1, 0.f);
    if constexpr (J == 70) wphi_rd(c, p);
    if constexpr (J == 76) wphi_pack(p);
    if constexpr (J == 78 || J == 79) y_scale<8 * (J - 78), 8 * (J - 77)>(p);
    constexpr int F0 = J < 84 ? J - 77 : J < 89 ? 7 + (3 * (J - 84) + 1) / 2 : J < 91 ? 15 : 15 + (9 * (J - 91) + 7) / 8;
    constexpr int F1 = J + 1 < 84 ? J - 76 : J + 1 < 89 ? 7 + (3 * (J - 83) + 1) / 2 : J + 1 < 91 ? 15 : 15 + (9 * (J - 90) + 7) / 8;
    if constexpr (J >= 77 && J < 99) fm_slices<F0, imin(24, F1)>(p);
    if constexpr (J == 92) phi_image<0>(c, p);
    if constexpr (J == 99) phi_image<1>(c, p);
    if constexpr (S == 16) kv_stage<VBUF, K>(c, p);
    if constexpr (CHK1 == 19 && S >= 19 && K >= 1) ld_h<7 * (S - 19) + K - 1, 7 * (S - 19) + K>(c, p.h1, c.l16, p.hbase);
    if constexpr (CHK1 == 18 && S == 18 && (K & 1)) ld_h<K / 2, K / 2 + 1>(c, p.h1, c.l16, p.hbase);
    if constexpr (CHK1 == 18 && S >= 19 && ((K & 1) || K == 4)) { constexpr int i = 4 + 5 * (S - 19) + (K < 4 ? K / 2 : (K + 1) / 2); ld_h<i, i + 1>(c, p.h1, c.l16, p.hbase); }
  } else {
    if constexpr (J >= 33 && J < 77) gelu_ops<false, gcum(J - 33, 44), gcum(J - 32, 44)>(p.y, p.g1, 0.f);
    if constexpr (J == 77 || J == 78) v_handoff<VBUF, J - 77>(c, p.a1);
    if constexpr (J == 79) wphi_rd(c, p);
    if constexpr (J == 83) wphi_pack(p);
    if constexpr (J >= 84 && J < 88) y_scale<4 * (J - 84), 4 * (J - 83)>(p);
  }
  if constexpr (J == 89 || J == 90) h_store<J - 89>(c, p.y, p.u1, 0);
  if constexpr (J >= P2_(R) - 23) gelu_ops<true, gcum(J - P2_(R) + 23, 27), gcum(J - P2_(R) + 24, 27)>(p.y, p.g2, p.om);
  if constexpr (J == P2_(R) - 26) addr2(c, p);
  if constexpr (TOUCH1 && S == S_FLAG1 && K == 6) touch<upos(45) + 4096, true>(c.lane < 16 ? c.lane * 128 : OOB, c.sw, c.oh);
  if constexpr (S == 11 + R && K == 3) st_dev(c, (u32x4){~0u, ~0u, ~0u, ~0u}, c.l16 + R * 1024, c.op);
}
template <int R, int S, int J0, int K, int N> DI void slots(Ctx& c, P1& p) {
  if constexpr (K < N) {
    constexpr int nu = iss_(S) - iss_(S - 1), u0 = iss_(S - 1);
    constexpr int ua = K == 0 ? 0 : ((2 * K + R) * nu) / (2 * N), ub = K == N - 1 ? nu : ((2 * K + 2 + R) * nu) / (2 * N);
    if constexpr (R == 1 && S >= 18 && (K == 0 || K == 4)) __builtin_amdgcn_s_setprio(K == 0);
    mm_item<R, J0 + K - 4>(p);
    if (ORD) SB0();
    if constexpr (ub > ua) unit_m0<u0 + ua>(c);
    rd_item<R, (J0 + K) ^ SWP>(p);
    if constexpr (ub > ua) { unit_go<R, u0 + ua>(c); issue_range<R, u0 + ua + 1, u0 + ub>(c); }
    if (ORD) SB0();
    slot_work<R, S, K, J0 + K>(c, p);
    SB0();
    slots<R, S, J0, K + 1, N>(c, p);
  }
}
template <int R, int S> DI void step(Ctx& c, P1& p) {
  if constexpr (S == S_CHK || S == S_CHK0 || S == S_CHKG) {
    asm volatile("s_waitcnt vmcnt(%1)\n\ts_barrier" : "+v"(p.poll) : "n"(vm_(R, S)) : "memory");
    if constexpr (S == S_CHK) { PIN2(CHK1 == 19 ? p.u2 : p.g2, p.a0); flag_spin(c, p.poll, c.vflag); }
    else if constexpr (S == S_CHK0) { PIN1(p.g1); flag_spin(c, p.poll, c.vflag0); }
    else {
      PIN2(p.u1, R ? p.a1 : p.a0);
      if (__builtin_expect(SPIN_MAX && any_(p.poll[1] != (u32)c.tag), 0)) {
        u32 spin = 0;
        do { __builtin_amdgcn_s_sleep(2); asm volatile("" ::: "memory"); p.poll = ld_g(c); } while (any_(p.poll[1] != (u32)c.tag) && ++spin < SPIN_MAX);
      }
      p.gq = u2f(p.poll[0]); p.om = 1.0f - p.gq;
      PIN2(p.gq, p.om);
    }
  } else if constexpr ((S == 16 && R == 0) || (S == 20 && NPRE > 4)) wait_bar<vm_(R, S)>();
  else wait_bar_v<vm_(R, S)>();
  if constexpr (S < 2 || S >= 6) STAMP(S < 2 ? S + 1 : S - 3);
  if constexpr (S == DELAYS) __builtin_amdgcn_s_sleep(16);
  if constexpr (S == S_FLAG0 && R == 0) flag_publish(c, 0);
  if constexpr (S == S_FLAG1 && R == 0) flag_publish(c, 1);
  if constexpr (S == S_POLL || S == S_POLL0) asm volatile("buffer_load_dwordx2 %0, %1, %2, 0 offen sc1" : "=v"(p.poll) : "v"(S == S_POLL ? c.vflag : c.vflag0), "s"(c.sw) : "memory");
  if constexpr (S == S_GATE) asm volatile("buffer_load_dwordx2 %0, %1, %2, %3 offen sc1" : "=v"(p.poll) : "v"((c.lane & 31) * 8), "s"(c.sw), "s"(c.og) : "memory");
  SB0();
  slots<R, S, item0_(R, S), 0, item0_(R, S + 1) - item0_(R, S)>(c, p);
}
template <int R, int S, int E> DI void steps(Ctx& c, P1& p) { if constexpr (S < E) { step<R, S>(c, p); steps<R, S + 1, E>(c, p); } }
template <int R, int A, int B, int RE, class F> DI void run(P1& p, F&& extra) {
  if constexpr (A < B) {
    mm_item<R, A - 4>(p);
    if (ORD) SB0();
    if constexpr (A < RE) rd_item<R, A ^ 1>(p);
    if (ORD) SB0();
    extra(std::integral_constant<int, A>{});
    SB0();
    run<R, A + 1, B, RE>(p, extra);
  } else { PIN2(p.c[0], p.c[1]); }
}
template <int K, class F> DI void fmq(P1& p, F&& extra) {
  if constexpr (K < 24) { fm_slice<K>(p); extra(std::integral_constant<int, K>{}); SB0(); fmq<K + 1>(p, extra); }
}
template <int R, int J> DI void rd4(P1& p) { rd_item<R, J + 1>(p); rd_item<R, J>(p); rd_item<R, J + 3>(p); rd_item<R, J + 2>(p); }

template <int R> DI void ffn_wg(Ctx& c, P1& p) {
  const u32 l = c.lane;
  const int tg2 = c.tg & 1, ks = c.tg >> 1;
  const int TG = 2 * (R == 1 ? c.kh : 1 - c.kh) + tg2;
  p.hbase = WS_H + (u32)(((c.t * 4 + TG) * 112 + 56 * c.kh + 2 * ks) * 1024);
  if (XB & 4) p.hbase = 0x40000000u;
  asm volatile("" : "+s"(p.hbase));
  PIN1(c.awp);
  SB0();
  issue_range<R, 3, PRO>(c);
  asm volatile("s_waitcnt vmcnt(%0)" :: "n"(PRO - 3) : "memory");
  st_dev(c, lds_rd128(spos(17) + c.m0w + c.l16), c.vrp, 0);
  if constexpr (R == 0) st_dev(c, (u32x4){~0u, ~0u, ~0u, ~0u}, c.l16, c.okvp);
  p.g1 = p.u1 = p.a0 = p.a1 = p.g2 = p.u2 = p.c[0] = p.c[1] = (f32x16){};
  steps<R, 0, NS>(c, p);
  STAMP(18);
  auto none = [](auto) {};
  constexpr int B = P2_(R);
  const u32 h = l >> 5;
  const u32 obase = ((XB & 8) ? 0x40000000u : WS_O) + (u32)(((c.t * 4 + TG) * 16 + 8 * c.kh + 4 * ks) * 2048);
  const u32 l16h = c.l16 + 4096, vflag2 = c.vflag + ((l & 31) < 7 ? 8 : 0);
  auto epi2 = [&](auto J) {
    if constexpr (J.value < B + 4) gelu_ops<true, gcum(J.value - B + 23, 27), gcum(J.value - B + 24, 27)>(p.y, p.g2, p.om);
    if constexpr (J.value == B + 5 || J.value == B + 6) h_store<J.value - B - 5>(c, p.y, p.u2, 1);
  };
  if constexpr (R == 0) {
    run<R, 140, 140 + XP, 999>(p, [&](auto J) {
      epi2(J);
      if constexpr (NPRE && J.value == 139 + XP - NPRE) { PIN2(p.h1[10], p.h1[11]); PIN2(p.h1[12], p.h1[13]); }
      if constexpr (J.value >= 140 + XP - NPRE) unit_half<71 - (J.value - 140 - XP + NPRE) / 2, J.value & 1>(c);
    });
    STAMP(19);
    wait_bar_v<NPRE>();
    STAMP(20);
    flag_publish(c, 2);
    run<R, 140 + XP, 172, 999>(p, [&](auto J) { if constexpr (J.value - 140 - XP + NPRE < 8) unit_half<71 - (J.value - 140 - XP + NPRE) / 2, J.value & 1>(c); });
    STAMP(21);
    __builtin_amdgcn_s_sleep(PSLEEP);
    flag_spin(c, __builtin_amdgcn_raw_buffer_load_b64(c.sw, vflag2, 0, 16), vflag2);
    STAMP(22);
    ld_h<0, NB0>(c, p.h2, l16h, p.hbase);
    run<R, 172, 200, 999>(p, [&](auto J) {
      if constexpr (J.value - 172 + NB0 < 14) ld_h<J.value - 172 + NB0, J.value - 171 + NB0>(c, p.h2, l16h, p.hbase);
      if constexpr (J.value == 187) { asm volatile("s_barrier" ::: "memory"); STAMP(23); }
      if constexpr (J.value >= OPOS && J.value < OPOS + 8) ld_1k<J.value - OPOS, J.value - OPOS + 1>(c, p.of, obase);
    });
  } else {
    __builtin_amdgcn_s_setprio(1);
    run<R, 168, 172, 999>(p, epi2);
    fm_slices<0, 2>(p);
    SB0();
    h_store<0>(c, p.y, p.u2, 1); h_store<1>(c, p.y, p.u2, 1);
    SB0();
    STAMP(19);
    fmq<3>(p, [&](auto K) {
      if constexpr (K.value == XQ) {
        wait_bar_v<0>();

        ld_1k<0, 4>(c, p.kv, WS_KVR + (u32)c.hd * 4096);
      }
      if constexpr (K.value > XQ && K.value <= XQ + H1Q) ld_h<K.value - XQ - 1, K.value - XQ>(c, p.h1, c.l16, p.hbase);
    });
    STAMP(21);
    chk<0, 4>(p.kv, [&] { ld_1k<0, 4>(c, p.kv, WS_KVR + (u32)c.hd * 4096); });
    o_publish(c, p.kv, p.ph, p.gq);
    __builtin_amdgcn_s_setprio(0);
    STAMP(22);
    u32x2 f2;
    run<R, 172, C2, 999>(p, [&](auto J) {
      if constexpr (J.value - 172 + H1Q < 14) ld_h<J.value - 172 + H1Q, J.value - 171 + H1Q>(c, p.h1, c.l16, p.hbase);
      if constexpr (J.value == P2) f2 = __builtin_amdgcn_raw_buffer_load_b64(c.sw, vflag2, 0, 16);
    });
    flag_spin(c, f2, vflag2);
    STAMP(24);
        ld_h<0, NB1>(c, p.h2, l16h, p.hbase);
    run<R, C2, 228, 999>(p, [&](auto J) {
      if constexpr (J.value - C2 + NB1 < 14) ld_h<J.value - C2 + NB1, J.value - C2 + NB1 + 1>(c, p.h2, l16h, p.hbase);
      if constexpr (J.value == YR1) { asm volatile("s_barrier" ::: "memory"); STAMP(23); }
      if constexpr (J.value >= O1 && J.value < O1 + 8) ld_1k<J.value - O1, J.value - O1 + 1>(c, p.of, obase);
    });
  }
  STAMP(27);
  chk<0, 8>(p.of, [&] { ld_1k<0, 8>(c, p.of, obase); });
  STAMP(28);
  run<R, B + 60, B + 76, 999>(p, none);
  STAMP(29);
  f32x16 &keep = p.c[0], &give = p.c[1];
  u32x4 pp[2];
  auto ld_p = [&] { pp[0] = ld_dev(c, c.l16, c.op ^ (8 * 16384)); pp[1] = ld_dev(c, c.l16 + 1024, c.op ^ (8 * 16384)); };
  if constexpr (R == 1) ld_p();
  const u32 xa = spos(2) + c.l16;
#pragma unroll
  for (int g = 0; g < 4; ++g) *(LAS f32x4*)(xa + c.w * 4096 + g * 1024) = (f32x4){give[4 * g], give[4 * g + 1], give[4 * g + 2], give[4 * g + 3]};
  {
    volatile LAS u32* fl = (volatile LAS u32*)(spos(1) + 4096);
    asm volatile("s_waitcnt lgkmcnt(0)" ::: "memory");
    fl[c.w] = 1u;
    u32 spin = 0;
    while (__builtin_amdgcn_readfirstlane(fl[c.w ^ 2]) == 0 && ++spin < (1u << 22)) {}
    asm volatile("" ::: "memory");
  }
#pragma unroll
  for (int g = 0; g < 4; ++g) {
    f32x4 v = *(LAS f32x4*)(xa + (c.w ^ 2) * 4096 + g * 1024);
#pragma unroll
    for (int i = 0; i < 4; ++i) keep[4 * g + i] += v[i];
  }
  if constexpr (R == 0) {
    st_dev(c, sanit(pack8<0>(keep)), c.l16, c.op);
    st_dev(c, sanit(pack8<1>(keep)), c.l16 + 1024, c.op);
    STAMP(30);
  } else {
    PIN1(keep);
    chk<0, 2>(pp, ld_p);
    STAMP(30);
#pragma unroll
    for (int d = 0; d < 4; ++d) {
      keep[2 * d] += blo(pp[0][d]); keep[2 * d + 1] += bhi(pp[0][d]);
      keep[8 + 2 * d] += blo(pp[1][d]); keep[9 + 2 * d] += bhi(pp[1][d]);
    }
    const u32 vo = (l & 31) * 896 + h * 16, oo = (c.t * 128 + c.kh * 64 + tg2 * 32) * 896 + c.jj * 128 + ks * 64;
#pragma unroll
    for (int s = 0; s < 2; ++s) {
      u32x4 v = s ? pack8<1>(keep) : pack8<0>(keep);
      auto a = __builtin_amdgcn_permlane32_swap(v[0], v[2], false, false);
      auto b = __builtin_amdgcn_permlane32_swap(v[1], v[3], false, false);
      u32x4 o = {a[0], b[0], a[1], b[1]};
      __builtin_amdgcn_raw_buffer_store_b128(o, mk(c.out, 2048 * 896), vo + s * 32, oo, OUTAUX);
      asm volatile("s_nop 1" :: "v"(o) : "memory");
    }
    if (__builtin_amdgcn_workgroup_id_x() == 0 && c.w == 4) {
      u32x2 v = {(u32)c.tag, (u32)(c.tag >> 32)};
      __builtin_amdgcn_raw_buffer_store_b64(v, c.sw, l == 0 ? 0u : OOB, 0, 16);
    }
    STAMP(31);
  }
}

template <int R, int A0, int A, int B, int ST> DI void pipe(P1& p) {
  if constexpr (A - 3 * ST < B) {
    if constexpr (A - 3 * ST >= A0) mm_item<R, A - 3 * ST>(p);
    if constexpr (A < B) rd_item<R, A>(p);
    SB0();
    pipe<R, A0, A + ST, B, ST>(p);
  }
}
constexpr u32 sxpos(int ch) { return spos(2 * ch - 2); }
template <int N, int E> DI void spare_x(const Ctx& c) {
  if constexpr (N < E) { set_m0<sxpos(N >> 1) + (N & 1) * UNIT>(c); asm volatile("s_nop 0"); dma<0>((N & 1) ? c.vx2 : c.vx, c.sx, c.rot[N >> 1] * 128); spare_x<N + 1, E>(c); }
}
template <int R> DI void spare_wg(Ctx& c, P1& p) {
  const u32 l = c.lane;
  set_m0<spos(12)>(c); asm volatile("s_nop 0"); dma<0>(c.l16, mk(c.gw, 896), c.w == 0 ? 0u : 0x40000000u);
  const u32 okvr = WS_KVR + (u32)(c.t * 4096 + (2 * c.kh + (c.w & 1)) * 1024);
  const u32x4 ones = {~0u, ~0u, ~0u, ~0u};
  st_dev(c, ones, R == 0 || c.tg < 2 ? c.l16 : OOB, R == 0 ? c.okvp : okvr);
  spare_x<2, 4>(c);
  asm volatile("s_waitcnt vmcnt(4)" ::: "memory");
  st_dev(c, lds_rd128(spos(17) + c.m0w + c.l16), c.vrp, 0);
  spare_x<4, 14>(c);
  wait_bar<0>();
  if constexpr (R == 0) flag_publish(c, 0);
  if (!OL)
  {
    const u32 mo = WS_O + ((c.t * 4 + c.tg) * 16 + 8 * c.kh + 4 * R) * 2048;
#pragma unroll
    for (int i = 0; i < 8; ++i) st_dev(c, ones, c.l16 + (i & 3) * 1024, mo + (i >> 2) * 4096);
  }
#pragma unroll
  for (int s = 0; s < 28; ++s) p.xf[s] = as_frag(lds_rd128((p.ax ^ (u32)(32 * (s & 3))) + sxpos(s >> 2)));
  STAMP(1);
  f32x16 ga = {};
#pragma unroll
  for (int s = 14 * R; s < 14 * R + 14; ++s) ga = mfma(as_frag(lds_rd128(spos(12) + c.rot[s >> 2] * 128 + (s & 3) * 32 + (l >> 5) * 16)), p.xf[s], ga);
  PIN1(ga);
  float g1 = 0.f;
  if constexpr (R == 0) *(LAS float*)(spos(17) + c.tg * 256 + l * 4) = ga[0];
  u32x2 f0 = __builtin_amdgcn_raw_buffer_load_b64(c.sw, c.vflag0, 0, 16);
  bar_l();
  if constexpr (R == 1) {
    g1 = __builtin_amdgcn_rcpf(1.0f + __builtin_amdgcn_exp2f(-LOG2E * (ga[0] + *(LAS float*)(spos(17) + c.tg * 256 + l * 4))));
    __builtin_amdgcn_raw_buffer_store_b64((u32x2){f2u(g1), (u32)c.tag}, c.sw, l < 32 ? l * 8 : OOB, c.og, 16);
  }
  flag_spin(c, f0, c.vflag0);
  issue_range<R, 29, 36>(c); issue_range<R, 28, 29>(c);
  issue_range<R, 39, 40>(c); issue_range<R, 42, 43>(c);
  p.a0 = p.a1 = (f32x16){};
  wait_bar<6>();
  STAMP(2);
  issue_range<R, 36, 37>(c);
  if (OL)
  {
    const u32 mo = WS_O + ((c.t * 4 + c.tg) * 16 + 8 * c.kh + 4 * R) * 2048;
#pragma unroll
    for (int i = 0; i < 8; ++i) st_dev(c, ones, c.l16 + (i & 3) * 1024, mo + (i >> 2) * 4096);
  }
  pipe<R, 44, 44, 60, 1>(p);
  wait_bar_v<0>();
  if constexpr (R == 0) flag_publish(c, 1);
  issue_range<R, 45, 46>(c);
  wphi_rd(c, p);
  pipe<R, 60, 60, 72, 1>(p);
  wphi_pack(p);
  wait_bar_v<0>();
  if constexpr (R == 0) { fm_slices<0, 24>(p); phi_image<0>(c, p); phi_image<1>(c, p); }
  else { v_handoff<spos(1), 0>(c, p.a1); v_handoff<spos(1), 1>(c, p.a1); pipe<R, 85, 85, 140, 2>(p); PIN1(p.a0); }
  bar_l();
  if constexpr (R == 0) {
    if (c.w >= 2) {
      if (PFS) {
        const u32 e = (c.t & 3) * 2 + (c.w & 1);
#pragma unroll 1
        for (u32 i = 0; i < 13; ++i) { touch<spos(13), false>(l * 128 + i * 8192, mk(c.wd + c.kh * 802816 + e * 100352, 100352), 0); __builtin_amdgcn_s_sleep(PFS); }
#pragma unroll 1
        for (u32 i = 0; i < 7; ++i) { touch<spos(13), false>(l * 128 + i * 8192, mk(c.wo + e * 50176, 50176), 0); __builtin_amdgcn_s_sleep(PFS); }
      }
      return;
    }
    kv_stage<spos(1), 0>(c, p); kv_stage<spos(1), 1>(c, p); kv_stage<spos(1), 2>(c, p); kv_stage<spos(1), 3>(c, p);
    kv_stage<spos(1), 4>(c, p); kv_stage<spos(1), 5>(c, p); kv_stage<spos(1), 7>(c, p);
    STAMP(3);
    flag_spin(c, __builtin_amdgcn_raw_buffer_load_b64(c.sw, c.vflag, 0, 16), c.vflag);
    STAMP(4);
    u32x4 v[16];
    const u32 ro = WS_KVP + (u32)(c.t * 65536 + (2 * c.kh + c.w) * 1024);
    ld_lin<0, 16>(c, v, ro, 4096);
    chk<0, 16>(v, [&] { ld_lin<0, 16>(c, v, ro, 4096); });
    STAMP(5);
    float s[8], s2[8];
    set8(s, v[0]); set8(s2, v[1]);
#pragma unroll
    for (int i = 2; i < 16; i += 2) { add8(s, v[i]); add8(s2, v[i + 1]); }
#pragma unroll
    for (int i = 0; i < 8; ++i) s[i] += s2[i];
    st_dev(c, sanit(pack8<0>(s)), c.l16, okvr);
    STAMP(6);
  } else {
    fm_slices<0, 24>(p);
    flag_spin(c, __builtin_amdgcn_raw_buffer_load_b64(c.sw, c.vflag, 0, 16), c.vflag);
    u32x4 v[4];
    const u32 kvo = WS_KVR + (u32)c.hd * 4096;
    ld_1k<0, 4>(c, v, kvo);
    chk<0, 4>(v, [&] { __builtin_amdgcn_s_sleep(8); ld_1k<0, 4>(c, v, kvo); });
    o_publish(c, v, p.ph, g1);
    STAMP(7);
  }
}

extern "C" __global__ __launch_bounds__(512, 2) void fused_kernel(const char* x, const char* wqkv, const char* wphi, const char* wo, const char* wg,
                                                                   const char* wu, const char* wd, const char* gw, char* out, char* ws) {
  Ctx c;
  const int bid = __builtin_amdgcn_workgroup_id_x(), xcd = bid & 7, idx = bid >> 3;
  const u32 tid = __builtin_amdgcn_workitem_id_x();
  c.gw = gw; c.wd = wd; c.wo = wo; c.out = out; c.ws = ws;
  c.t = 4 * (xcd >> 1) + (idx & 3); c.kh = xcd & 1; c.jj = idx >> 2; c.hd = 8 * c.kh + c.jj; c.j = 7 * c.kh + c.jj;
  c.w = __builtin_amdgcn_readfirstlane(tid >> 6); c.tg = c.w & 3; c.r = c.w >> 2;
  c.lane = tid & 63; c.l16 = c.lane * 16; c.m0w = c.w * 1024;
  const u32 l = c.lane, w = c.w;
  STAMP(0);
  const u64 rt = 0x2106543210654321ull, r0 = RJ * c.jj + RT * (idx & 3) + RX * (xcd >> 1) + RK * c.kh;
  const u32 rs = (u32)(rt >> (4 * (r0 > 9 ? r0 - 7 : r0)));
#pragma unroll
  for (int i = 0; i < 7; ++i) c.rot[i] = (rs >> (4 * i)) & 7;
  const u32 row8 = 8 * w + (l >> 3), sw2 = ((l & 7) ^ (((l >> 4) & 1) << 2)) << 4;
  c.vx = row8 * 896 + (((l & 7) ^ ((4 * w + (l >> 4)) & 7)) << 4); c.vx2 = c.vx + 64 * 896;
  c.vw = row8 * 3584 + sw2;
  asm volatile("" : "+v"(c.vx), "+v"(c.vx2), "+v"(c.vw));
  asm volatile("" : "+s"(c.rot[0]), "+s"(c.m0w));
  SB0();
  const u64 epoch = *(const __attribute__((address_space(4))) u64*)ws;
  c.sx = mk(x + c.t * 114688, 114688);
  c.sg = mk(wg + c.j * 256, 448 * 3584 - c.j * 256);
  if (c.jj < 7) issue_range<0, 0, 3>(c);
  else {
    u32 lo; u64 pc;
    asm volatile("s_getpc_b64 %0\n.Lpf_here:\n\ts_mov_b32 %1, .Lpf_here-fused_kernel" : "=s"(pc), "=s"(lo));
    asm volatile("s_mov_b32 m0, %0\n\ts_nop 0\n\tbuffer_load_dword %1, %2, 0 offen lds"
                 :: "i"(spos(13)), "v"((w * 64 + l) * 128), "s"(mk((const char*)(pc - lo), CODE_BYTES)) : "memory");
    spare_x<0, 2>(c);
  }
  {
    const u32 win = 56 * c.hd - 8 * (c.hd & 1), i = (w & 1) * 64 + l;
    const bool ok = i < 112 && w < 6;
    set_m0<spos(17)>(c); asm volatile("s_nop 0");
    dma<0>(ok ? (i >> 2) * 2688 + (i & 3) * 16 : OOB, mk(wqkv, 448 * 2688), c.t * (28 * 2688) + (w >> 1) * 896 + win);
    c.vrp = ok ? WS_WA + c.hd * 86016 + (w >> 1) * 28672 + c.t * 1792 + i * 16 : OOB;
  }
  SB0();
  c.tag = epoch + 1;
  c.v2 = row8 * 896 + sw2; c.v2b = c.v2 + 64 * 896;
  c.vq = WS_WA + c.hd * 86016 + c.tg * 1024 + c.l16; c.vkv = c.vq + 28672 * (1 + c.r);
  c.vflag0 = WS_FLAG + (((l & 15) * 16 + c.hd) << 7);
  c.sp = mk(wphi + c.hd * 1568, 1568);
  c.sw = mk(ws, WS_END);
  c.oh = WS_H + (u32)(((c.t * 4 + c.tg) * 112 + 8 * c.j + 2 * c.r) * 1024);
  c.oo = WS_O + (u32)(((c.t * 4 + c.tg) * 16 + c.hd) * 2048);
  c.op = WS_P + (u32)((c.t * 16 + c.hd) * 16384 + c.tg * 2048);
  c.okvp = WS_KVP + (u32)((c.hd * 16 + c.t) * 4096 + c.tg * 1024);
  c.oflag = WS_FLAG + (u32)(c.t * 16 + c.hd) * 128;
  c.og = WS_G + (u32)(c.kh * 2048 + c.t * 128 + c.tg * 32) * 8;
  if (XB & 1) c.op = 0x40000000u;
  if (XB & 2) c.oh = 0x40000000u;
  if (XB & 8) c.oo = 0x40000000u;
  static_assert(WS_P % (16 * 16384) == 0, "the partner's slot is op ^ (8 * 16384)");
  c.vflag = flag_off(c, c.jj == 7); c.vone = tid == 0 ? 0u : OOB;
  { const u32 rho = l & 31, f = rho < 26 ? rho : ((rho == 28 || rho == 29) ? rho - 2 : 0);
    c.awp = upos(28) + 5 * 1024 + 2 * (28 * (4 * (l >> 5) - 4 * (c.hd & 1)) + f); }
  asm volatile("" : "+s"(c.tag), "+s"(c.oh), "+s"(c.oo), "+s"(c.op), "+s"(c.okvp), "+s"(c.oflag), "+s"(c.og) :: "memory");
  asm volatile("" : "+v"(c.vflag), "+v"(c.vflag0), "+v"(c.vone), "+v"(c.vrp));
  P1 p;
  {
    const u32 q = (l & 15) >> 2, pp = l & 3, blk = (l >> 4) & 1, h = l >> 5, r = l & 31;
    p.aw = mkseg((8 * h + q) * 128 + ((64 * c.r) ^ ((q >> 1) << 6)) + 32 * blk + 8 * pp);
    p.aq = mkseg((8 * h + q) * 64 + 32 * blk + 8 * pp);
    p.ax = (32 * c.tg + r) * 128 + ((h ^ ((r >> 1) & 7)) << 4);
  }
  if (c.jj < 7) {
    c.su = mk(wu + c.j * 256, (XB & 32) ? 0 : 448 * 3584 - c.j * 256);
    c.sd = mk(wd + c.kh * 896 * 896 + c.jj * 128, (XB & 16) ? 0 : 896 * 896 - c.jj * 128);
    c.so = mk(wo + c.kh * 224 * 896 + c.jj * 128, (XB & 16) ? 0 : 224 * 896 - c.jj * 128);
    if (c.r == 0) { ffn_wg<0>(c, p); STAMP_END(); __builtin_amdgcn_endpgm(); }
    ffn_wg<1>(c, p);
    STAMP_END();
    __builtin_amdgcn_endpgm();
  }
  if (c.r == 0) { spare_wg<0>(c, p); STAMP_END(); __builtin_amdgcn_endpgm(); }
  spare_wg<1>(c, p);
  STAMP_END();
}

#if defined(__HIP_DEVICE_COMPILE__)
#pragma clang attribute pop
#endif

extern "C" void kernel_launch(void* const* d_in, const int* in_sizes, int n_in, void* d_out, int out_size, void* d_ws, size_t ws_size,
                              hipStream_t stream) {
  (void)hipFuncSetAttribute((const void*)fused_kernel, hipFuncAttributeMaxDynamicSharedMemorySize, LDS_BYTES);
  fused_kernel<<<256, 512, LDS_BYTES, stream>>>((const char*)d_in[0], (const char*)d_in[1], (const char*)d_in[2], (const char*)d_in[3],
                                                (const char*)d_in[5], (const char*)d_in[6], (const char*)d_in[7], (const char*)d_in[8],
                                                (char*)d_out, (char*)d_ws);
}
```

```cpp
#include <hip/hip_runtime.h>
#include <hip/hip_bf16.h>
#include <type_traits>
#pragma clang diagnostic ignored "-Wint-to-pointer-cast"

typedef unsigned u32;
typedef unsigned long long u64;
typedef u32 u32x4 __attribute__((ext_vector_type(4)));
typedef u32 u32x2 __attribute__((ext_vector_type(2)));
typedef short s16x4 __attribute__((ext_vector_type(4)));
typedef __bf16 bf16x8 __attribute__((ext_vector_type(8)));
typedef __bf16 bf16x2 __attribute__((ext_vector_type(2)));
typedef float f32x16 __attribute__((ext_vector_type(16)));
typedef float f32x4 __attribute__((ext_vector_type(4)));
typedef float f32x2 __attribute__((ext_vector_type(2)));
typedef __amdgpu_buffer_rsrc_t rsrc_t;
#define DI __device__ __forceinline__
#define LAS __attribute__((address_space(3)))
#define SB0() __builtin_amdgcn_sched_barrier(0)
#define PIN1(a) asm volatile("" : "+v"(a))
#define PIN2(a, b) asm volatile("" : "+v"(a), "+v"(b))

constexpr u32 WS_FLAG = 4096;
constexpr u32 WS_G    = 131072;
constexpr u32 WS_KVR  = 65536;
constexpr u32 WS_KVP  = 262144;
constexpr u32 WS_O    = WS_KVP + 256 * 4096;
constexpr u32 WS_P    = WS_O + 64 * 32 * 1024;
constexpr u32 WS_H    = WS_P + 256 * 16384;
constexpr u32 WS_WA   = WS_H + 64 * 112 * 1024;
constexpr u32 WS_END  = WS_WA + 16 * 3 * 28672;

constexpr int UNIT = 8192, RING = 18;
constexpr u32 SCR = 0, RBASE = 2 * UNIT;
constexpr int LDS_BYTES = 163840;
constexpr u32 OOB = 0x80000000u;
constexpr u32 CODE_BYTES = 40960;
#define ABL 0
#define ORD 1
#define A4 9
#define SWP 1
#define DELAYS 99
#define NOGELU 0
#define NOFM 0
#define I7 33
#define I8 36
#define I9 38
#define I10 41
#define I11 44
#define I12 47
#define I18 62
#define NOMM 0
#define RJ 1
#define RT 2
#define RX 1
#define RK 0
#define XB 0
#define PH 7
#define OL 1
#define OUTAUX 0
#define NB0 10
#define C2 196
#define P2 186
#define NPRE 4
#define NB1 2
#define YR1 199
#define O1 204
#define PROV 9
#define POLL0 5
#define POLL1 17
#define GATE 11
#define PFS 0
#define TOUCH1 0
#define TOUCH2 0
#define XP 24
#define XQ 14
#define H1Q 6
#define H1R 10
#define PSLEEP 4
#define OPOS 184
#define YP 10
#define CHK1 19
#define FLAG0 2
#define I19 65
#define I20 68
constexpr u32 SPIN_MAX = ABL ? 0 : 1u << 16;
constexpr float LOG2E = 1.4426950408889634f;

#if defined(__HIP_DEVICE_COMPILE__)
#pragma clang attribute push(__attribute__((target("no-packed-fp32-ops"))), apply_to = function)
#endif
constexpr u32 spos(int k) { return RBASE + k * UNIT; }
constexpr int w2c_(int i) { constexpr int t[18] = {16, 17, 0, 1, 2, 3, 4, 5, 6, 7, 8, 9, 10, 11, 12, 13, 14, 15}; return t[i]; }
constexpr int w2s_(int c) { constexpr int t[18] = {2, 3, 4, 5, 6, 10, 11, 12, 13, 15, 14, 16, 7, 8, -2, -1, 0, 1}; return t[c]; }
constexpr int uslot(int n) {
  constexpr int t[26] = {-1, 10, 11, 12, 13, 14, 15, 16, 17, 0, 1, 2, 3, 4, 5, 6, 10, 11, 12, 13, 14, 16, 7, 8, 9, 17};
  return n < 28 ? n % RING : n < 54 ? t[n - 28] : w2s_(w2c_(n - 54));
}
constexpr u32 upos(int n) { return spos(uslot(n)); }
constexpr u32 VBUF = RBASE + 15 * UNIT;
constexpr int imin(int a, int b) { return a < b ? a : b; }

struct Ctx {
  const char *gw, *wd, *wo;
  char *out, *ws;
  int t, kh, jj, hd, j, w, tg, r;
  u32 lane, l16, m0w;
  u32 vx, vx2, vw, vq, vkv, v2, v2b;
  rsrc_t sx, sg, su, sq, sp, sd, so, sw;
  u64 tag;
  u32 rot[7];
  u32 oh, oo, op, okvp;
  u32 oflag, og;
  u32 vrp, vflag0;
  u32 vflag, awp, vone;
};

DI rsrc_t mk(const void* p, u32 bytes) { return __builtin_amdgcn_make_buffer_rsrc((void*)p, 0, bytes, 0x00020000); }
template <u32 L> DI void set_m0(const Ctx& c) { if (!(ABL & 1)) asm volatile("s_add_u32 m0, %0, %1" :: "s"(c.m0w), "i"(L) : "memory", "scc"); }
DI void dma_dev(u32 voff, rsrc_t rs, u32 soff) {
  if (!(ABL & 1)) asm volatile("buffer_load_dwordx4 %0, %1, %2 offen sc1 lds" :: "v"(voff), "s"(rs), "s"(soff) : "memory");
}
template <u32 OFF> DI void dma(u32 voff, rsrc_t rs, u32 soff) {
  if (!(ABL & 1)) asm volatile("buffer_load_dwordx4 %0, %1, %2 offen offset:%3 lds" :: "v"(voff), "s"(rs), "s"(soff), "i"(OFF) : "memory");
}
template <int N> DI void wait_bar() { asm volatile("s_waitcnt vmcnt(%0) lgkmcnt(0)\n\ts_barrier" :: "n"(N) : "memory"); }
template <int N> DI void wait_bar_v() { asm volatile("s_waitcnt vmcnt(%0)\n\ts_barrier" :: "n"(N) : "memory"); }
DI void bar_l() { asm volatile("s_waitcnt lgkmcnt(0)\n\ts_barrier" ::: "memory"); }

constexpr u32 ioff_(int n) { return n >= 36 && n < 54 && !(n < 47 && n % 3 == 0) ? 128 : 0; }
template <int N> DI void unit_m0(const Ctx& c) { set_m0<upos(N) - ioff_(N)>(c); }
template <int R, int N> DI void unit_go(const Ctx& c) {
  if constexpr (N < 21) {
    if constexpr (N % 3 < 2) dma<0>(N % 3 ? c.vx2 : c.vx, c.sx, c.rot[N / 3] * 128);
    else dma<0>(c.vw, c.sg, c.rot[N / 3] * (64 * 3584));
  } else if constexpr (N >= 29 && N < 36) dma_dev(c.vkv, c.sw, c.rot[N - 29] * 4096);
  else if constexpr (N == 28) dma<0>(c.l16, c.sp, c.w == 5 ? 0u : c.w == 6 ? 1024u : 0x40000000u);
  else if constexpr (N < 28) dma<0>(c.vw, c.su, c.rot[N - 21] * (64 * 3584));
  else if constexpr (N < 47) {
    if constexpr (N % 3 == 0) dma_dev(c.vq, c.sw, N == 45 && R == 1 ? 0x40000000u : c.rot[(2 * (N - 36) / 3 + R) % 7] * 4096);
    else dma<128>(c.vw, c.sg, c.rot[2 * ((N - 36) / 3) + N % 3 - 1] * (64 * 3584));
  } else if constexpr (N < 54) dma<128>(c.vw, c.su, c.rot[N - 47] * (64 * 3584));
  else {
    constexpr int k = w2c_(N - 54);
    if constexpr (k < 14) dma<0>(k < 7 ? c.v2 : c.v2b, c.sd, c.rot[k % 7] * (128 * 896));
    else dma<0>((k & 1) ? c.v2b : c.v2, c.so, k < 16 ? 0u : 128u * 896u);
  }
}
template <int N, int H> DI void unit_half(const Ctx& c) {
  set_m0<upos(N) + H * 4096>(c);
  constexpr int k = w2c_(N - 54);
  const u32 so = (k < 14 ? c.rot[k % 7] * (128 * 896) : k < 16 ? 0u : 128u * 896u) + H * (32 * 896);
  asm volatile("s_nop 0");
  dma<0>((k < 14 ? k >= 7 : (k & 1)) ? c.v2b : c.v2, k < 14 ? c.sd : c.so, so);
}
template <int R, int A, int B> DI void issue_range(const Ctx& c) {
  if constexpr (A < B) { unit_m0<A>(c); asm volatile("s_nop 0"); unit_go<R, A>(c); issue_range<R, A + 1, B>(c); }
}

struct Seg { u32 b[3]; };
DI Seg mkseg(u32 a) { Seg s; s.b[0] = a; s.b[1] = a + 65536; s.b[2] = a + 131072; asm volatile("" : "+v"(s.b[0]), "+v"(s.b[1]), "+v"(s.b[2])); return s; }
DI u32 sa(const Seg& s, u32 off) { return s.b[off >> 16] + (off & 0xffffu); }
DI f32x16 mfma(bf16x8 a, bf16x8 b, f32x16 c) { if (NOMM) { asm volatile("" : "+v"(c) : "v"(a), "v"(b)); return c; } return __builtin_amdgcn_mfma_f32_32x32x16_bf16(a, b, c, 0, 0, 0); }
DI bf16x8 as_frag(u32x4 v) { return __builtin_bit_cast(bf16x8, v); }
DI bf16x8 tr_frag(u32 a0, u32 a1) {
  if (NOMM > 1) { bf16x8 z; asm volatile("" : "=v"(z)); return z; }
  s16x4 lo = __builtin_amdgcn_ds_read_tr16_b64_v4i16((LAS s16x4*)a0);
  s16x4 hi = __builtin_amdgcn_ds_read_tr16_b64_v4i16((LAS s16x4*)a1);
  return __builtin_bit_cast(bf16x8, __builtin_shufflevector(lo, hi, 0, 1, 2, 3, 4, 5, 6, 7));
}
DI u32x4 lds_rd128(u32 a) { return *(LAS u32x4*)a; }
DI void lds_wr128(u32 a, u32x4 v) { *(LAS u32x4*)a = v; }
DI u32 f2u(float f) { return __builtin_bit_cast(u32, f); }
DI float u2f(u32 u) { return __builtin_bit_cast(float, u); }
DI u32 pk(float a, float b) { f32x2 t = {a, b}; return __builtin_bit_cast(u32, __builtin_convertvector(t, bf16x2)); }
template <int S, class V> DI u32x4 pack8(const V& v) {
  u32x4 r = {pk(v[8 * S], v[8 * S + 1]), pk(v[8 * S + 2], v[8 * S + 3]), pk(v[8 * S + 4], v[8 * S + 5]), pk(v[8 * S + 6], v[8 * S + 7])};
  return r;
}
DI float blo(u32 d) { return u2f(d << 16); }
DI float bhi(u32 d) { return u2f(d & 0xffff0000u); }
DI u32 umax(u32 a, u32 b) { return a > b ? a : b; }
DI bool any_(bool p) { return __builtin_amdgcn_ballot_w64(p) != 0; }
DI u32x4 sanit(u32x4 v) { return __builtin_elementwise_min(v, (u32x4){0xfffffffeu, 0xfffffffeu, 0xfffffffeu, 0xfffffffeu}); }
DI void st_dev(const Ctx& c, u32x4 v, u32 voff, u32 soff) {
  if (ABL & 8) { asm volatile("" :: "v"(v)); return; }
  __builtin_amdgcn_raw_buffer_store_b128(v, c.sw, voff, soff, 16);
  asm volatile("s_nop 1" :: "v"(v) : "memory");
}
DI u32x4 ld_dev(const Ctx& c, u32 voff, u32 soff) {
  if (ABL & 16) { u32x4 z = {voff, soff, voff, soff}; asm volatile("" : "+v"(z)); return z; }
  return __builtin_amdgcn_raw_buffer_load_b128(c.sw, voff, soff, 16);
}
DI f32x2 xhalf(float v) { auto p = __builtin_amdgcn_permlane32_swap(f2u(v), f2u(v), false, false); f32x2 r = {u2f(p[0]), u2f(p[1])}; return r; }

#define STAMP(i)
#define STAMP_END()

struct P1 {
  Seg aw, aq; u32 ax, ka, kb, kc;
  bf16x8 xf[28], f[8], af[8], vf[8];
  f32x16 g1, u1, a0, a1, g2, u2, fp, k0, c[2];
  Seg s2[2]; u32 ob[2];
  u32x4 h2[14], of[8];
  u32x4 wp[2], ph[4], h1[14], kv[4];
  u32 wr[16];
  float y[16], e[14], sum, mxl, mnl;
  u32x2 poll;
  float gq, om;
  u32 hbase;
};

DI void wphi_rd(const Ctx& c, P1& p) {
#pragma unroll
  for (int i = 0; i < 16; ++i) p.wr[i] = *(LAS unsigned short*)(c.awp + 56 * (16 * (i >> 3) + 8 * ((i >> 2) & 1) + (i & 3)));
}
DI void wphi_pack(P1& p) {
#pragma unroll
  for (int i = 0; i < 8; ++i) p.wp[i >> 2][i & 3] = p.wr[2 * i] | (p.wr[2 * i + 1] << 16);
  PIN2(p.wp[0], p.wp[1]);
}
template <int K> DI void fm_slice(P1& p) {
  if constexpr (NOFM) { if constexpr (K == 0) { p.ph[0] = p.ph[2] = pack8<0>(p.a0); p.ph[1] = p.ph[3] = pack8<1>(p.a0); PIN2(p.ph[0], p.ph[1]); PIN2(p.ph[2], p.ph[3]); } }
  else if constexpr (K == 0) { p.fp = mfma(as_frag(p.wp[0]), as_frag(pack8<0>(p.a0)), (f32x16){}); }
  else if constexpr (K == 1) { p.fp = mfma(as_frag(p.wp[1]), as_frag(pack8<1>(p.a0)), p.fp); }
  else if constexpr (K == 2) { }
  else if constexpr (K == 3) {
    PIN1(p.fp);
    float mx = p.fp[0];
#pragma unroll
    for (int i = 1; i < 14; ++i) mx = __builtin_fmaxf(mx, p.fp[i]);
    p.mxl = mx; PIN1(p.mxl);
  } else if constexpr (K == 4) {
    float mn = p.fp[0];
#pragma unroll
    for (int i = 1; i < 14; ++i) mn = __builtin_fminf(mn, p.fp[i]);
    p.mnl = mn; PIN1(p.mnl);
  } else if constexpr (K == 5) {
    f32x2 t = xhalf(p.mxl); p.mxl = __builtin_fmaxf(t[0], t[1]) * LOG2E;
    t = xhalf(p.mnl); p.mnl = __builtin_fminf(t[0], t[1]) * LOG2E;
    PIN2(p.mxl, p.mnl);
  } else {
    constexpr int NEG = K >= 15, k = K - 6 - 9 * NEG;
    if constexpr (k < 5) {
#pragma unroll
      for (int i = 3 * k; i < imin(14, 3 * k + 3); ++i) {
        p.e[i] = __builtin_amdgcn_exp2f(NEG ? __builtin_fmaf(p.fp[i], -LOG2E, p.mnl) : __builtin_fmaf(p.fp[i], LOG2E, -p.mxl));
        PIN1(p.e[i]);
      }
      if constexpr (k == 1) p.sum = p.e[0] + p.e[1] + p.e[2];
      if constexpr (k > 1) p.sum += p.e[3 * k - 3] + p.e[3 * k - 2] + p.e[3 * k - 1];
      if constexpr (k > 0) PIN1(p.sum);
    } else if constexpr (k == 5) {
      f32x2 t = xhalf(p.sum + p.e[12] + p.e[13]); p.sum = __builtin_amdgcn_rcpf(t[0] + t[1]); PIN1(p.sum);
    } else if constexpr (k < 8) {
#pragma unroll
      for (int i = 7 * (k - 6); i < 7 * (k - 5); ++i) { p.e[i] *= p.sum; PIN1(p.e[i]); }
    } else {
      u32x4 o1 = {pk(p.e[8], p.e[9]), pk(p.e[10], p.e[11]), pk(p.e[12], p.e[13]), 0u};
      p.ph[2 * NEG] = pack8<0>(p.e); p.ph[2 * NEG + 1] = o1; PIN2(p.ph[2 * NEG], p.ph[2 * NEG + 1]);
    }
  }
}
template <int A, int B> DI void fm_slices(P1& p) { if constexpr (A < B) { fm_slice<A>(p); fm_slices<A + 1, B>(p); } }
template <u32 VB, int S> DI void v_handoff(const Ctx& c, const f32x16& av) {
  u32x4 vf = pack8<S>(av);
  const bool one = (c.lane & 31) == ((c.hd & 1) ? 0u : 28u);
#pragma unroll
  for (int d = 0; d < 4; ++d) vf[d] = one ? 0x3f803f80u : vf[d];
  lds_wr128(VB + c.tg * 2048 + c.l16 + S * 1024, vf);
}
template <int T> DI void phi_image(const Ctx& c, P1& p) {
  const u32 tok = c.lane & 31, h = c.lane >> 5;
  const u32 ft = (((tok >> 1) & 1) << 3) | (tok & 1) | (((tok >> 2) & 3) << 1);
  const u32 wa = SCR + c.tg * 4096 + tok * 128 + ((h ^ ft) << 3);
#pragma unroll
  for (int g = 0; g < 4; ++g) {
    u32x2 v = {p.ph[2 * T + (g >> 1)][2 * (g & 1)], p.ph[2 * T + (g >> 1)][2 * (g & 1) + 1]};
    *(LAS u32x2*)(wa ^ (u32)((8 * T + 2 * g) << 3)) = v;
  }
}
template <u32 VB, int K> DI void kv_stage(const Ctx& c, P1& p) {
  if constexpr (K >= 2 && K <= 5) {
    p.k0 = mfma(p.af[2 * K - 4], p.vf[2 * K - 4], K == 2 ? (f32x16){} : p.k0);
    p.k0 = mfma(p.af[2 * K - 3], p.vf[2 * K - 3], p.k0);
  }
  if constexpr (K == 0) {
    const u32 l = c.lane, h = l >> 5, q = (l & 15) >> 2, pp = l & 3, blk = (l >> 4) & 1;
    p.ka = SCR + (4 * h + q) * 128 + (((4 * blk + pp) ^ (q & 1) ^ (h << 1) ^ ((q >> 1) << 3) ^ ((c.tg & 1) << 3)) << 3);
    p.kb = p.ka ^ 32u; p.kc = VB + c.l16;
    asm volatile("" : "+v"(p.ka), "+v"(p.kb), "+v"(p.kc));
  }
  if constexpr (K < 4) {
#pragma unroll
    for (int s = 0; s < 2; ++s) {
      p.af[2 * K + s] = tr_frag(p.ka + K * 4096 + s * 2048, p.kb + K * 4096 + s * 2048 + 1024);
      p.vf[2 * K + s] = as_frag(lds_rd128(p.kc + K * 2048 + s * 1024));
    }
  }
  if constexpr (K == 7) {
    const u32 vo = c.tg < 2 ? c.l16 : OOB;
    st_dev(c, sanit(pack8<0>(p.k0)), vo, c.okvp + c.tg * 1024);
    st_dev(c, sanit(pack8<1>(p.k0)), vo + 1024, c.okvp + c.tg * 1024);
  }
}
DI void add8(float (&s)[8], u32x4 v) {
#pragma unroll
  for (int d = 0; d < 4; ++d) { s[2 * d] += blo(v[d]); s[2 * d + 1] += bhi(v[d]); }
}
DI void set8(float (&s)[8], u32x4 v) {
#pragma unroll
  for (int d = 0; d < 4; ++d) { s[2 * d] = blo(v[d]); s[2 * d + 1] = bhi(v[d]); }
}
template <int A, int B, int N> DI bool has_mark(const u32x4 (&v)[N]) {
  u32 m = 0;
#pragma unroll
  for (int i = A; i < B; ++i) m = umax(umax(m, v[i][0]), umax(v[i][1], umax(v[i][2], v[i][3])));
  return any_(m == 0xffffffffu);
}
template <int A, int B, int N, class F> DI void chk(u32x4 (&v)[N], F&& reload) {
  if (__builtin_expect(SPIN_MAX && has_mark<A, B>(v), 0)) {
    u32 spin = 0;
    do { asm volatile("" ::: "memory"); reload(); } while (has_mark<A, B>(v) && ++spin < SPIN_MAX);
  }
}
template <int A, int B, int N> DI void ld_lin(const Ctx& c, u32x4 (&v)[N], u32 soff, u32 stride) {
#pragma unroll
  for (int i = A; i < B; ++i) v[i] = ld_dev(c, c.l16, soff + i * stride);
}
template <int A, int B, int N> DI void ld_1k(const Ctx& c, u32x4 (&v)[N], u32 soff) {
#pragma unroll
  for (int i = A; i < B; ++i) v[i] = ld_dev(c, c.l16 + (i & 3) * 1024, soff + (i >> 2) * 4096);
}
template <int A, int B> DI void ld_h(const Ctx& c, u32x4 (&v)[14], u32 voff, u32 soff) {
#pragma unroll
  for (int i = A; i < B; ++i) v[i] = ld_dev(c, voff + (i & 1) * 1024, soff + c.rot[i >> 1] * 8192);
}
DI void o_publish(const Ctx& c, const u32x4 (&kv)[4], const u32x4 (&phq)[4], float g) {
  f32x16 n = {};
#pragma unroll
  for (int i = 0; i < 4; ++i) n = mfma(as_frag(kv[i]), as_frag(phq[i]), n);
  const bool odd = c.hd & 1;
  f32x2 t = xhalf(odd ? n[0] : n[12]);
  float sc = g * __builtin_amdgcn_rcpf((odd ? t[0] : t[1]) + 1e-6f);
#pragma unroll
  for (int i = 0; i < 16; ++i) n[i] *= sc;
  u32x4 f0 = sanit(pack8<0>(n)), f1 = sanit(pack8<1>(n));
  const bool z0 = odd && c.lane < 32, z1 = !odd && c.lane >= 32;
  f0[0] = z0 ? 0 : f0[0]; f0[1] = z0 ? 0 : f0[1];
  f1[2] = z1 ? 0 : f1[2]; f1[3] = z1 ? 0 : f1[3];
  st_dev(c, f0, c.l16, c.oo);
  st_dev(c, f1, c.l16 + 1024, c.oo);
}
DI u32 flag_off(const Ctx& c, bool spare) {
  const u32 i = c.lane & 15;
  u32 red = (c.hd * 16 + 7 + 8 * (i & 1)) << 7, sp = ((c.t * 16 + 7 + 8 * c.kh) << 7) + 8;
  u32 lo = spare ? (i * 16 + c.t) << 7 : ((c.t * 16 + (i < 7 ? 8 * c.kh + i : c.hd ^ 8)) << 7) + 8;
  asm volatile("" : "+v"(red), "+v"(sp), "+v"(lo));
  return WS_FLAG + ((c.lane & 16) ? red : (spare || i < 8) ? lo : sp);
}
DI bool flag_bad(const Ctx& c, u32x2 f) { return any_(f[0] != (u32)c.tag || f[1] != (u32)(c.tag >> 32)); }
DI void flag_spin(const Ctx& c, u32x2 f, u32 voff) {
  if (__builtin_expect(SPIN_MAX && flag_bad(c, f), 0)) {
    u32 spin = 0;
    do {
      __builtin_amdgcn_s_sleep(2);
      asm volatile("" ::: "memory");
      f = __builtin_amdgcn_raw_buffer_load_b64(c.sw, voff, 0, 16);
    } while (flag_bad(c, f) && ++spin < SPIN_MAX);
  }
  asm volatile("" ::: "memory");
}
DI void flag_publish(const Ctx& c, u32 word) {
  u32x2 v = {(u32)c.tag, (u32)(c.tag >> 32)};
  __builtin_amdgcn_raw_buffer_store_b64(v, c.sw, c.vone + word * 8, c.oflag, 16);
}
DI u32x2 ld_g(const Ctx& c) { return __builtin_amdgcn_raw_buffer_load_b64(c.sw, (c.lane & 31) * 8, c.og, 16); }

template <u32 L, bool DEV> DI void touch(u32 voff, rsrc_t rs, u32 soff) {
  if (ABL & 9) return;
  if constexpr (DEV) asm volatile("s_mov_b32 m0, %0\n\ts_nop 0\n\tbuffer_load_dword %1, %2, %3 offen sc1 lds" :: "i"(L), "v"(voff), "s"(rs), "s"(soff) : "memory");
  else asm volatile("s_mov_b32 m0, %0\n\ts_nop 0\n\tbuffer_load_dword %1, %2, %3 offen lds" :: "i"(L), "v"(voff), "s"(rs), "s"(soff) : "memory");
}

constexpr int PRO = PROV;
constexpr int NS = 21;
constexpr int need_(int s) {
  return s < 0 ? 0 : s < 7 ? 3 * (s + 1) : s < 9 ? 23 + 2 * (s - 7) : s < 12 ? 31 + 2 * (s - 9) : s < 14 ? 36 : s < 18 ? 39 + 3 * (s - 14) : s < 20 ? 50 + 2 * (s - 18) : 62;
}
constexpr int iss_(int s) {
  constexpr int t[14] = {I7, I8, I9, I10, I11, I12, 49, 51, 53, 56, 59, I18, I19, I20};
  return s < 0 ? PRO : s < 7 ? imin(imin(29, 18 + need_(s - 2)), PRO + 3 * (s + 1) + (s >= A4 ? s - A4 + 1 : 0)) : t[s - 7];
}
constexpr int ustep(int n) {
  if (n == 34 || n == 28) return 16;
  if (n >= 25 && n < 28) return n == 25 ? 12 : 13;
  int s = 0; while (s < 20 && need_(s) <= n) ++s; return s;
}
constexpr bool ring_ok() {
  for (int s = -1; s < NS; ++s)
    for (int n = s < 0 ? 0 : iss_(s - 1); n < iss_(s); ++n) {
      if (uslot(n) == -2 && s < 18) return false;
      for (int m = 0; m < n; ++m) if (uslot(m) == uslot(n) && ustep(m) + 2 > s) return false;
    }
  return true;
}
static_assert(ring_ok(), "ring schedule");
constexpr int S_FLAG0 = FLAG0, S_POLL0 = POLL0, S_CHK0 = 7, S_GATE = GATE, S_CHKG = 13, S_FLAG1 = 16, S_POLL = POLL1, S_CHK = CHK1;
constexpr int oth_(int r, int s) {
  return s == S_FLAG0 ? 1 - r : s == S_POLL0 ? 1 - r : s == 10 ? (GATE == 10) : s == 11 ? 1 - r + (GATE == 11) : s == 12 ? r : s == 14 ? 2 : s == 16 ? 3 - 3 * r + TOUCH1 :
         s == S_POLL ? 1 - r : s == 18 ? (r || CHK1 == 19 ? 0 : 4) : s == 19 || s == 20 ? (r ? 0 : CHK1 == 19 ? 7 : 5) : 0;
}
constexpr int tot_(int r, int s) { return iss_(s) - iss_(s - 1) + oth_(r, s); }
constexpr int vm_(int r, int s) {
  int sp = -1; while (iss_(sp) < need_(s)) ++sp;
  int n = iss_(sp) - need_(s) + (sp < 0 ? 2 - r : 0);
  for (int k = sp + 1; k < s; ++k) n += tot_(r, k);
  if (s == S_FLAG0) { int m = 0; for (int k = 0; k < s; ++k) m += tot_(r, k); n = imin(n, m); }
  if (s == S_FLAG1) n = imin(n, tot_(r, 15));
  if (((s == S_CHK0 || s == S_CHK) && r == 0) || s == S_CHKG) {
    int m = -1; for (int k = s == S_CHK0 ? S_POLL0 : s == S_CHKG ? S_GATE : S_POLL; k < s; ++k) m += tot_(r, k);
    n = imin(n, m);
  }
  return n;
}
constexpr int P2_(int r) { return r ? 168 : 140; }
constexpr int item0_(int r, int s) { return s < 7 ? 4 * s : (!r || s < 15) ? 8 * s - 28 : s < 18 ? 84 + 16 * (s - 14) : 8 * s; }
template <int R, int J> DI void rd_item(P1& p) {
  constexpr int U2 = P2_(R) - 28;
  if constexpr (J >= P2_(R)) {
    constexpr int k = J - P2_(R);
    if constexpr (k < 56) {
      constexpr u32 o = spos(w2s_(k / 4)) + ((k / 2) & 1) * 2048;
      p.f[J % 8] = tr_frag(sa(p.s2[k & 1], o), sa(p.s2[k & 1], o + 1024));
    } else if constexpr (k < 72) {
      constexpr int i = (k - 56) / 2, hh = i >> 1;
      constexpr u32 o = (28 * hh - 4 * (hh & 1) + 16 * (i & 1)) * 128;
      p.f[J % 8] = tr_frag(p.ob[k & 1] + o, p.ob[k & 1] + o + 1024);
    }
  } else if constexpr ((J >= 44 && J < 72) || (R == 1 && J >= 84 && J < U2 && (J & 1))) {
    constexpr int k = J < 72 ? J - 44 : (J - 84) / 2, ch = k / 4;
    constexpr u32 o = (J < 72 ? upos(29 + ch) + R * 4096 : upos(36 + 3 * (ch / 2)) + (ch & 1) * 4096) + (k % 4) * 1024;
    p.f[J % 8] = tr_frag(sa(p.aq, o), sa(p.aq, o + 256));
  } else {
    constexpr int k = J < 84 ? J : J < U2 ? (J - 84) / (1 + R) : J - U2, ch = k / 4;
    constexpr u32 o = upos(J < 28 ? 3 * ch + 2 : J < 44 ? 14 + ch : J < 84 ? 7 + ch : J < U2 ? 37 + 3 * (ch / 2) + (ch & 1) : 47 + ch) + (k % 4) * 2048;
    p.f[J % 8] = tr_frag(sa(p.aw, o), sa(p.aw, o + 512));
  }
}
template <int R, int J> DI void mm_item(P1& p) {
  constexpr int U2 = P2_(R) - 28;
  if constexpr (J < 0) {}
  else if constexpr (J >= P2_(R)) {
    constexpr int k = J - P2_(R);
    p.c[k & 1] = mfma(p.f[J % 8], as_frag(k < 28 ? p.h1[(k / 2) % 14] : k < 56 ? p.h2[(k / 2) % 14] : p.of[(k / 2 + 4) % 8]), p.c[k & 1]);
  } else if constexpr (J >= 44 && J < 72) {
    if constexpr (R == 1) p.a1 = mfma(p.xf[J - 44], p.f[J % 8], p.a1); else p.a0 = mfma(p.f[J % 8], p.xf[J - 44], p.a0);
  } else {
    f32x16& acc = J < 28 ? p.g1 : J < 84 ? p.u1 : J >= U2 ? p.u2 : (R == 1 && (J & 1)) ? p.a0 : p.g2;
    acc = mfma(p.f[J % 8], p.xf[J < 84 ? J % 28 : J < U2 ? (J - 84) / (1 + R) : J - U2], acc);
  }
}
template <int R, int A, int B> DI void mm_range(P1& p) { if constexpr (A < B) { mm_item<R, A>(p); mm_range<R, A + 1, B>(p); } }

constexpr float GK1 = -2.0f * 0.7978845608028654f * LOG2E, GK2 = GK1 * 0.044715f;
template <bool SC, int OP> DI void gelu_op(float (&y)[16], const f32x16& g, float om) {
  constexpr int st = OP % 4, i = OP / 4 - st;
  if constexpr (NOGELU && i >= 0 && i < 16) { if constexpr (st == 3) { y[i] = g[i]; PIN1(y[i]); } }
  else if constexpr (i >= 0 && i < 16) {
    if constexpr (st == 0) y[i] = g[i] * __builtin_fmaf(g[i] * g[i], GK2, GK1);
    else if constexpr (st == 1) y[i] = __builtin_amdgcn_exp2f(y[i]);
    else if constexpr (st == 2) y[i] = __builtin_amdgcn_rcpf(1.0f + y[i]);
    else y[i] = SC ? (g[i] * om) * y[i] : g[i] * y[i];
    PIN1(y[i]);
  }
}
constexpr int gcost(int op) { return op / 4 - op % 4 < 0 || op / 4 - op % 4 > 15 ? 0 : op % 4 == 0 ? 3 : op % 4 == 1 ? 2 : op % 4 == 2 ? 3 : 1; }
constexpr int gcum(int k, int n) { if (k >= n) return 76; int op = 0, cst = 0; while (cst * n < k * 144) cst += gcost(op++); return op; }
template <bool SC, int A, int B> DI void gelu_ops(float (&y)[16], const f32x16& g, float om) { if constexpr (A < B) { gelu_op<SC, A>(y, g, om); gelu_ops<SC, A + 1, B>(y, g, om); } }
template <int S> DI void h_store(const Ctx& c, const float (&y)[16], const f32x16& u, int blk) {
  float h[8];
#pragma unroll
  for (int i = 0; i < 8; ++i) h[i] = y[8 * S + i] * u[8 * S + i];
  st_dev(c, pack8<0>(h), c.l16 + S * 1024, c.oh + blk * 4096);
}
template <int A, int B> DI void y_scale(P1& p) {
#pragma unroll
  for (int i = A; i < B; ++i) { p.y[i] *= p.om; PIN1(p.y[i]); }
}
DI void addr2(const Ctx& c, P1& p) {
  const u32 l = c.lane, ks = c.tg >> 1, qq = (l & 15) >> 2;
  const u32 bk = ((4 * (l >> 5) + qq) * 128 + ((qq >> 1) << 6) + 32 * ((l >> 4) & 1) + 8 * (l & 3)) ;
  p.s2[0] = mkseg(bk + ks * 4096); p.s2[1] = mkseg((bk ^ 64) + ks * 4096);
  p.ob[0] = bk + ks * (112 * 128); p.ob[1] = (bk ^ 64) + ks * (112 * 128);
  PIN2(p.ob[0], p.ob[1]);
}
template <int R, int S, int K, int J> DI void slot_work(Ctx& c, P1& p) {
  if constexpr (S < 7) p.xf[J] = as_frag(lds_rd128((p.ax + upos(3 * S)) ^ (u32)(32 * K)));
  if constexpr (R == 0) {
    if constexpr (J >= 33 && J < 75) gelu_ops<false, gcum(J - 33, 42), gcum(J - 32, 42)>(p.y, p.g1, 0.f);
    if constexpr (J == 70) wphi_rd(c, p);
    if constexpr (J == 76) wphi_pack(p);
    if constexpr (J == 78 || J == 79) y_scale<8 * (J - 78), 8 * (J - 77)>(p);
    constexpr int F0 = J < 84 ? J - 77 : J < 89 ? 7 + (3 * (J - 84) + 1) / 2 : J < 91 ? 15 : 15 + (9 * (J - 91) + 7) / 8;
    constexpr int F1 = J + 1 < 84 ? J - 76 : J + 1 < 89 ? 7 + (3 * (J - 83) + 1) / 2 : J + 1 < 91 ? 15 : 15 + (9 * (J - 90) + 7) / 8;
    if constexpr (J >= 77 && J < 99) fm_slices<F0, imin(24, F1)>(p);
    if constexpr (J == 92) phi_image<0>(c, p);
    if constexpr (J == 99) phi_image<1>(c, p);
    if constexpr (S == 16) kv_stage<VBUF, K>(c, p);
    if constexpr (CHK1 == 19 && S >= 19 && K >= 1) ld_h<7 * (S - 19) + K - 1, 7 * (S - 19) + K>(c, p.h1, c.l16, p.hbase);
    if constexpr (CHK1 == 18 && S == 18 && (K & 1)) ld_h<K / 2, K / 2 + 1>(c, p.h1, c.l16, p.hbase);
    if constexpr (CHK1 == 18 && S >= 19 && ((K & 1) || K == 4)) { constexpr int i = 4 + 5 * (S - 19) + (K < 4 ? K / 2 : (K + 1) / 2); ld_h<i, i + 1>(c, p.h1, c.l16, p.hbase); }
  } else {
    if constexpr (J >= 33 && J < 77) gelu_ops<false, gcum(J - 33, 44), gcum(J - 32, 44)>(p.y, p.g1, 0.f);
    if constexpr (J == 77 || J == 78) v_handoff<VBUF, J - 77>(c, p.a1);
    if constexpr (J == 79) wphi_rd(c, p);
    if constexpr (J == 83) wphi_pack(p);
    if constexpr (J >= 84 && J < 88) y_scale<4 * (J - 84), 4 * (J - 83)>(p);
  }
  if constexpr (J == 89 || J == 90) h_store<J - 89>(c, p.y, p.u1, 0);
  if constexpr (J >= P2_(R) - 23) gelu_ops<true, gcum(J - P2_(R) + 23, 27), gcum(J - P2_(R) + 24, 27)>(p.y, p.g2, p.om);
  if constexpr (J == P2_(R) - 26) addr2(c, p);
  if constexpr (TOUCH1 && S == S_FLAG1 && K == 6) touch<upos(45) + 4096, true>(c.lane < 16 ? c.lane * 128 : OOB, c.sw, c.oh);
  if constexpr (S == 11 + R && K == 3) st_dev(c, (u32x4){~0u, ~0u, ~0u, ~0u}, c.l16 + R * 1024, c.op);
}
template <int R, int S, int J0, int K, int N> DI void slots(Ctx& c, P1& p) {
  if constexpr (K < N) {
    constexpr int nu = iss_(S) - iss_(S - 1), u0 = iss_(S - 1);
    constexpr int ua = K == 0 ? 0 : ((2 * K + R) * nu) / (2 * N), ub = K == N - 1 ? nu : ((2 * K + 2 + R) * nu) / (2 * N);
    if constexpr (R == 1 && S >= 18 && (K == 0 || K == 4)) __builtin_amdgcn_s_setprio(K == 0);
    mm_item<R, J0 + K - 4>(p);
    if (ORD) SB0();
    if constexpr (ub > ua) unit_m0<u0 + ua>(c);
    rd_item<R, (J0 + K) ^ SWP>(p);
    if constexpr (ub > ua) { unit_go<R, u0 + ua>(c); issue_range<R, u0 + ua + 1, u0 + ub>(c); }
    if (ORD) SB0();
    slot_work<R, S, K, J0 + K>(c, p);
    SB0();
    slots<R, S, J0, K + 1, N>(c, p);
  }
}
template <int R, int S> DI void step(Ctx& c, P1& p) {
  if constexpr ((S == S_CHK || S == S_CHK0) && R == 0) {
    asm volatile("s_waitcnt vmcnt(%1)" : "+v"(p.poll) : "n"(vm_(R, S)) : "memory");
    PIN1(S == S_CHK ? p.u2 : p.g1);
    flag_spin(c, p.poll, S == S_CHK ? c.vflag : c.vflag0);
    asm volatile("s_barrier" ::: "memory");
  } else if constexpr (S == S_CHKG) {
    asm volatile("s_waitcnt vmcnt(%1)\n\ts_barrier" : "+v"(p.poll) : "n"(vm_(R, S)) : "memory");
    {
      PIN2(p.u1, R ? p.a1 : p.a0);
      if (__builtin_expect(SPIN_MAX && any_(p.poll[1] != (u32)c.tag), 0)) {
        u32 spin = 0;
        do { __builtin_amdgcn_s_sleep(2); asm volatile("" ::: "memory"); p.poll = ld_g(c); } while (any_(p.poll[1] != (u32)c.tag) && ++spin < SPIN_MAX);
      }
      p.gq = u2f(p.poll[0]); p.om = 1.0f - p.gq;
      PIN2(p.gq, p.om);
    }
  } else if constexpr ((S == 16 && R == 0) || (S == 20 && NPRE > 4)) wait_bar<vm_(R, S)>();
  else wait_bar_v<vm_(R, S)>();
  if constexpr (S < 2 || S >= 6) STAMP(S < 2 ? S + 1 : S - 3);
  if constexpr (S == DELAYS) __builtin_amdgcn_s_sleep(16);
  if constexpr (S == S_FLAG0 && R == 0) flag_publish(c, 0);
  if constexpr (S == S_FLAG1 && R == 0) flag_publish(c, 1);
  if constexpr ((S == S_POLL || S == S_POLL0) && R == 0) asm volatile("buffer_load_dwordx2 %0, %1, %2, 0 offen sc1" : "=v"(p.poll) : "v"(S == S_POLL ? c.vflag : c.vflag0), "s"(c.sw) : "memory");
  if constexpr (S == S_GATE) asm volatile("buffer_load_dwordx2 %0, %1, %2, %3 offen sc1" : "=v"(p.poll) : "v"((c.lane & 31) * 8), "s"(c.sw), "s"(c.og) : "memory");
  SB0();
  slots<R, S, item0_(R, S), 0, item0_(R, S + 1) - item0_(R, S)>(c, p);
}
template <int R, int S, int E> DI void steps(Ctx& c, P1& p) { if constexpr (S < E) { step<R, S>(c, p); steps<R, S + 1, E>(c, p); } }
template <int R, int A, int B, int RE, class F> DI void run(P1& p, F&& extra) {
  if constexpr (A < B) {
    mm_item<R, A - 4>(p);
    if (ORD) SB0();
    if constexpr (A < RE) rd_item<R, A ^ 1>(p);
    if (ORD) SB0();
    extra(std::integral_constant<int, A>{});
    SB0();
    run<R, A + 1, B, RE>(p, extra);
  } else { PIN2(p.c[0], p.c[1]); }
}
template <int K, class F> DI void fmq(P1& p, F&& extra) {
  if constexpr (K < 24) { fm_slice<K>(p); extra(std::integral_constant<int, K>{}); SB0(); fmq<K + 1>(p, extra); }
}
template <int R, int J> DI void rd4(P1& p) { rd_item<R, J + 1>(p); rd_item<R, J>(p); rd_item<R, J + 3>(p); rd_item<R, J + 2>(p); }

template <int R> DI void ffn_wg(Ctx& c, P1& p) {
  const u32 l = c.lane;
  const int tg2 = c.tg & 1, ks = c.tg >> 1;
  const int TG = 2 * (R == 1 ? c.kh : 1 - c.kh) + tg2;
  p.hbase = WS_H + (u32)(((c.t * 4 + TG) * 112 + 56 * c.kh + 2 * ks) * 1024);
  if (XB & 4) p.hbase = 0x40000000u;
  asm volatile("" : "+s"(p.hbase));
  PIN1(c.awp);
  SB0();
  issue_range<R, 3, PRO>(c);
  asm volatile("s_waitcnt vmcnt(%0)" :: "n"(PRO - 3) : "memory");
  st_dev(c, lds_rd128(spos(17) + c.m0w + c.l16), c.vrp, 0);
  if constexpr (R == 0) st_dev(c, (u32x4){~0u, ~0u, ~0u, ~0u}, c.l16, c.okvp);
  p.g1 = p.u1 = p.a0 = p.a1 = p.g2 = p.u2 = p.c[0] = p.c[1] = (f32x16){};
  steps<R, 0, NS>(c, p);
  STAMP(18);
  auto none = [](auto) {};
  constexpr int B = P2_(R);
  const u32 h = l >> 5;
  const u32 obase = ((XB & 8) ? 0x40000000u : WS_O) + (u32)(((c.t * 4 + TG) * 16 + 8 * c.kh + 4 * ks) * 2048);
  const u32 l16h = c.l16 + 4096, vflag2 = c.vflag + ((l & 31) < 7 ? 8 : 0);
  auto epi2 = [&](auto J) {
    if constexpr (J.value < B + 4) gelu_ops<true, gcum(J.value - B + 23, 27), gcum(J.value - B + 24, 27)>(p.y, p.g2, p.om);
    if constexpr (J.value == B + 5 || J.value == B + 6) h_store<J.value - B - 5>(c, p.y, p.u2, 1);
  };
  if constexpr (R == 0) {
    run<R, 140, 140 + XP, 999>(p, [&](auto J) {
      epi2(J);
      if constexpr (NPRE && J.value == 139 + XP - NPRE) { PIN2(p.h1[10], p.h1[11]); PIN2(p.h1[12], p.h1[13]); }
      if constexpr (J.value >= 140 + XP - NPRE) unit_half<71 - (J.value - 140 - XP + NPRE) / 2, J.value & 1>(c);
    });
    STAMP(19);
    wait_bar_v<NPRE>();
    STAMP(20);
    flag_publish(c, 2);
    run<R, 140 + XP, 172, 999>(p, [&](auto J) { if constexpr (J.value - 140 - XP + NPRE < 8) unit_half<71 - (J.value - 140 - XP + NPRE) / 2, J.value & 1>(c); });
    STAMP(21);
    __builtin_amdgcn_s_sleep(PSLEEP);
    flag_spin(c, __builtin_amdgcn_raw_buffer_load_b64(c.sw, vflag2, 0, 16), vflag2);
    STAMP(22);
    ld_h<0, NB0>(c, p.h2, l16h, p.hbase);
    run<R, 172, 200, 999>(p, [&](auto J) {
      if constexpr (J.value - 172 + NB0 < 14) ld_h<J.value - 172 + NB0, J.value - 171 + NB0>(c, p.h2, l16h, p.hbase);
      if constexpr (J.value == 187) { asm volatile("s_barrier" ::: "memory"); STAMP(23); }
      if constexpr (J.value >= OPOS && J.value < OPOS + 8) ld_1k<J.value - OPOS, J.value - OPOS + 1>(c, p.of, obase);
    });
  } else {
    __builtin_amdgcn_s_setprio(1);
    run<R, 168, 172, 999>(p, epi2);
    fm_slices<0, 2>(p);
    SB0();
    h_store<0>(c, p.y, p.u2, 1); h_store<1>(c, p.y, p.u2, 1);
    SB0();
    STAMP(19);
    fmq<3>(p, [&](auto K) {
      if constexpr (K.value == XQ) {
        wait_bar_v<0>();

        ld_1k<0, 4>(c, p.kv, WS_KVR + (u32)c.hd * 4096);
      }
      if constexpr (K.value > XQ && K.value <= XQ + H1Q) ld_h<K.value - XQ - 1, K.value - XQ>(c, p.h1, c.l16, p.hbase);
    });
    STAMP(21);
    chk<0, 4>(p.kv, [&] { ld_1k<0, 4>(c, p.kv, WS_KVR + (u32)c.hd * 4096); });
    o_publish(c, p.kv, p.ph, p.gq);
    __builtin_amdgcn_s_setprio(0);
    STAMP(22);
    u32x2 f2;
    run<R, 172, C2, 999>(p, [&](auto J) {
      if constexpr (J.value - 172 + H1Q < 14) ld_h<J.value - 172 + H1Q, J.value - 171 + H1Q>(c, p.h1, c.l16, p.hbase);
      if constexpr (J.value == P2) f2 = __builtin_amdgcn_raw_buffer_load_b64(c.sw, vflag2, 0, 16);
    });
    flag_spin(c, f2, vflag2);
    STAMP(24);
        ld_h<0, NB1>(c, p.h2, l16h, p.hbase);
    run<R, C2, 228, 999>(p, [&](auto J) {
      if constexpr (J.value - C2 + NB1 < 14) ld_h<J.value - C2 + NB1, J.value - C2 + NB1 + 1>(c, p.h2, l16h, p.hbase);
      if constexpr (J.value == YR1) { asm volatile("s_barrier" ::: "memory"); STAMP(23); }
      if constexpr (J.value >= O1 && J.value < O1 + 8) ld_1k<J.value - O1, J.value - O1 + 1>(c, p.of, obase);
    });
  }
  STAMP(27);
  chk<0, 8>(p.of, [&] { ld_1k<0, 8>(c, p.of, obase); });
  STAMP(28);
  run<R, B + 60, B + 76, 999>(p, none);
  STAMP(29);
  u32x4 pp[2];
  auto ld_p = [&] { pp[0] = ld_dev(c, c.l16, c.op ^ (8 * 16384)); pp[1] = ld_dev(c, c.l16 + 1024, c.op ^ (8 * 16384)); };
  if constexpr (R == 1) ld_p();
  const u32 xw = R ? (c.tg ? spos(8) + c.tg * UNIT : spos(6)) : spos(2) + c.tg * UNIT;
  const u32 xo = R ? (c.tg != 2 ? spos(8) + (c.tg ^ 2) * UNIT : spos(6)) : spos(2) + (c.tg ^ 2) * UNIT;
  {
    const u32 wa = xw + (l & 31) * 256 + ((h ^ (l & 7)) << 4);
#pragma unroll
    for (int g = 0; g < 8; ++g) {
      const f32x16& v = p.c[g >> 2];
      *(LAS f32x4*)((wa ^ (u32)((g & 3) << 5)) + (g >> 2) * 128) = (f32x4){v[4 * (g & 3)], v[4 * (g & 3) + 1], v[4 * (g & 3) + 2], v[4 * (g & 3) + 3]};
    }
  }
  {
    volatile LAS u32* fl = (volatile LAS u32*)(spos(1) + 4096);
    asm volatile("s_waitcnt lgkmcnt(0)" ::: "memory");
    fl[c.w] = 1u;
    u32 spin = 0;
    while (__builtin_amdgcn_readfirstlane(fl[c.w ^ 2]) == 0 && ++spin < (1u << 22)) {}
    asm volatile("" ::: "memory");
  }
  float r16[16];
  {
    const u32 ra = (ks * 16 + (l >> 3)) * 256 + (l & 4) * 32 + (((2 * (l & 3)) ^ (l >> 3)) << 4);
#pragma unroll
    for (int q = 0; q < 4; ++q) {
      const u32 o = (ra ^ (u32)((q & 1) << 4)) + (q >> 1) * 2048;
      const f32x4 a = *(LAS f32x4*)(xw + o), b = *(LAS f32x4*)(xo + o);
#pragma unroll
      for (int i = 0; i < 4; ++i) r16[4 * q + i] = a[i] + b[i];
    }
  }
  if constexpr (R == 0) {
    st_dev(c, sanit(pack8<0>(r16)), c.l16, c.op);
    st_dev(c, sanit(pack8<1>(r16)), c.l16 + 1024, c.op);
    STAMP(30);
  } else {
#pragma unroll
    for (int i = 0; i < 16; ++i) PIN1(r16[i]);
    chk<0, 2>(pp, ld_p);
    STAMP(30);
#pragma unroll
    for (int d = 0; d < 4; ++d) {
      r16[2 * d] += blo(pp[0][d]); r16[2 * d + 1] += bhi(pp[0][d]);
      r16[8 + 2 * d] += blo(pp[1][d]); r16[9 + 2 * d] += bhi(pp[1][d]);
    }
    const u32 vo = (l >> 3) * 896 + (l & 7) * 16, oo = (c.t * 128 + c.kh * 64 + tg2 * 32 + ks * 16) * 896 + c.jj * 128;
#pragma unroll
    for (int s = 0; s < 2; ++s) {
      u32x4 o = s ? pack8<1>(r16) : pack8<0>(r16);
      __builtin_amdgcn_raw_buffer_store_b128(o, mk(c.out, 2048 * 896), vo, oo + s * (8 * 896), OUTAUX);
      asm volatile("s_nop 1" :: "v"(o) : "memory");
    }
    if (__builtin_amdgcn_workgroup_id_x() == 0 && c.w == 4) {
      u32x2 v = {(u32)c.tag, (u32)(c.tag >> 32)};
      __builtin_amdgcn_raw_buffer_store_b64(v, c.sw, l == 0 ? 0u : OOB, 0, 16);
    }
    STAMP(31);
  }
}

template <int R, int A0, int A, int B, int ST> DI void pipe(P1& p) {
  if constexpr (A - 3 * ST < B) {
    if constexpr (A - 3 * ST >= A0) mm_item<R, A - 3 * ST>(p);
    if constexpr (A < B) rd_item<R, A>(p);
    SB0();
    pipe<R, A0, A + ST, B, ST>(p);
  }
}
constexpr u32 sxpos(int ch) { return spos(2 * ch - 2); }
template <int N, int E> DI void spare_x(const Ctx& c) {
  if constexpr (N < E) { set_m0<sxpos(N >> 1) + (N & 1) * UNIT>(c); asm volatile("s_nop 0"); dma<0>((N & 1) ? c.vx2 : c.vx, c.sx, c.rot[N >> 1] * 128); spare_x<N + 1, E>(c); }
}
template <int R> DI void spare_wg(Ctx& c, P1& p) {
  const u32 l = c.lane;
  set_m0<spos(12)>(c); asm volatile("s_nop 0"); dma<0>(c.l16, mk(c.gw, 896), c.w == 0 ? 0u : 0x40000000u);
  const u32 okvr = WS_KVR + (u32)(c.t * 4096 + (2 * c.kh + (c.w & 1)) * 1024);
  const u32x4 ones = {~0u, ~0u, ~0u, ~0u};
  st_dev(c, ones, R == 0 || c.tg < 2 ? c.l16 : OOB, R == 0 ? c.okvp : okvr);
  spare_x<2, 4>(c);
  asm volatile("s_waitcnt vmcnt(4)" ::: "memory");
  st_dev(c, lds_rd128(spos(17) + c.m0w + c.l16), c.vrp, 0);
  spare_x<4, 14>(c);
  wait_bar<0>();
  if constexpr (R == 0) flag_publish(c, 0);
  if (!OL)
  {
    const u32 mo = WS_O + ((c.t * 4 + c.tg) * 16 + 8 * c.kh + 4 * R) * 2048;
#pragma unroll
    for (int i = 0; i < 8; ++i) st_dev(c, ones, c.l16 + (i & 3) * 1024, mo + (i >> 2) * 4096);
  }
#pragma unroll
  for (int s = 0; s < 28; ++s) p.xf[s] = as_frag(lds_rd128((p.ax ^ (u32)(32 * (s & 3))) + sxpos(s >> 2)));
  STAMP(1);
  f32x16 ga = {};
#pragma unroll
  for (int s = 14 * R; s < 14 * R + 14; ++s) ga = mfma(as_frag(lds_rd128(spos(12) + c.rot[s >> 2] * 128 + (s & 3) * 32 + (l >> 5) * 16)), p.xf[s], ga);
  PIN1(ga);
  float g1 = 0.f;
  if constexpr (R == 0) *(LAS float*)(spos(17) + c.tg * 256 + l * 4) = ga[0];
  u32x2 f0 = __builtin_amdgcn_raw_buffer_load_b64(c.sw, c.vflag0, 0, 16);
  bar_l();
  if constexpr (R == 1) {
    g1 = __builtin_amdgcn_rcpf(1.0f + __builtin_amdgcn_exp2f(-LOG2E * (ga[0] + *(LAS float*)(spos(17) + c.tg * 256 + l * 4))));
    __builtin_amdgcn_raw_buffer_store_b64((u32x2){f2u(g1), (u32)c.tag}, c.sw, l < 32 ? l * 8 : OOB, c.og, 16);
  }
  flag_spin(c, f0, c.vflag0);
  issue_range<R, 29, 36>(c); issue_range<R, 28, 29>(c);
  issue_range<R, 39, 40>(c); issue_range<R, 42, 43>(c);
  p.a0 = p.a1 = (f32x16){};
  wait_bar<6>();
  STAMP(2);
  issue_range<R, 36, 37>(c);
  if (OL)
  {
    const u32 mo = WS_O + ((c.t * 4 + c.tg) * 16 + 8 * c.kh + 4 * R) * 2048;
#pragma unroll
    for (int i = 0; i < 8; ++i) st_dev(c, ones, c.l16 + (i & 3) * 1024, mo + (i >> 2) * 4096);
  }
  pipe<R, 44, 44, 60, 1>(p);
  wait_bar_v<0>();
  if constexpr (R == 0) flag_publish(c, 1);
  issue_range<R, 45, 46>(c);
  wphi_rd(c, p);
  pipe<R, 60, 60, 72, 1>(p);
  wphi_pack(p);
  wait_bar_v<0>();
  if constexpr (R == 0) { fm_slices<0, 24>(p); phi_image<0>(c, p); phi_image<1>(c, p); }
  else { v_handoff<spos(1), 0>(c, p.a1); v_handoff<spos(1), 1>(c, p.a1); pipe<R, 85, 85, 140, 2>(p); PIN1(p.a0); }
  bar_l();
  if constexpr (R == 0) {
    if (c.w >= 2) {
      if (PFS) {
        const u32 e = (c.t & 3) * 2 + (c.w & 1);
#pragma unroll 1
        for (u32 i = 0; i < 13; ++i) { touch<spos(13), false>(l * 128 + i * 8192, mk(c.wd + c.kh * 802816 + e * 100352, 100352), 0); __builtin_amdgcn_s_sleep(PFS); }
#pragma unroll 1
        for (u32 i = 0; i < 7; ++i) { touch<spos(13), false>(l * 128 + i * 8192, mk(c.wo + e * 50176, 50176), 0); __builtin_amdgcn_s_sleep(PFS); }
      }
      return;
    }
    kv_stage<spos(1), 0>(c, p); kv_stage<spos(1), 1>(c, p); kv_stage<spos(1), 2>(c, p); kv_stage<spos(1), 3>(c, p);
    kv_stage<spos(1), 4>(c, p); kv_stage<spos(1), 5>(c, p); kv_stage<spos(1), 7>(c, p);
    STAMP(3);
    flag_spin(c, __builtin_amdgcn_raw_buffer_load_b64(c.sw, c.vflag, 0, 16), c.vflag);
    STAMP(4);
    u32x4 v[16];
    const u32 ro = WS_KVP + (u32)(c.t * 65536 + (2 * c.kh + c.w) * 1024);
    ld_lin<0, 16>(c, v, ro, 4096);
    chk<0, 16>(v, [&] { ld_lin<0, 16>(c, v, ro, 4096); });
    STAMP(5);
    float s[8], s2[8];
    set8(s, v[0]); set8(s2, v[1]);
#pragma unroll
    for (int i = 2; i < 16; i += 2) { add8(s, v[i]); add8(s2, v[i + 1]); }
#pragma unroll
    for (int i = 0; i < 8; ++i) s[i] += s2[i];
    st_dev(c, sanit(pack8<0>(s)), c.l16, okvr);
    STAMP(6);
  } else {
    fm_slices<0, 24>(p);
    flag_spin(c, __builtin_amdgcn_raw_buffer_load_b64(c.sw, c.vflag, 0, 16), c.vflag);
    u32x4 v[4];
    const u32 kvo = WS_KVR + (u32)c.hd * 4096;
    ld_1k<0, 4>(c, v, kvo);
    chk<0, 4>(v, [&] { __builtin_amdgcn_s_sleep(8); ld_1k<0, 4>(c, v, kvo); });
    o_publish(c, v, p.ph, g1);
    STAMP(7);
  }
}

extern "C" __global__ __launch_bounds__(512, 2) void fused_kernel(const char* x, const char* wqkv, const char* wphi, const char* wo, const char* wg,
                                                                   const char* wu, const char* wd, const char* gw, char* out, char* ws) {
  Ctx c;
  const int bid = __builtin_amdgcn_workgroup_id_x(), xcd = bid & 7, idx = bid >> 3;
  const u32 tid = __builtin_amdgcn_workitem_id_x();
  c.gw = gw; c.wd = wd; c.wo = wo; c.out = out; c.ws = ws;
  c.t = 4 * (xcd >> 1) + (idx & 3); c.kh = xcd & 1; c.jj = idx >> 2; c.hd = 8 * c.kh + c.jj; c.j = 7 * c.kh + c.jj;
  c.w = __builtin_amdgcn_readfirstlane(tid >> 6); c.tg = c.w & 3; c.r = c.w >> 2;
  c.lane = tid & 63; c.l16 = c.lane * 16; c.m0w = c.w * 1024;
  const u32 l = c.lane, w = c.w;
  STAMP(0);
  const u64 rt = 0x2106543210654321ull, r0 = RJ * c.jj + RT * (idx & 3) + RX * (xcd >> 1) + RK * c.kh;
  const u32 rs = (u32)(rt >> (4 * (r0 > 9 ? r0 - 7 : r0)));
#pragma unroll
  for (int i = 0; i < 7; ++i) c.rot[i] = (rs >> (4 * i)) & 7;
  const u32 row8 = 8 * w + (l >> 3), sw2 = ((l & 7) ^ (((l >> 4) & 1) << 2)) << 4;
  c.vx = row8 * 896 + (((l & 7) ^ ((4 * w + (l >> 4)) & 7)) << 4); c.vx2 = c.vx + 64 * 896;
  c.vw = row8 * 3584 + sw2;
  asm volatile("" : "+v"(c.vx), "+v"(c.vx2), "+v"(c.vw));
  asm volatile("" : "+s"(c.rot[0]), "+s"(c.m0w));
  SB0();
  const u64 epoch = *(const __attribute__((address_space(4))) u64*)ws;
  c.sx = mk(x + c.t * 114688, 114688);
  c.sg = mk(wg + c.j * 256, 448 * 3584 - c.j * 256);
  if (c.jj < 7) issue_range<0, 0, 3>(c);
  else {
    u32 lo; u64 pc;
    asm volatile("s_getpc_b64 %0\n.Lpf_here:\n\ts_mov_b32 %1, .Lpf_here-fused_kernel" : "=s"(pc), "=s"(lo));
    asm volatile("s_mov_b32 m0, %0\n\ts_nop 0\n\tbuffer_load_dword %1, %2, 0 offen lds"
                 :: "i"(spos(13)), "v"((w * 64 + l) * 128), "s"(mk((const char*)(pc - lo), CODE_BYTES)) : "memory");
    spare_x<0, 2>(c);
  }
  {
    const u32 win = 56 * c.hd - 8 * (c.hd & 1), i = (w & 1) * 64 + l;
    const bool ok = i < 112 && w < 6;
    set_m0<spos(17)>(c); asm volatile("s_nop 0");
    dma<0>(ok ? (i >> 2) * 2688 + (i & 3) * 16 : OOB, mk(wqkv, 448 * 2688), c.t * (28 * 2688) + (w >> 1) * 896 + win);
    c.vrp = ok ? WS_WA + c.hd * 86016 + (w >> 1) * 28672 + c.t * 1792 + i * 16 : OOB;
  }
  SB0();
  c.tag = epoch + 1;
  c.v2 = row8 * 896 + sw2; c.v2b = c.v2 + 64 * 896;
  c.vq = WS_WA + c.hd * 86016 + c.tg * 1024 + c.l16; c.vkv = c.vq + 28672 * (1 + c.r);
  c.vflag0 = WS_FLAG + (((l & 15) * 16 + c.hd) << 7);
  c.sp = mk(wphi + c.hd * 1568, 1568);
  c.sw = mk(ws, WS_END);
  c.oh = WS_H + (u32)(((c.t * 4 + c.tg) * 112 + 8 * c.j + 2 * c.r) * 1024);
  c.oo = WS_O + (u32)(((c.t * 4 + c.tg) * 16 + c.hd) * 2048);
  c.op = WS_P + (u32)((c.t * 16 + c.hd) * 16384 + c.tg * 2048);
  c.okvp = WS_KVP + (u32)((c.hd * 16 + c.t) * 4096 + c.tg * 1024);
  c.oflag = WS_FLAG + (u32)(c.t * 16 + c.hd) * 128;
  c.og = WS_G + (u32)(c.kh * 2048 + c.t * 128 + c.tg * 32) * 8;
  if (XB & 1) c.op = 0x40000000u;
  if (XB & 2) c.oh = 0x40000000u;
  if (XB & 8) c.oo = 0x40000000u;
  static_assert(WS_P % (16 * 16384) == 0, "the partner's slot is op ^ (8 * 16384)");
  c.vflag = flag_off(c, c.jj == 7); c.vone = tid == 0 ? 0u : OOB;
  { const u32 rho = l & 31, f = rho < 26 ? rho : ((rho == 28 || rho == 29) ? rho - 2 : 0);
    c.awp = upos(28) + 5 * 1024 + 2 * (28 * (4 * (l >> 5) - 4 * (c.hd & 1)) + f); }
  asm volatile("" : "+s"(c.tag), "+s"(c.oh), "+s"(c.oo), "+s"(c.op), "+s"(c.okvp), "+s"(c.oflag), "+s"(c.og) :: "memory");
  asm volatile("" : "+v"(c.vflag), "+v"(c.vflag0), "+v"(c.vone), "+v"(c.vrp));
  P1 p;
  {
    const u32 q = (l & 15) >> 2, pp = l & 3, blk = (l >> 4) & 1, h = l >> 5, r = l & 31;
    p.aw = mkseg((8 * h + q) * 128 + ((64 * c.r) ^ ((q >> 1) << 6)) + 32 * blk + 8 * pp);
    p.aq = mkseg((8 * h + q) * 64 + 32 * blk + 8 * pp);
    p.ax = (32 * c.tg + r) * 128 + ((h ^ ((r >> 1) & 7)) << 4);
  }
  if (c.jj < 7) {
    c.su = mk(wu + c.j * 256, (XB & 32) ? 0 : 448 * 3584 - c.j * 256);
    c.sd = mk(wd + c.kh * 896 * 896 + c.jj * 128, (XB & 16) ? 0 : 896 * 896 - c.jj * 128);
    c.so = mk(wo + c.kh * 224 * 896 + c.jj * 128, (XB & 16) ? 0 : 224 * 896 - c.jj * 128);
    if (c.r == 0) { ffn_wg<0>(c, p); STAMP_END(); __builtin_amdgcn_endpgm(); }
    ffn_wg<1>(c, p);
    STAMP_END();
    __builtin_amdgcn_endpgm();
  }
  if (c.r == 0) { spare_wg<0>(c, p); STAMP_END(); __builtin_amdgcn_endpgm(); }
  spare_wg<1>(c, p);
  STAMP_END();
}

#if defined(__HIP_DEVICE_COMPILE__)
#pragma clang attribute pop
#endif

extern "C" void kernel_launch(void* const* d_in, const int* in_sizes, int n_in, void* d_out, int out_size, void* d_ws, size_t ws_size,
                              hipStream_t stream) {
  (void)hipFuncSetAttribute((const void*)fused_kernel, hipFuncAttributeMaxDynamicSharedMemorySize, LDS_BYTES);
  fused_kernel<<<256, 512, LDS_BYTES, stream>>>((const char*)d_in[0], (const char*)d_in[1], (const char*)d_in[2], (const char*)d_in[3],
                                                (const char*)d_in[5], (const char*)d_in[6], (const char*)d_in[7], (const char*)d_in[8],
                                                (char*)d_out, (char*)d_ws);
}
```

```cpp
#define STAMPS 2
#define SMASK (1u<<3)
#define PROBE_K 3
#include <hip/hip_runtime.h>
#include <hip/hip_bf16.h>
#include <type_traits>
#pragma clang diagnostic ignored "-Wint-to-pointer-cast"

typedef unsigned u32;
typedef unsigned long long u64;
typedef u32 u32x4 __attribute__((ext_vector_type(4)));
typedef u32 u32x2 __attribute__((ext_vector_type(2)));
typedef short s16x4 __attribute__((ext_vector_type(4)));
typedef __bf16 bf16x8 __attribute__((ext_vector_type(8)));
typedef __bf16 bf16x2 __attribute__((ext_vector_type(2)));
typedef float f32x16 __attribute__((ext_vector_type(16)));
typedef float f32x4 __attribute__((ext_vector_type(4)));
typedef float f32x2 __attribute__((ext_vector_type(2)));
typedef __amdgpu_buffer_rsrc_t rsrc_t;
#define DI __device__ __forceinline__
#define LAS __attribute__((address_space(3)))
#define SB0() __builtin_amdgcn_sched_barrier(0)
#define PIN1(a) asm volatile("" : "+v"(a))
#define PIN2(a, b) asm volatile("" : "+v"(a), "+v"(b))

constexpr u32 WS_FLAG = 4096;
constexpr u32 WS_G    = 131072;
constexpr u32 WS_KVR  = 65536;
constexpr u32 WS_KVP  = 262144;
constexpr u32 WS_O    = WS_KVP + 256 * 4096;
constexpr u32 WS_P    = WS_O + 64 * 32 * 1024;
constexpr u32 WS_H    = WS_P + 256 * 16384;
constexpr u32 WS_WA   = WS_H + 64 * 112 * 1024;
constexpr u32 WS_END  = WS_WA + 16 * 3 * 28672;
constexpr u32 WS_STAMP = 32u << 20;

constexpr int UNIT = 8192, RING = 18;
constexpr u32 SCR = 0, RBASE = 2 * UNIT;
constexpr int LDS_BYTES = 163840;
constexpr u32 OOB = 0x80000000u;
constexpr u32 CODE_BYTES = 40960;
#define ABL 0
#define ORD 1
#define A4 9
#define SWP 1
#define DELAYS 99
#define NOGELU 0
#define NOFM 0
#define I7 33
#define I8 36
#define I9 38
#define I10 41
#define I11 44
#define I12 47
#define I18 62
#define NOMM 0
#define RJ 1
#define RT 2
#define RX 1
#define RK 0
#define XB 0
#define PH 7
#define OL 1
#define OUTAUX 0
#define NB0 10
#define C2 196
#define P2 186
#define NPRE 4
#define NB1 2
#define YR1 199
#define O1 204
#define PROV 9
#define POLL0 5
#define POLL1 17
#define GATE 11
#define PFS 0
#define TOUCH1 0
#define TOUCH2 0
#define XP 24
#define XQ 14
#define H1Q 6
#define H1R 10
#define PSLEEP 4
#define OPOS 184
#define YP 10
#define CHK1 19
#define FLAG0 2
#define I19 65
#define I20 68
constexpr u32 SPIN_MAX = ABL ? 0 : 1u << 16;
constexpr float LOG2E = 1.4426950408889634f;

#if defined(__HIP_DEVICE_COMPILE__)
#pragma clang attribute push(__attribute__((target("no-packed-fp32-ops"))), apply_to = function)
#endif
constexpr u32 spos(int k) { return RBASE + k * UNIT; }
constexpr int w2c_(int i) { constexpr int t[18] = {16, 17, 0, 1, 2, 3, 4, 5, 6, 7, 8, 9, 10, 11, 12, 13, 14, 15}; return t[i]; }
constexpr int w2s_(int c) { constexpr int t[18] = {2, 3, 4, 5, 6, 10, 11, 12, 13, 15, 14, 16, 7, 8, -2, -1, 0, 1}; return t[c]; }
constexpr int uslot(int n) {
  constexpr int t[26] = {-1, 10, 11, 12, 13, 14, 15, 16, 17, 0, 1, 2, 3, 4, 5, 6, 10, 11, 12, 13, 14, 16, 7, 8, 9, 17};
  return n < 28 ? n % RING : n < 54 ? t[n - 28] : w2s_(w2c_(n - 54));
}
constexpr u32 upos(int n) { return spos(uslot(n)); }
constexpr u32 VBUF = RBASE + 15 * UNIT;
constexpr int imin(int a, int b) { return a < b ? a : b; }

struct Ctx {
  const char *gw, *wd, *wo;
  char *out, *ws;
  int t, kh, jj, hd, j, w, tg, r;
  u32 lane, l16, m0w;
  u32 vx, vx2, vw, vq, vkv, v2, v2b;
  rsrc_t sx, sg, su, sq, sp, sd, so, sw;
  u64 tag;
  u32 vst, vsc;
  u32 rot[7];
  u32 oh, oo, op, okvp;
  u32 oflag, og;
  u32 vrp, vflag0;
  u32 vflag, awp, vone;
};

DI rsrc_t mk(const void* p, u32 bytes) { return __builtin_amdgcn_make_buffer_rsrc((void*)p, 0, bytes, 0x00020000); }
template <u32 L> DI void set_m0(const Ctx& c) { if (!(ABL & 1)) asm volatile("s_add_u32 m0, %0, %1" :: "s"(c.m0w), "i"(L) : "memory", "scc"); }
DI void dma_dev(u32 voff, rsrc_t rs, u32 soff) {
  if (!(ABL & 1)) asm volatile("buffer_load_dwordx4 %0, %1, %2 offen sc1 lds" :: "v"(voff), "s"(rs), "s"(soff) : "memory");
}
template <u32 OFF> DI void dma(u32 voff, rsrc_t rs, u32 soff) {
  if (!(ABL & 1)) asm volatile("buffer_load_dwordx4 %0, %1, %2 offen offset:%3 lds" :: "v"(voff), "s"(rs), "s"(soff), "i"(OFF) : "memory");
}
template <int N> DI void wait_bar() { asm volatile("s_waitcnt vmcnt(%0) lgkmcnt(0)\n\ts_barrier" :: "n"(N) : "memory"); }
template <int N> DI void wait_bar_v() { asm volatile("s_waitcnt vmcnt(%0)\n\ts_barrier" :: "n"(N) : "memory"); }
DI void bar_l() { asm volatile("s_waitcnt lgkmcnt(0)\n\ts_barrier" ::: "memory"); }

constexpr u32 ioff_(int n) { return n >= 36 && n < 54 && !(n < 47 && n % 3 == 0) ? 128 : 0; }
template <int N> DI void unit_m0(const Ctx& c) { set_m0<upos(N) - ioff_(N)>(c); }
template <int R, int N> DI void unit_go(const Ctx& c) {
  if constexpr (N < 21) {
    if constexpr (N % 3 < 2) dma<0>(N % 3 ? c.vx2 : c.vx, c.sx, c.rot[N / 3] * 128);
    else dma<0>(c.vw, c.sg, c.rot[N / 3] * (64 * 3584));
  } else if constexpr (N >= 29 && N < 36) dma_dev(c.vkv, c.sw, c.rot[N - 29] * 4096);
  else if constexpr (N == 28) dma<0>(c.l16, c.sp, c.w == 5 ? 0u : c.w == 6 ? 1024u : 0x40000000u);
  else if constexpr (N < 28) dma<0>(c.vw, c.su, c.rot[N - 21] * (64 * 3584));
  else if constexpr (N < 47) {
    if constexpr (N % 3 == 0) dma_dev(c.vq, c.sw, N == 45 && R == 1 ? 0x40000000u : c.rot[(2 * (N - 36) / 3 + R) % 7] * 4096);
    else dma<128>(c.vw, c.sg, c.rot[2 * ((N - 36) / 3) + N % 3 - 1] * (64 * 3584));
  } else if constexpr (N < 54) dma<128>(c.vw, c.su, c.rot[N - 47] * (64 * 3584));
  else {
    constexpr int k = w2c_(N - 54);
    if constexpr (k < 14) dma<0>(k < 7 ? c.v2 : c.v2b, c.sd, c.rot[k % 7] * (128 * 896));
    else dma<0>((k & 1) ? c.v2b : c.v2, c.so, k < 16 ? 0u : 128u * 896u);
  }
}
template <int N, int H> DI void unit_half(const Ctx& c) {
  set_m0<upos(N) + H * 4096>(c);
  constexpr int k = w2c_(N - 54);
  const u32 so = (k < 14 ? c.rot[k % 7] * (128 * 896) : k < 16 ? 0u : 128u * 896u) + H * (32 * 896);
  asm volatile("s_nop 0");
  dma<0>((k < 14 ? k >= 7 : (k & 1)) ? c.v2b : c.v2, k < 14 ? c.sd : c.so, so);
}
template <int R, int A, int B> DI void issue_range(const Ctx& c) {
  if constexpr (A < B) { unit_m0<A>(c); asm volatile("s_nop 0"); unit_go<R, A>(c); issue_range<R, A + 1, B>(c); }
}

struct Seg { u32 b[3]; };
DI Seg mkseg(u32 a) { Seg s; s.b[0] = a; s.b[1] = a + 65536; s.b[2] = a + 131072; asm volatile("" : "+v"(s.b[0]), "+v"(s.b[1]), "+v"(s.b[2])); return s; }
DI u32 sa(const Seg& s, u32 off) { return s.b[off >> 16] + (off & 0xffffu); }
DI f32x16 mfma(bf16x8 a, bf16x8 b, f32x16 c) { if (NOMM) { asm volatile("" : "+v"(c) : "v"(a), "v"(b)); return c; } return __builtin_amdgcn_mfma_f32_32x32x16_bf16(a, b, c, 0, 0, 0); }
DI bf16x8 as_frag(u32x4 v) { return __builtin_bit_cast(bf16x8, v); }
DI bf16x8 tr_frag(u32 a0, u32 a1) {
  if (NOMM > 1) { bf16x8 z; asm volatile("" : "=v"(z)); return z; }
  s16x4 lo = __builtin_amdgcn_ds_read_tr16_b64_v4i16((LAS s16x4*)a0);
  s16x4 hi = __builtin_amdgcn_ds_read_tr16_b64_v4i16((LAS s16x4*)a1);
  return __builtin_bit_cast(bf16x8, __builtin_shufflevector(lo, hi, 0, 1, 2, 3, 4, 5, 6, 7));
}
DI u32x4 lds_rd128(u32 a) { return *(LAS u32x4*)a; }
DI void lds_wr128(u32 a, u32x4 v) { *(LAS u32x4*)a = v; }
DI u32 f2u(float f) { return __builtin_bit_cast(u32, f); }
DI float u2f(u32 u) { return __builtin_bit_cast(float, u); }
DI u32 pk(float a, float b) { f32x2 t = {a, b}; return __builtin_bit_cast(u32, __builtin_convertvector(t, bf16x2)); }
template <int S, class V> DI u32x4 pack8(const V& v) {
  u32x4 r = {pk(v[8 * S], v[8 * S + 1]), pk(v[8 * S + 2], v[8 * S + 3]), pk(v[8 * S + 4], v[8 * S + 5]), pk(v[8 * S + 6], v[8 * S + 7])};
  return r;
}
DI float blo(u32 d) { return u2f(d << 16); }
DI float bhi(u32 d) { return u2f(d & 0xffff0000u); }
DI u32 umax(u32 a, u32 b) { return a > b ? a : b; }
DI bool any_(bool p) { return __builtin_amdgcn_ballot_w64(p) != 0; }
DI u32x4 sanit(u32x4 v) { return __builtin_elementwise_min(v, (u32x4){0xfffffffeu, 0xfffffffeu, 0xfffffffeu, 0xfffffffeu}); }
DI void st_dev(const Ctx& c, u32x4 v, u32 voff, u32 soff) {
  if (ABL & 8) { asm volatile("" :: "v"(v)); return; }
  __builtin_amdgcn_raw_buffer_store_b128(v, c.sw, voff, soff, 16);
  asm volatile("s_nop 1" :: "v"(v) : "memory");
}
DI u32x4 ld_dev(const Ctx& c, u32 voff, u32 soff) {
  if (ABL & 16) { u32x4 z = {voff, soff, voff, soff}; asm volatile("" : "+v"(z)); return z; }
  return __builtin_amdgcn_raw_buffer_load_b128(c.sw, voff, soff, 16);
}
DI f32x2 xhalf(float v) { auto p = __builtin_amdgcn_permlane32_swap(f2u(v), f2u(v), false, false); f32x2 r = {u2f(p[0]), u2f(p[1])}; return r; }

DI void stamp(Ctx& c, int i) {
  SB0();
  u64 tm, tc; asm volatile("s_memrealtime %0\n\ts_memtime %1\n\ts_waitcnt lgkmcnt(0)" : "=&s"(tm), "=&s"(tc) :: "memory");
  c.vst = c.lane == (u32)i ? (u32)tm : c.vst; c.vsc = c.lane == (u32)i ? (u32)tc : c.vsc; PIN2(c.vst, c.vsc);
  SB0();
}
DI void stamp_end(Ctx& c) {
  u32x2 v = {c.vst, c.vsc};
  __builtin_amdgcn_raw_buffer_store_b64(v, mk(c.ws + WS_STAMP, 1 << 20), c.lane < 32 ? c.lane * 8 : OOB, (__builtin_amdgcn_workgroup_id_x() * 8 + c.w) * 256, 0);
}
#define STAMP(i) do { if ((STAMPS == 2 && ((SMASK >> (i)) & 1)) || (i) == 0 || (i) == 31) stamp(c, i); } while (0)
#define STAMP_END() stamp_end(c)

struct P1 {
  Seg aw, aq; u32 ax, ka, kb, kc;
  bf16x8 xf[28], f[8], af[8], vf[8];
  f32x16 g1, u1, a0, a1, g2, u2, fp, k0, c[2];
  Seg s2[2]; u32 ob[2];
  u32x4 h2[14], of[8];
  u32x4 wp[2], ph[4], h1[14], kv[4];
  u32 wr[16];
  float y[16], e[14], sum, mxl, mnl;
  u32x2 poll;
  float gq, om;
  u32 hbase;
};

DI void wphi_rd(const Ctx& c, P1& p) {
#pragma unroll
  for (int i = 0; i < 16; ++i) p.wr[i] = *(LAS unsigned short*)(c.awp + 56 * (16 * (i >> 3) + 8 * ((i >> 2) & 1) + (i & 3)));
}
DI void wphi_pack(P1& p) {
#pragma unroll
  for (int i = 0; i < 8; ++i) p.wp[i >> 2][i & 3] = p.wr[2 * i] | (p.wr[2 * i + 1] << 16);
  PIN2(p.wp[0], p.wp[1]);
}
template <int K> DI void fm_slice(P1& p) {
  if constexpr (NOFM) { if constexpr (K == 0) { p.ph[0] = p.ph[2] = pack8<0>(p.a0); p.ph[1] = p.ph[3] = pack8<1>(p.a0); PIN2(p.ph[0], p.ph[1]); PIN2(p.ph[2], p.ph[3]); } }
  else if constexpr (K == 0) { p.fp = mfma(as_frag(p.wp[0]), as_frag(pack8<0>(p.a0)), (f32x16){}); }
  else if constexpr (K == 1) { p.fp = mfma(as_frag(p.wp[1]), as_frag(pack8<1>(p.a0)), p.fp); }
  else if constexpr (K == 2) { }
  else if constexpr (K == 3) {
    PIN1(p.fp);
    float mx = p.fp[0];
#pragma unroll
    for (int i = 1; i < 14; ++i) mx = __builtin_fmaxf(mx, p.fp[i]);
    p.mxl = mx; PIN1(p.mxl);
  } else if constexpr (K == 4) {
    float mn = p.fp[0];
#pragma unroll
    for (int i = 1; i < 14; ++i) mn = __builtin_fminf(mn, p.fp[i]);
    p.mnl = mn; PIN1(p.mnl);
  } else if constexpr (K == 5) {
    f32x2 t = xhalf(p.mxl); p.mxl = __builtin_fmaxf(t[0], t[1]) * LOG2E;
    t = xhalf(p.mnl); p.mnl = __builtin_fminf(t[0], t[1]) * LOG2E;
    PIN2(p.mxl, p.mnl);
  } else {
    constexpr int NEG = K >= 15, k = K - 6 - 9 * NEG;
    if constexpr (k < 5) {
#pragma unroll
      for (int i = 3 * k; i < imin(14, 3 * k + 3); ++i) {
        p.e[i] = __builtin_amdgcn_exp2f(NEG ? __builtin_fmaf(p.fp[i], -LOG2E, p.mnl) : __builtin_fmaf(p.fp[i], LOG2E, -p.mxl));
        PIN1(p.e[i]);
      }
      if constexpr (k == 1) p.sum = p.e[0] + p.e[1] + p.e[2];
      if constexpr (k > 1) p.sum += p.e[3 * k - 3] + p.e[3 * k - 2] + p.e[3 * k - 1];
      if constexpr (k > 0) PIN1(p.sum);
    } else if constexpr (k == 5) {
      f32x2 t = xhalf(p.sum + p.e[12] + p.e[13]); p.sum = __builtin_amdgcn_rcpf(t[0] + t[1]); PIN1(p.sum);
    } else if constexpr (k < 8) {
#pragma unroll
      for (int i = 7 * (k - 6); i < 7 * (k - 5); ++i) { p.e[i] *= p.sum; PIN1(p.e[i]); }
    } else {
      u32x4 o1 = {pk(p.e[8], p.e[9]), pk(p.e[10], p.e[11]), pk(p.e[12], p.e[13]), 0u};
      p.ph[2 * NEG] = pack8<0>(p.e); p.ph[2 * NEG + 1] = o1; PIN2(p.ph[2 * NEG], p.ph[2 * NEG + 1]);
    }
  }
}
template <int A, int B> DI void fm_slices(P1& p) { if constexpr (A < B) { fm_slice<A>(p); fm_slices<A + 1, B>(p); } }
template <u32 VB, int S> DI void v_handoff(const Ctx& c, const f32x16& av) {
  u32x4 vf = pack8<S>(av);
  const bool one = (c.lane & 31) == ((c.hd & 1) ? 0u : 28u);
#pragma unroll
  for (int d = 0; d < 4; ++d) vf[d] = one ? 0x3f803f80u : vf[d];
  lds_wr128(VB + c.tg * 2048 + c.l16 + S * 1024, vf);
}
template <int T> DI void phi_image(const Ctx& c, P1& p) {
  const u32 tok = c.lane & 31, h = c.lane >> 5;
  const u32 ft = (((tok >> 1) & 1) << 3) | (tok & 1) | (((tok >> 2) & 3) << 1);
  const u32 wa = SCR + c.tg * 4096 + tok * 128 + ((h ^ ft) << 3);
#pragma unroll
  for (int g = 0; g < 4; ++g) {
    u32x2 v = {p.ph[2 * T + (g >> 1)][2 * (g & 1)], p.ph[2 * T + (g >> 1)][2 * (g & 1) + 1]};
    *(LAS u32x2*)(wa ^ (u32)((8 * T + 2 * g) << 3)) = v;
  }
}
template <u32 VB, int K> DI void kv_stage(const Ctx& c, P1& p) {
  if constexpr (K >= 2 && K <= 5) {
    p.k0 = mfma(p.af[2 * K - 4], p.vf[2 * K - 4], K == 2 ? (f32x16){} : p.k0);
    p.k0 = mfma(p.af[2 * K - 3], p.vf[2 * K - 3], p.k0);
  }
  if constexpr (K == 0) {
    const u32 l = c.lane, h = l >> 5, q = (l & 15) >> 2, pp = l & 3, blk = (l >> 4) & 1;
    p.ka = SCR + (4 * h + q) * 128 + (((4 * blk + pp) ^ (q & 1) ^ (h << 1) ^ ((q >> 1) << 3) ^ ((c.tg & 1) << 3)) << 3);
    p.kb = p.ka ^ 32u; p.kc = VB + c.l16;
    asm volatile("" : "+v"(p.ka), "+v"(p.kb), "+v"(p.kc));
  }
  if constexpr (K < 4) {
#pragma unroll
    for (int s = 0; s < 2; ++s) {
      p.af[2 * K + s] = tr_frag(p.ka + K * 4096 + s * 2048, p.kb + K * 4096 + s * 2048 + 1024);
      p.vf[2 * K + s] = as_frag(lds_rd128(p.kc + K * 2048 + s * 1024));
    }
  }
  if constexpr (K == 7) {
    const u32 vo = c.tg < 2 ? c.l16 : OOB;
    st_dev(c, sanit(pack8<0>(p.k0)), vo, c.okvp + c.tg * 1024);
    st_dev(c, sanit(pack8<1>(p.k0)), vo + 1024, c.okvp + c.tg * 1024);
  }
}
DI void add8(float (&s)[8], u32x4 v) {
#pragma unroll
  for (int d = 0; d < 4; ++d) { s[2 * d] += blo(v[d]); s[2 * d + 1] += bhi(v[d]); }
}
DI void set8(float (&s)[8], u32x4 v) {
#pragma unroll
  for (int d = 0; d < 4; ++d) { s[2 * d] = blo(v[d]); s[2 * d + 1] = bhi(v[d]); }
}
template <int A, int B, int N> DI bool has_mark(const u32x4 (&v)[N]) {
  u32 m = 0;
#pragma unroll
  for (int i = A; i < B; ++i) m = umax(umax(m, v[i][0]), umax(v[i][1], umax(v[i][2], v[i][3])));
  return any_(m == 0xffffffffu);
}
template <int A, int B, int N, class F> DI void chk(u32x4 (&v)[N], F&& reload) {
  if (__builtin_expect(SPIN_MAX && has_mark<A, B>(v), 0)) {
    u32 spin = 0;
    do { asm volatile("" ::: "memory"); reload(); } while (has_mark<A, B>(v) && ++spin < SPIN_MAX);
  }
}
template <int A, int B, int N> DI void ld_lin(const Ctx& c, u32x4 (&v)[N], u32 soff, u32 stride) {
#pragma unroll
  for (int i = A; i < B; ++i) v[i] = ld_dev(c, c.l16, soff + i * stride);
}
template <int A, int B, int N> DI void ld_1k(const Ctx& c, u32x4 (&v)[N], u32 soff) {
#pragma unroll
  for (int i = A; i < B; ++i) v[i] = ld_dev(c, c.l16 + (i & 3) * 1024, soff + (i >> 2) * 4096);
}
template <int A, int B> DI void ld_h(const Ctx& c, u32x4 (&v)[14], u32 voff, u32 soff) {
#pragma unroll
  for (int i = A; i < B; ++i) v[i] = ld_dev(c, voff + (i & 1) * 1024, soff + c.rot[i >> 1] * 8192);
}
DI void o_publish(const Ctx& c, const u32x4 (&kv)[4], const u32x4 (&phq)[4], float g) {
  f32x16 n = {};
#pragma unroll
  for (int i = 0; i < 4; ++i) n = mfma(as_frag(kv[i]), as_frag(phq[i]), n);
  const bool odd = c.hd & 1;
  f32x2 t = xhalf(odd ? n[0] : n[12]);
  float sc = g * __builtin_amdgcn_rcpf((odd ? t[0] : t[1]) + 1e-6f);
#pragma unroll
  for (int i = 0; i < 16; ++i) n[i] *= sc;
  u32x4 f0 = sanit(pack8<0>(n)), f1 = sanit(pack8<1>(n));
  const bool z0 = odd && c.lane < 32, z1 = !odd && c.lane >= 32;
  f0[0] = z0 ? 0 : f0[0]; f0[1] = z0 ? 0 : f0[1];
  f1[2] = z1 ? 0 : f1[2]; f1[3] = z1 ? 0 : f1[3];
  st_dev(c, f0, c.l16, c.oo);
  st_dev(c, f1, c.l16 + 1024, c.oo);
}
DI u32 flag_off(const Ctx& c, bool spare) {
  const u32 i = c.lane & 15;
  u32 red = (c.hd * 16 + 7 + 8 * (i & 1)) << 7, sp = ((c.t * 16 + 7 + 8 * c.kh) << 7) + 8;
  u32 lo = spare ? (i * 16 + c.t) << 7 : ((c.t * 16 + (i < 7 ? 8 * c.kh + i : c.hd ^ 8)) << 7) + 8;
  asm volatile("" : "+v"(red), "+v"(sp), "+v"(lo));
  return WS_FLAG + ((c.lane & 16) ? red : (spare || i < 8) ? lo : sp);
}
DI bool flag_bad(const Ctx& c, u32x2 f) { return any_(f[0] != (u32)c.tag || f[1] != (u32)(c.tag >> 32)); }
DI void flag_spin(const Ctx& c, u32x2 f, u32 voff) {
  if (__builtin_expect(SPIN_MAX && flag_bad(c, f), 0)) {
    u32 spin = 0;
    do {
      __builtin_amdgcn_s_sleep(2);
      asm volatile("" ::: "memory");
      f = __builtin_amdgcn_raw_buffer_load_b64(c.sw, voff, 0, 16);
    } while (flag_bad(c, f) && ++spin < SPIN_MAX);
  }
  asm volatile("" ::: "memory");
}
DI void flag_publish(const Ctx& c, u32 word) {
  u32x2 v = {(u32)c.tag, (u32)(c.tag >> 32)};
  __builtin_amdgcn_raw_buffer_store_b64(v, c.sw, c.vone + word * 8, c.oflag, 16);
}
DI u32x2 ld_g(const Ctx& c) { return __builtin_amdgcn_raw_buffer_load_b64(c.sw, (c.lane & 31) * 8, c.og, 16); }

template <u32 L, bool DEV> DI void touch(u32 voff, rsrc_t rs, u32 soff) {
  if (ABL & 9) return;
  if constexpr (DEV) asm volatile("s_mov_b32 m0, %0\n\ts_nop 0\n\tbuffer_load_dword %1, %2, %3 offen sc1 lds" :: "i"(L), "v"(voff), "s"(rs), "s"(soff) : "memory");
  else asm volatile("s_mov_b32 m0, %0\n\ts_nop 0\n\tbuffer_load_dword %1, %2, %3 offen lds" :: "i"(L), "v"(voff), "s"(rs), "s"(soff) : "memory");
}

constexpr int PRO = PROV;
constexpr int NS = 21;
constexpr int need_(int s) {
  return s < 0 ? 0 : s < 7 ? 3 * (s + 1) : s < 9 ? 23 + 2 * (s - 7) : s < 12 ? 31 + 2 * (s - 9) : s < 14 ? 36 : s < 18 ? 39 + 3 * (s - 14) : s < 20 ? 50 + 2 * (s - 18) : 62;
}
constexpr int iss_(int s) {
  constexpr int t[14] = {I7, I8, I9, I10, I11, I12, 49, 51, 53, 56, 59, I18, I19, I20};
  return s < 0 ? PRO : s < 7 ? imin(imin(29, 18 + need_(s - 2)), PRO + 3 * (s + 1) + (s >= A4 ? s - A4 + 1 : 0)) : t[s - 7];
}
constexpr int ustep(int n) {
  if (n == 34 || n == 28) return 16;
  if (n >= 25 && n < 28) return n == 25 ? 12 : 13;
  int s = 0; while (s < 20 && need_(s) <= n) ++s; return s;
}
constexpr bool ring_ok() {
  for (int s = -1; s < NS; ++s)
    for (int n = s < 0 ? 0 : iss_(s - 1); n < iss_(s); ++n) {
      if (uslot(n) == -2 && s < 18) return false;
      for (int m = 0; m < n; ++m) if (uslot(m) == uslot(n) && ustep(m) + 2 > s) return false;
    }
  return true;
}
static_assert(ring_ok(), "ring schedule");
constexpr int S_FLAG0 = FLAG0, S_POLL0 = POLL0, S_CHK0 = 7, S_GATE = GATE, S_CHKG = 13, S_FLAG1 = 16, S_POLL = POLL1, S_CHK = CHK1;
constexpr int oth_(int r, int s) {
  return s == S_FLAG0 ? 1 - r : s == S_POLL0 ? 1 - r : s == 10 ? (GATE == 10) : s == 11 ? 1 - r + (GATE == 11) : s == 12 ? r : s == 14 ? 2 : s == 16 ? 3 - 3 * r + TOUCH1 :
         s == S_POLL ? 1 - r : s == 18 ? (r || CHK1 == 19 ? 0 : 4) : s == 19 || s == 20 ? (r ? 0 : CHK1 == 19 ? 7 : 5) : 0;
}
constexpr int tot_(int r, int s) { return iss_(s) - iss_(s - 1) + oth_(r, s); }
constexpr int vm_(int r, int s) {
  int sp = -1; while (iss_(sp) < need_(s)) ++sp;
  int n = iss_(sp) - need_(s) + (sp < 0 ? 2 - r : 0);
  for (int k = sp + 1; k < s; ++k) n += tot_(r, k);
  if (s == S_FLAG0) { int m = 0; for (int k = 0; k < s; ++k) m += tot_(r, k); n = imin(n, m); }
  if (s == S_FLAG1) n = imin(n, tot_(r, 15));
  if (((s == S_CHK0 || s == S_CHK) && r == 0) || s == S_CHKG) {
    int m = -1; for (int k = s == S_CHK0 ? S_POLL0 : s == S_CHKG ? S_GATE : S_POLL; k < s; ++k) m += tot_(r, k);
    n = imin(n, m);
  }
  return n;
}
constexpr int P2_(int r) { return r ? 168 : 140; }
constexpr int item0_(int r, int s) { return s < 7 ? 4 * s : (!r || s < 15) ? 8 * s - 28 : s < 18 ? 84 + 16 * (s - 14) : 8 * s; }
template <int R, int J> DI void rd_item(P1& p) {
  constexpr int U2 = P2_(R) - 28;
  if constexpr (J >= P2_(R)) {
    constexpr int k = J - P2_(R);
    if constexpr (k < 56) {
      constexpr u32 o = spos(w2s_(k / 4)) + ((k / 2) & 1) * 2048;
      p.f[J % 8] = tr_frag(sa(p.s2[k & 1], o), sa(p.s2[k & 1], o + 1024));
    } else if constexpr (k < 72) {
      constexpr int i = (k - 56) / 2, hh = i >> 1;
      constexpr u32 o = (28 * hh - 4 * (hh & 1) + 16 * (i & 1)) * 128;
      p.f[J % 8] = tr_frag(p.ob[k & 1] + o, p.ob[k & 1] + o + 1024);
    }
  } else if constexpr ((J >= 44 && J < 72) || (R == 1 && J >= 84 && J < U2 && (J & 1))) {
    constexpr int k = J < 72 ? J - 44 : (J - 84) / 2, ch = k / 4;
    constexpr u32 o = (J < 72 ? upos(29 + ch) + R * 4096 : upos(36 + 3 * (ch / 2)) + (ch & 1) * 4096) + (k % 4) * 1024;
    p.f[J % 8] = tr_frag(sa(p.aq, o), sa(p.aq, o + 256));
  } else {
    constexpr int k = J < 84 ? J : J < U2 ? (J - 84) / (1 + R) : J - U2, ch = k / 4;
    constexpr u32 o = upos(J < 28 ? 3 * ch + 2 : J < 44 ? 14 + ch : J < 84 ? 7 + ch : J < U2 ? 37 + 3 * (ch / 2) + (ch & 1) : 47 + ch) + (k % 4) * 2048;
    p.f[J % 8] = tr_frag(sa(p.aw, o), sa(p.aw, o + 512));
  }
}
template <int R, int J> DI void mm_item(P1& p) {
  constexpr int U2 = P2_(R) - 28;
  if constexpr (J < 0) {}
  else if constexpr (J >= P2_(R)) {
    constexpr int k = J - P2_(R);
    p.c[k & 1] = mfma(p.f[J % 8], as_frag(k < 28 ? p.h1[(k / 2) % 14] : k < 56 ? p.h2[(k / 2) % 14] : p.of[(k / 2 + 4) % 8]), p.c[k & 1]);
  } else if constexpr (J >= 44 && J < 72) {
    if constexpr (R == 1) p.a1 = mfma(p.xf[J - 44], p.f[J % 8], p.a1); else p.a0 = mfma(p.f[J % 8], p.xf[J - 44], p.a0);
  } else {
    f32x16& acc = J < 28 ? p.g1 : J < 84 ? p.u1 : J >= U2 ? p.u2 : (R == 1 && (J & 1)) ? p.a0 : p.g2;
    acc = mfma(p.f[J % 8], p.xf[J < 84 ? J % 28 : J < U2 ? (J - 84) / (1 + R) : J - U2], acc);
  }
}
template <int R, int A, int B> DI void mm_range(P1& p) { if constexpr (A < B) { mm_item<R, A>(p); mm_range<R, A + 1, B>(p); } }

constexpr float GK1 = -2.0f * 0.7978845608028654f * LOG2E, GK2 = GK1 * 0.044715f;
template <bool SC, int OP> DI void gelu_op(float (&y)[16], const f32x16& g, float om) {
  constexpr int st = OP % 4, i = OP / 4 - st;
  if constexpr (NOGELU && i >= 0 && i < 16) { if constexpr (st == 3) { y[i] = g[i]; PIN1(y[i]); } }
  else if constexpr (i >= 0 && i < 16) {
    if constexpr (st == 0) y[i] = g[i] * __builtin_fmaf(g[i] * g[i], GK2, GK1);
    else if constexpr (st == 1) y[i] = __builtin_amdgcn_exp2f(y[i]);
    else if constexpr (st == 2) y[i] = __builtin_amdgcn_rcpf(1.0f + y[i]);
    else y[i] = SC ? (g[i] * om) * y[i] : g[i] * y[i];
    PIN1(y[i]);
  }
}
constexpr int gcost(int op) { return op / 4 - op % 4 < 0 || op / 4 - op % 4 > 15 ? 0 : op % 4 == 0 ? 3 : op % 4 == 1 ? 2 : op % 4 == 2 ? 3 : 1; }
constexpr int gcum(int k, int n) { if (k >= n) return 76; int op = 0, cst = 0; while (cst * n < k * 144) cst += gcost(op++); return op; }
template <bool SC, int A, int B> DI void gelu_ops(float (&y)[16], const f32x16& g, float om) { if constexpr (A < B) { gelu_op<SC, A>(y, g, om); gelu_ops<SC, A + 1, B>(y, g, om); } }
template <int S> DI void h_store(const Ctx& c, const float (&y)[16], const f32x16& u, int blk) {
  float h[8];
#pragma unroll
  for (int i = 0; i < 8; ++i) h[i] = y[8 * S + i] * u[8 * S + i];
  st_dev(c, pack8<0>(h), c.l16 + S * 1024, c.oh + blk * 4096);
}
template <int A, int B> DI void y_scale(P1& p) {
#pragma unroll
  for (int i = A; i < B; ++i) { p.y[i] *= p.om; PIN1(p.y[i]); }
}
DI void addr2(const Ctx& c, P1& p) {
  const u32 l = c.lane, ks = c.tg >> 1, qq = (l & 15) >> 2;
  const u32 bk = ((4 * (l >> 5) + qq) * 128 + ((qq >> 1) << 6) + 32 * ((l >> 4) & 1) + 8 * (l & 3)) ;
  p.s2[0] = mkseg(bk + ks * 4096); p.s2[1] = mkseg((bk ^ 64) + ks * 4096);
  p.ob[0] = bk + ks * (112 * 128); p.ob[1] = (bk ^ 64) + ks * (112 * 128);
  PIN2(p.ob[0], p.ob[1]);
}
template <int R, int S, int K, int J> DI void slot_work(Ctx& c, P1& p) {
  if constexpr (S < 7) p.xf[J] = as_frag(lds_rd128((p.ax + upos(3 * S)) ^ (u32)(32 * K)));
  if constexpr (R == 0) {
    if constexpr (J >= 33 && J < 75) gelu_ops<false, gcum(J - 33, 42), gcum(J - 32, 42)>(p.y, p.g1, 0.f);
    if constexpr (J == 70) wphi_rd(c, p);
    if constexpr (J == 76) wphi_pack(p);
    if constexpr (J == 78 || J == 79) y_scale<8 * (J - 78), 8 * (J - 77)>(p);
    constexpr int F0 = J < 84 ? J - 77 : J < 89 ? 7 + (3 * (J - 84) + 1) / 2 : J < 91 ? 15 : 15 + (9 * (J - 91) + 7) / 8;
    constexpr int F1 = J + 1 < 84 ? J - 76 : J + 1 < 89 ? 7 + (3 * (J - 83) + 1) / 2 : J + 1 < 91 ? 15 : 15 + (9 * (J - 90) + 7) / 8;
    if constexpr (J >= 77 && J < 99) fm_slices<F0, imin(24, F1)>(p);
    if constexpr (J == 92) phi_image<0>(c, p);
    if constexpr (J == 99) phi_image<1>(c, p);
    if constexpr (S == 16) kv_stage<VBUF, K>(c, p);
    if constexpr (CHK1 == 19 && S >= 19 && K >= 1) ld_h<7 * (S - 19) + K - 1, 7 * (S - 19) + K>(c, p.h1, c.l16, p.hbase);
    if constexpr (CHK1 == 18 && S == 18 && (K & 1)) ld_h<K / 2, K / 2 + 1>(c, p.h1, c.l16, p.hbase);
    if constexpr (CHK1 == 18 && S >= 19 && ((K & 1) || K == 4)) { constexpr int i = 4 + 5 * (S - 19) + (K < 4 ? K / 2 : (K + 1) / 2); ld_h<i, i + 1>(c, p.h1, c.l16, p.hbase); }
  } else {
    if constexpr (J >= 33 && J < 77) gelu_ops<false, gcum(J - 33, 44), gcum(J - 32, 44)>(p.y, p.g1, 0.f);
    if constexpr (J == 77 || J == 78) v_handoff<VBUF, J - 77>(c, p.a1);
    if constexpr (J == 79) wphi_rd(c, p);
    if constexpr (J == 83) wphi_pack(p);
    if constexpr (J >= 84 && J < 88) y_scale<4 * (J - 84), 4 * (J - 83)>(p);
  }
  if constexpr (J == 89 || J == 90) h_store<J - 89>(c, p.y, p.u1, 0);
  if constexpr (J >= P2_(R) - 23) gelu_ops<true, gcum(J - P2_(R) + 23, 27), gcum(J - P2_(R) + 24, 27)>(p.y, p.g2, p.om);
  if constexpr (J == P2_(R) - 26) addr2(c, p);
  if constexpr (TOUCH1 && S == S_FLAG1 && K == 6) touch<upos(45) + 4096, true>(c.lane < 16 ? c.lane * 128 : OOB, c.sw, c.oh);
  if constexpr (S == 11 + R && K == 3) st_dev(c, (u32x4){~0u, ~0u, ~0u, ~0u}, c.l16 + R * 1024, c.op);
}
template <int R, int S, int J0, int K, int N> DI void slots(Ctx& c, P1& p) {
  if constexpr (K < N) {
    constexpr int nu = iss_(S) - iss_(S - 1), u0 = iss_(S - 1);
    constexpr int ua = K == 0 ? 0 : ((2 * K + R) * nu) / (2 * N), ub = K == N - 1 ? nu : ((2 * K + 2 + R) * nu) / (2 * N);
    if constexpr (R == 1 && S >= 18 && (K == 0 || K == 4)) __builtin_amdgcn_s_setprio(K == 0);
    mm_item<R, J0 + K - 4>(p);
    if (ORD) SB0();
    if constexpr (ub > ua) unit_m0<u0 + ua>(c);
    rd_item<R, (J0 + K) ^ SWP>(p);
    if constexpr (ub > ua) { unit_go<R, u0 + ua>(c); issue_range<R, u0 + ua + 1, u0 + ub>(c); }
    if (ORD) SB0();
    slot_work<R, S, K, J0 + K>(c, p);
    SB0();
    slots<R, S, J0, K + 1, N>(c, p);
  }
}
template <int R, int S> DI void step(Ctx& c, P1& p) {
  if constexpr ((S == S_CHK || S == S_CHK0) && R == 0) {
    asm volatile("s_waitcnt vmcnt(%1)" : "+v"(p.poll) : "n"(vm_(R, S)) : "memory");
    PIN1(S == S_CHK ? p.u2 : p.g1);
    flag_spin(c, p.poll, S == S_CHK ? c.vflag : c.vflag0);
    asm volatile("s_barrier" ::: "memory");
  } else if constexpr (S == S_CHKG) {
    asm volatile("s_waitcnt vmcnt(%1)\n\ts_barrier" : "+v"(p.poll) : "n"(vm_(R, S)) : "memory");
    {
      PIN2(p.u1, R ? p.a1 : p.a0);
      if (__builtin_expect(SPIN_MAX && any_(p.poll[1] != (u32)c.tag), 0)) {
        u32 spin = 0;
        do { __builtin_amdgcn_s_sleep(2); asm volatile("" ::: "memory"); p.poll = ld_g(c); } while (any_(p.poll[1] != (u32)c.tag) && ++spin < SPIN_MAX);
      }
      p.gq = u2f(p.poll[0]); p.om = 1.0f - p.gq;
      PIN2(p.gq, p.om);
    }
  } else if constexpr ((S == 16 && R == 0) || (S == 20 && NPRE > 4)) wait_bar<vm_(R, S)>();
  else wait_bar_v<vm_(R, S)>();
  if constexpr (S < 2 || S >= 6) STAMP(S < 2 ? S + 1 : S - 3);
  if constexpr (S == DELAYS) __builtin_amdgcn_s_sleep(16);
  if constexpr (S == S_FLAG0 && R == 0) flag_publish(c, 0);
  if constexpr (S == S_FLAG1 && R == 0) flag_publish(c, 1);
  if constexpr ((S == S_POLL || S == S_POLL0) && R == 0) asm volatile("buffer_load_dwordx2 %0, %1, %2, 0 offen sc1" : "=v"(p.poll) : "v"(S == S_POLL ? c.vflag : c.vflag0), "s"(c.sw) : "memory");
  if constexpr (S == S_GATE) asm volatile("buffer_load_dwordx2 %0, %1, %2, %3 offen sc1" : "=v"(p.poll) : "v"((c.lane & 31) * 8), "s"(c.sw), "s"(c.og) : "memory");
  SB0();
  slots<R, S, item0_(R, S), 0, item0_(R, S + 1) - item0_(R, S)>(c, p);
}
template <int R, int S, int E> DI void steps(Ctx& c, P1& p) { if constexpr (S < E) { step<R, S>(c, p); steps<R, S + 1, E>(c, p); } }
template <int R, int A, int B, int RE, class F> DI void run(P1& p, F&& extra) {
  if constexpr (A < B) {
    mm_item<R, A - 4>(p);
    if (ORD) SB0();
    if constexpr (A < RE) rd_item<R, A ^ 1>(p);
    if (ORD) SB0();
    extra(std::integral_constant<int, A>{});
    SB0();
    run<R, A + 1, B, RE>(p, extra);
  } else { PIN2(p.c[0], p.c[1]); }
}
template <int K, class F> DI void fmq(P1& p, F&& extra) {
  if constexpr (K < 24) { fm_slice<K>(p); extra(std::integral_constant<int, K>{}); SB0(); fmq<K + 1>(p, extra); }
}
template <int R, int J> DI void rd4(P1& p) { rd_item<R, J + 1>(p); rd_item<R, J>(p); rd_item<R, J + 3>(p); rd_item<R, J + 2>(p); }

template <int R> DI void ffn_wg(Ctx& c, P1& p) {
  const u32 l = c.lane;
  const int tg2 = c.tg & 1, ks = c.tg >> 1;
  const int TG = 2 * (R == 1 ? c.kh : 1 - c.kh) + tg2;
  p.hbase = WS_H + (u32)(((c.t * 4 + TG) * 112 + 56 * c.kh + 2 * ks) * 1024);
  if (XB & 4) p.hbase = 0x40000000u;
  asm volatile("" : "+s"(p.hbase));
  PIN1(c.awp);
  SB0();
  issue_range<R, 3, PRO>(c);
  asm volatile("s_waitcnt vmcnt(%0)" :: "n"(PRO - 3) : "memory");
  st_dev(c, lds_rd128(spos(17) + c.m0w + c.l16), c.vrp, 0);
  if constexpr (R == 0) st_dev(c, (u32x4){~0u, ~0u, ~0u, ~0u}, c.l16, c.okvp);
  p.g1 = p.u1 = p.a0 = p.a1 = p.g2 = p.u2 = p.c[0] = p.c[1] = (f32x16){};
  steps<R, 0, NS>(c, p);
  STAMP(18);
  auto none = [](auto) {};
  constexpr int B = P2_(R);
  const u32 h = l >> 5;
  const u32 obase = ((XB & 8) ? 0x40000000u : WS_O) + (u32)(((c.t * 4 + TG) * 16 + 8 * c.kh + 4 * ks) * 2048);
  const u32 l16h = c.l16 + 4096, vflag2 = c.vflag + ((l & 31) < 7 ? 8 : 0);
  auto epi2 = [&](auto J) {
    if constexpr (J.value < B + 4) gelu_ops<true, gcum(J.value - B + 23, 27), gcum(J.value - B + 24, 27)>(p.y, p.g2, p.om);
    if constexpr (J.value == B + 5 || J.value == B + 6) h_store<J.value - B - 5>(c, p.y, p.u2, 1);
  };
  if constexpr (R == 0) {
    run<R, 140, 140 + XP, 999>(p, [&](auto J) {
      epi2(J);
      if constexpr (NPRE && J.value == 139 + XP - NPRE) { PIN2(p.h1[10], p.h1[11]); PIN2(p.h1[12], p.h1[13]); }
      if constexpr (J.value >= 140 + XP - NPRE) unit_half<71 - (J.value - 140 - XP + NPRE) / 2, J.value & 1>(c);
    });
    STAMP(19);
    wait_bar_v<NPRE>();
    STAMP(20);
    flag_publish(c, 2);
    run<R, 140 + XP, 172, 999>(p, [&](auto J) { if constexpr (J.value - 140 - XP + NPRE < 8) unit_half<71 - (J.value - 140 - XP + NPRE) / 2, J.value & 1>(c); });
    STAMP(21);
    __builtin_amdgcn_s_sleep(PSLEEP);
    flag_spin(c, __builtin_amdgcn_raw_buffer_load_b64(c.sw, vflag2, 0, 16), vflag2);
    STAMP(22);
    ld_h<0, NB0>(c, p.h2, l16h, p.hbase);
    run<R, 172, 200, 999>(p, [&](auto J) {
      if constexpr (J.value - 172 + NB0 < 14) ld_h<J.value - 172 + NB0, J.value - 171 + NB0>(c, p.h2, l16h, p.hbase);
      if constexpr (J.value == 187) { asm volatile("s_barrier" ::: "memory"); STAMP(23); }
      if constexpr (J.value >= OPOS && J.value < OPOS + 8) ld_1k<J.value - OPOS, J.value - OPOS + 1>(c, p.of, obase);
    });
  } else {
    __builtin_amdgcn_s_setprio(1);
    run<R, 168, 172, 999>(p, epi2);
    fm_slices<0, 2>(p);
    SB0();
    h_store<0>(c, p.y, p.u2, 1); h_store<1>(c, p.y, p.u2, 1);
    SB0();
    STAMP(19);
    fmq<3>(p, [&](auto K) {
      if constexpr (K.value == XQ) {
        wait_bar_v<0>();

        ld_1k<0, 4>(c, p.kv, WS_KVR + (u32)c.hd * 4096);
      }
      if constexpr (K.value > XQ && K.value <= XQ + H1Q) ld_h<K.value - XQ - 1, K.value - XQ>(c, p.h1, c.l16, p.hbase);
    });
    STAMP(21);
    chk<0, 4>(p.kv, [&] { ld_1k<0, 4>(c, p.kv, WS_KVR + (u32)c.hd * 4096); });
    o_publish(c, p.kv, p.ph, p.gq);
    __builtin_amdgcn_s_setprio(0);
    STAMP(22);
    u32x2 f2;
    run<R, 172, C2, 999>(p, [&](auto J) {
      if constexpr (J.value - 172 + H1Q < 14) ld_h<J.value - 172 + H1Q, J.value - 171 + H1Q>(c, p.h1, c.l16, p.hbase);
      if constexpr (J.value == P2) f2 = __builtin_amdgcn_raw_buffer_load_b64(c.sw, vflag2, 0, 16);
    });
    flag_spin(c, f2, vflag2);
    STAMP(24);
        ld_h<0, NB1>(c, p.h2, l16h, p.hbase);
    run<R, C2, 228, 999>(p, [&](auto J) {
      if constexpr (J.value - C2 + NB1 < 14) ld_h<J.value - C2 + NB1, J.value - C2 + NB1 + 1>(c, p.h2, l16h, p.hbase);
      if constexpr (J.value == YR1) { asm volatile("s_barrier" ::: "memory"); STAMP(23); }
      if constexpr (J.value >= O1 && J.value < O1 + 8) ld_1k<J.value - O1, J.value - O1 + 1>(c, p.of, obase);
    });
  }
  STAMP(27);
  chk<0, 8>(p.of, [&] { ld_1k<0, 8>(c, p.of, obase); });
  STAMP(28);
  run<R, B + 60, B + 76, 999>(p, none);
  STAMP(29);
  u32x4 pp[2];
  auto ld_p = [&] { pp[0] = ld_dev(c, c.l16, c.op ^ (8 * 16384)); pp[1] = ld_dev(c, c.l16 + 1024, c.op ^ (8 * 16384)); };
  if constexpr (R == 1) ld_p();
  const u32 xw = R ? (c.tg ? spos(8) + c.tg * UNIT : spos(6)) : spos(2) + c.tg * UNIT;
  const u32 xo = R ? (c.tg != 2 ? spos(8) + (c.tg ^ 2) * UNIT : spos(6)) : spos(2) + (c.tg ^ 2) * UNIT;
  {
    const u32 wa = xw + (l & 31) * 256 + ((h ^ (l & 7)) << 4);
#pragma unroll
    for (int g = 0; g < 8; ++g) {
      const f32x16& v = p.c[g >> 2];
      *(LAS f32x4*)((wa ^ (u32)((g & 3) << 5)) + (g >> 2) * 128) = (f32x4){v[4 * (g & 3)], v[4 * (g & 3) + 1], v[4 * (g & 3) + 2], v[4 * (g & 3) + 3]};
    }
  }
  {
    volatile LAS u32* fl = (volatile LAS u32*)(spos(1) + 4096);
    asm volatile("s_waitcnt lgkmcnt(0)" ::: "memory");
    fl[c.w] = 1u;
    u32 spin = 0;
    while (__builtin_amdgcn_readfirstlane(fl[c.w ^ 2]) == 0 && ++spin < (1u << 22)) {}
    asm volatile("" ::: "memory");
  }
  float r16[16];
  {
    const u32 ra = (ks * 16 + (l >> 3)) * 256 + (l & 4) * 32 + (((2 * (l & 3)) ^ (l >> 3)) << 4);
#pragma unroll
    for (int q = 0; q < 4; ++q) {
      const u32 o = (ra ^ (u32)((q & 1) << 4)) + (q >> 1) * 2048;
      const f32x4 a = *(LAS f32x4*)(xw + o), b = *(LAS f32x4*)(xo + o);
#pragma unroll
      for (int i = 0; i < 4; ++i) r16[4 * q + i] = a[i] + b[i];
    }
  }
  if constexpr (R == 0) {
    st_dev(c, sanit(pack8<0>(r16)), c.l16, c.op);
    st_dev(c, sanit(pack8<1>(r16)), c.l16 + 1024, c.op);
    STAMP(30);
  } else {
#pragma unroll
    for (int i = 0; i < 16; ++i) PIN1(r16[i]);
    chk<0, 2>(pp, ld_p);
    STAMP(30);
#pragma unroll
    for (int d = 0; d < 4; ++d) {
      r16[2 * d] += blo(pp[0][d]); r16[2 * d + 1] += bhi(pp[0][d]);
      r16[8 + 2 * d] += blo(pp[1][d]); r16[9 + 2 * d] += bhi(pp[1][d]);
    }
    const u32 vo = (l >> 3) * 896 + (l & 7) * 16, oo = (c.t * 128 + c.kh * 64 + tg2 * 32 + ks * 16) * 896 + c.jj * 128;
#pragma unroll
    for (int s = 0; s < 2; ++s) {
      u32x4 o = s ? pack8<1>(r16) : pack8<0>(r16);
      __builtin_amdgcn_raw_buffer_store_b128(o, mk(c.out, 2048 * 896), vo, oo + s * (8 * 896), OUTAUX);
      asm volatile("s_nop 1" :: "v"(o) : "memory");
    }
    if (__builtin_amdgcn_workgroup_id_x() == 0 && c.w == 4) {
      u32x2 v = {(u32)c.tag, (u32)(c.tag >> 32)};
      __builtin_amdgcn_raw_buffer_store_b64(v, c.sw, l == 0 ? 0u : OOB, 0, 16);
    }
    STAMP(31);
  }
}

template <int R, int A0, int A, int B, int ST> DI void pipe(P1& p) {
  if constexpr (A - 3 * ST < B) {
    if constexpr (A - 3 * ST >= A0) mm_item<R, A - 3 * ST>(p);
    if constexpr (A < B) rd_item<R, A>(p);
    SB0();
    pipe<R, A0, A + ST, B, ST>(p);
  }
}
constexpr u32 sxpos(int ch) { return spos(2 * ch - 2); }
template <int N, int E> DI void spare_x(const Ctx& c) {
  if constexpr (N < E) { set_m0<sxpos(N >> 1) + (N & 1) * UNIT>(c); asm volatile("s_nop 0"); dma<0>((N & 1) ? c.vx2 : c.vx, c.sx, c.rot[N >> 1] * 128); spare_x<N + 1, E>(c); }
}
template <int R> DI void spare_wg(Ctx& c, P1& p) {
  const u32 l = c.lane;
  set_m0<spos(12)>(c); asm volatile("s_nop 0"); dma<0>(c.l16, mk(c.gw, 896), c.w == 0 ? 0u : 0x40000000u);
  const u32 okvr = WS_KVR + (u32)(c.t * 4096 + (2 * c.kh + (c.w & 1)) * 1024);
  const u32x4 ones = {~0u, ~0u, ~0u, ~0u};
  st_dev(c, ones, R == 0 || c.tg < 2 ? c.l16 : OOB, R == 0 ? c.okvp : okvr);
  spare_x<2, 4>(c);
  asm volatile("s_waitcnt vmcnt(4)" ::: "memory");
  st_dev(c, lds_rd128(spos(17) + c.m0w + c.l16), c.vrp, 0);
  spare_x<4, 14>(c);
  wait_bar<0>();
  if constexpr (R == 0) flag_publish(c, 0);
  if (!OL)
  {
    const u32 mo = WS_O + ((c.t * 4 + c.tg) * 16 + 8 * c.kh + 4 * R) * 2048;
#pragma unroll
    for (int i = 0; i < 8; ++i) st_dev(c, ones, c.l16 + (i & 3) * 1024, mo + (i >> 2) * 4096);
  }
#pragma unroll
  for (int s = 0; s < 28; ++s) p.xf[s] = as_frag(lds_rd128((p.ax ^ (u32)(32 * (s & 3))) + sxpos(s >> 2)));
  STAMP(1);
  f32x16 ga = {};
#pragma unroll
  for (int s = 14 * R; s < 14 * R + 14; ++s) ga = mfma(as_frag(lds_rd128(spos(12) + c.rot[s >> 2] * 128 + (s & 3) * 32 + (l >> 5) * 16)), p.xf[s], ga);
  PIN1(ga);
  float g1 = 0.f;
  if constexpr (R == 0) *(LAS float*)(spos(17) + c.tg * 256 + l * 4) = ga[0];
  u32x2 f0 = __builtin_amdgcn_raw_buffer_load_b64(c.sw, c.vflag0, 0, 16);
  bar_l();
  if constexpr (R == 1) {
    g1 = __builtin_amdgcn_rcpf(1.0f + __builtin_amdgcn_exp2f(-LOG2E * (ga[0] + *(LAS float*)(spos(17) + c.tg * 256 + l * 4))));
    __builtin_amdgcn_raw_buffer_store_b64((u32x2){f2u(g1), (u32)c.tag}, c.sw, l < 32 ? l * 8 : OOB, c.og, 16);
  }
  flag_spin(c, f0, c.vflag0);
  issue_range<R, 29, 36>(c); issue_range<R, 28, 29>(c);
  issue_range<R, 39, 40>(c); issue_range<R, 42, 43>(c);
  p.a0 = p.a1 = (f32x16){};
  wait_bar<6>();
  STAMP(2);
  issue_range<R, 36, 37>(c);
  if (OL)
  {
    const u32 mo = WS_O + ((c.t * 4 + c.tg) * 16 + 8 * c.kh + 4 * R) * 2048;
#pragma unroll
    for (int i = 0; i < 8; ++i) st_dev(c, ones, c.l16 + (i & 3) * 1024, mo + (i >> 2) * 4096);
  }
  pipe<R, 44, 44, 60, 1>(p);
  wait_bar_v<0>();
  if constexpr (R == 0) flag_publish(c, 1);
  issue_range<R, 45, 46>(c);
  wphi_rd(c, p);
  pipe<R, 60, 60, 72, 1>(p);
  wphi_pack(p);
  wait_bar_v<0>();
  if constexpr (R == 0) { fm_slices<0, 24>(p); phi_image<0>(c, p); phi_image<1>(c, p); }
  else { v_handoff<spos(1), 0>(c, p.a1); v_handoff<spos(1), 1>(c, p.a1); pipe<R, 85, 85, 140, 2>(p); PIN1(p.a0); }
  bar_l();
  if constexpr (R == 0) {
    if (c.w >= 2) {
      if (PFS) {
        const u32 e = (c.t & 3) * 2 + (c.w & 1);
#pragma unroll 1
        for (u32 i = 0; i < 13; ++i) { touch<spos(13), false>(l * 128 + i * 8192, mk(c.wd + c.kh * 802816 + e * 100352, 100352), 0); __builtin_amdgcn_s_sleep(PFS); }
#pragma unroll 1
        for (u32 i = 0; i < 7; ++i) { touch<spos(13), false>(l * 128 + i * 8192, mk(c.wo + e * 50176, 50176), 0); __builtin_amdgcn_s_sleep(PFS); }
      }
      return;
    }
    kv_stage<spos(1), 0>(c, p); kv_stage<spos(1), 1>(c, p); kv_stage<spos(1), 2>(c, p); kv_stage<spos(1), 3>(c, p);
    kv_stage<spos(1), 4>(c, p); kv_stage<spos(1), 5>(c, p); kv_stage<spos(1), 7>(c, p);
    STAMP(3);
    flag_spin(c, __builtin_amdgcn_raw_buffer_load_b64(c.sw, c.vflag, 0, 16), c.vflag);
    STAMP(4);
    u32x4 v[16];
    const u32 ro = WS_KVP + (u32)(c.t * 65536 + (2 * c.kh + c.w) * 1024);
    ld_lin<0, 16>(c, v, ro, 4096);
    chk<0, 16>(v, [&] { ld_lin<0, 16>(c, v, ro, 4096); });
    STAMP(5);
    float s[8], s2[8];
    set8(s, v[0]); set8(s2, v[1]);
#pragma unroll
    for (int i = 2; i < 16; i += 2) { add8(s, v[i]); add8(s2, v[i + 1]); }
#pragma unroll
    for (int i = 0; i < 8; ++i) s[i] += s2[i];
    st_dev(c, sanit(pack8<0>(s)), c.l16, okvr);
    STAMP(6);
  } else {
    fm_slices<0, 24>(p);
    flag_spin(c, __builtin_amdgcn_raw_buffer_load_b64(c.sw, c.vflag, 0, 16), c.vflag);
    u32x4 v[4];
    const u32 kvo = WS_KVR + (u32)c.hd * 4096;
    ld_1k<0, 4>(c, v, kvo);
    chk<0, 4>(v, [&] { __builtin_amdgcn_s_sleep(8); ld_1k<0, 4>(c, v, kvo); });
    o_publish(c, v, p.ph, g1);
    STAMP(7);
  }
}

extern "C" __global__ __launch_bounds__(512, 2) void fused_kernel(const char* x, const char* wqkv, const char* wphi, const char* wo, const char* wg,
                                                                   const char* wu, const char* wd, const char* gw, char* out, char* ws) {
  Ctx c;
  const int bid = __builtin_amdgcn_workgroup_id_x(), xcd = bid & 7, idx = bid >> 3;
  const u32 tid = __builtin_amdgcn_workitem_id_x();
  c.gw = gw; c.wd = wd; c.wo = wo; c.out = out; c.ws = ws;
  c.t = 4 * (xcd >> 1) + (idx & 3); c.kh = xcd & 1; c.jj = idx >> 2; c.hd = 8 * c.kh + c.jj; c.j = 7 * c.kh + c.jj;
  c.w = __builtin_amdgcn_readfirstlane(tid >> 6); c.tg = c.w & 3; c.r = c.w >> 2;
  c.lane = tid & 63; c.l16 = c.lane * 16; c.m0w = c.w * 1024;
  const u32 l = c.lane, w = c.w;
  c.vst = c.vsc = 0;
  STAMP(0);
  const u64 rt = 0x2106543210654321ull, r0 = RJ * c.jj + RT * (idx & 3) + RX * (xcd >> 1) + RK * c.kh;
  const u32 rs = (u32)(rt >> (4 * (r0 > 9 ? r0 - 7 : r0)));
#pragma unroll
  for (int i = 0; i < 7; ++i) c.rot[i] = (rs >> (4 * i)) & 7;
  const u32 row8 = 8 * w + (l >> 3), sw2 = ((l & 7) ^ (((l >> 4) & 1) << 2)) << 4;
  c.vx = row8 * 896 + (((l & 7) ^ ((4 * w + (l >> 4)) & 7)) << 4); c.vx2 = c.vx + 64 * 896;
  c.vw = row8 * 3584 + sw2;
  asm volatile("" : "+v"(c.vx), "+v"(c.vx2), "+v"(c.vw));
  asm volatile("" : "+s"(c.rot[0]), "+s"(c.m0w));
  SB0();
  const u64 epoch = *(const __attribute__((address_space(4))) u64*)ws;
  c.sx = mk(x + c.t * 114688, 114688);
  c.sg = mk(wg + c.j * 256, 448 * 3584 - c.j * 256);
  if (c.jj < 7) issue_range<0, 0, 3>(c);
  else {
    u32 lo; u64 pc;
    asm volatile("s_getpc_b64 %0\n.Lpf_here:\n\ts_mov_b32 %1, .Lpf_here-fused_kernel" : "=s"(pc), "=s"(lo));
    asm volatile("s_mov_b32 m0, %0\n\ts_nop 0\n\tbuffer_load_dword %1, %2, 0 offen lds"
                 :: "i"(spos(13)), "v"((w * 64 + l) * 128), "s"(mk((const char*)(pc - lo), CODE_BYTES)) : "memory");
    spare_x<0, 2>(c);
  }
  {
    const u32 win = 56 * c.hd - 8 * (c.hd & 1), i = (w & 1) * 64 + l;
    const bool ok = i < 112 && w < 6;
    set_m0<spos(17)>(c); asm volatile("s_nop 0");
    dma<0>(ok ? (i >> 2) * 2688 + (i & 3) * 16 : OOB, mk(wqkv, 448 * 2688), c.t * (28 * 2688) + (w >> 1) * 896 + win);
    c.vrp = ok ? WS_WA + c.hd * 86016 + (w >> 1) * 28672 + c.t * 1792 + i * 16 : OOB;
  }
  SB0();
  c.tag = epoch + 1;
  c.v2 = row8 * 896 + sw2; c.v2b = c.v2 + 64 * 896;
  c.vq = WS_WA + c.hd * 86016 + c.tg * 1024 + c.l16; c.vkv = c.vq + 28672 * (1 + c.r);
  c.vflag0 = WS_FLAG + (((l & 15) * 16 + c.hd) << 7);
  c.sp = mk(wphi + c.hd * 1568, 1568);
  c.sw = mk(ws, WS_END);
  c.oh = WS_H + (u32)(((c.t * 4 + c.tg) * 112 + 8 * c.j + 2 * c.r) * 1024);
  c.oo = WS_O + (u32)(((c.t * 4 + c.tg) * 16 + c.hd) * 2048);
  c.op = WS_P + (u32)((c.t * 16 + c.hd) * 16384 + c.tg * 2048);
  c.okvp = WS_KVP + (u32)((c.hd * 16 + c.t) * 4096 + c.tg * 1024);
  c.oflag = WS_FLAG + (u32)(c.t * 16 + c.hd) * 128;
  c.og = WS_G + (u32)(c.kh * 2048 + c.t * 128 + c.tg * 32) * 8;
  if (XB & 1) c.op = 0x40000000u;
  if (XB & 2) c.oh = 0x40000000u;
  if (XB & 8) c.oo = 0x40000000u;
  static_assert(WS_P % (16 * 16384) == 0, "the partner's slot is op ^ (8 * 16384)");
  c.vflag = flag_off(c, c.jj == 7); c.vone = tid == 0 ? 0u : OOB;
  { const u32 rho = l & 31, f = rho < 26 ? rho : ((rho == 28 || rho == 29) ? rho - 2 : 0);
    c.awp = upos(28) + 5 * 1024 + 2 * (28 * (4 * (l >> 5) - 4 * (c.hd & 1)) + f); }
  asm volatile("" : "+s"(c.tag), "+s"(c.oh), "+s"(c.oo), "+s"(c.op), "+s"(c.okvp), "+s"(c.oflag), "+s"(c.og) :: "memory");
  asm volatile("" : "+v"(c.vflag), "+v"(c.vflag0), "+v"(c.vone), "+v"(c.vrp));
  P1 p;
  {
    const u32 q = (l & 15) >> 2, pp = l & 3, blk = (l >> 4) & 1, h = l >> 5, r = l & 31;
    p.aw = mkseg((8 * h + q) * 128 + ((64 * c.r) ^ ((q >> 1) << 6)) + 32 * blk + 8 * pp);
    p.aq = mkseg((8 * h + q) * 64 + 32 * blk + 8 * pp);
    p.ax = (32 * c.tg + r) * 128 + ((h ^ ((r >> 1) & 7)) << 4);
  }
  if (c.jj < 7) {
    c.su = mk(wu + c.j * 256, (XB & 32) ? 0 : 448 * 3584 - c.j * 256);
    c.sd = mk(wd + c.kh * 896 * 896 + c.jj * 128, (XB & 16) ? 0 : 896 * 896 - c.jj * 128);
    c.so = mk(wo + c.kh * 224 * 896 + c.jj * 128, (XB & 16) ? 0 : 224 * 896 - c.jj * 128);
    if (c.r == 0) { ffn_wg<0>(c, p); STAMP_END(); __builtin_amdgcn_endpgm(); }
    ffn_wg<1>(c, p);
    STAMP_END();
    __builtin_amdgcn_endpgm();
  }
  if (c.r == 0) { spare_wg<0>(c, p); STAMP_END(); __builtin_amdgcn_endpgm(); }
  spare_wg<1>(c, p);
  STAMP_END();
}

#if defined(__HIP_DEVICE_COMPILE__)
#pragma clang attribute pop
#endif

__global__ void amp_k(const unsigned long long* st){
  unsigned tnow=(unsigned)__builtin_amdgcn_s_memrealtime(); asm volatile("":"+s"(tnow)::"memory");
  unsigned mn=~0u;
  for(int i=threadIdx.x;i<2048;i+=64){ unsigned a=(unsigned)st[i*32]; mn=a<mn?a:mn; }
  for(int o=32;o;o>>=1){ unsigned b=__shfl_xor(mn,o); mn=b<mn?b:mn; }
  unsigned sum=0,mx=0,cnt=0;
  for(int b=threadIdx.x;b<256;b+=64){ bool sp=(b>>5)==7; if(sp!=('s'=='s')) continue; unsigned d=(unsigned)st[(b*8+0)*32+PROBE_K]-mn; if(d<100000){ sum+=d; mx=d>mx?d:mx; cnt++; } }
  for(int o=32;o;o>>=1){ sum+=__shfl_xor(sum,o); cnt+=__shfl_xor(cnt,o); unsigned b=__shfl_xor(mx,o); mx=b>mx?b:mx; }
  unsigned long long m = 's'=='z' ? 0 : 's'=='e' ? (unsigned long long)(tnow-mn-mx)*100 : 's'=='m' ? (unsigned long long)mx*100 : (unsigned long long)sum*100/(cnt?cnt:1);
  unsigned long long t0=__builtin_amdgcn_s_memrealtime();
  while(__builtin_amdgcn_s_memrealtime()-t0<m) __builtin_amdgcn_s_sleep(4);
}
extern "C" void kernel_launch(void* const* d_in, const int* in_sizes, int n_in, void* d_out, int out_size, void* d_ws, size_t ws_size,
                              hipStream_t stream) {
  (void)hipFuncSetAttribute((const void*)fused_kernel, hipFuncAttributeMaxDynamicSharedMemorySize, LDS_BYTES);
  fused_kernel<<<256, 512, LDS_BYTES, stream>>>((const char*)d_in[0], (const char*)d_in[1], (const char*)d_in[2], (const char*)d_in[3],
                                                (const char*)d_in[5], (const char*)d_in[6], (const char*)d_in[7], (const char*)d_in[8],
                                                (char*)d_out, (char*)d_ws);
  amp_k<<<1,64,0,stream>>>((const unsigned long long*)((char*)d_ws + WS_STAMP));
}
```

```cpp
#include <hip/hip_runtime.h>
#include <hip/hip_bf16.h>
#include <type_traits>
#pragma clang diagnostic ignored "-Wint-to-pointer-cast"

typedef unsigned u32;
typedef unsigned long long u64;
typedef u32 u32x4 __attribute__((ext_vector_type(4)));
typedef u32 u32x2 __attribute__((ext_vector_type(2)));
typedef short s16x4 __attribute__((ext_vector_type(4)));
typedef __bf16 bf16x8 __attribute__((ext_vector_type(8)));
typedef __bf16 bf16x2 __attribute__((ext_vector_type(2)));
typedef float f32x16 __attribute__((ext_vector_type(16)));
typedef float f32x4 __attribute__((ext_vector_type(4)));
typedef float f32x2 __attribute__((ext_vector_type(2)));
typedef __amdgpu_buffer_rsrc_t rsrc_t;
#define DI __device__ __forceinline__
#define LAS __attribute__((address_space(3)))
#define SB0() __builtin_amdgcn_sched_barrier(0)
#define PIN1(a) asm volatile("" : "+v"(a))
#define PIN2(a, b) asm volatile("" : "+v"(a), "+v"(b))

constexpr u32 WS_FLAG = 4096;
constexpr u32 WS_G    = 131072;
constexpr u32 WS_KVR  = 65536;
constexpr u32 WS_KVP  = 262144;
constexpr u32 WS_O    = WS_KVP + 256 * 4096;
constexpr u32 WS_P    = WS_O + 64 * 32 * 1024;
constexpr u32 WS_H    = WS_P + 256 * 16384;
constexpr u32 WS_WA   = WS_H + 64 * 112 * 1024;
constexpr u32 WA_HEAD = 86 * 1024;
constexpr u32 WS_END  = WS_WA + 16 * WA_HEAD;

constexpr int UNIT = 8192, RING = 18;
constexpr u32 SCR = 0, RBASE = 2 * UNIT;
constexpr int LDS_BYTES = 163840;
constexpr u32 OOB = 0x80000000u;
constexpr u32 CODE_BYTES = 38912;
#define ABL 0
#define ORD 1
#define A4 9
#define SWP 1
#define DELAYS 99
#define NOGELU 0
#define NOFM 0
#define I7 33
#define I8 36
#define I9 38
#define I10 41
#define I11 44
#define I12 47
#define I18 62
#define NOMM 0
#define RJ 1
#define RT 2
#define RX 1
#define RK 0
#define XB 0
#define PH 7
#define OL 1
#define OUTAUX 0
#define NB0 10
#define C2 196
#define P2 186
#define NPRE 0
#define NB1 2
#define YR1 199
#define O1 204
#define PROV 9
#define POLL0 5
#define POLL1 17
#define GATE 11
#define PFS 0
#define TOUCH1 0
#define TOUCH2 0
#define XP 24
#define XQ 14
#define H1Q 6
#define H1R 10
#define PSLEEP 0
#define OPOS 184
#define YP 10
#define CHK1 19
#define FLAG0 2
#define I19 65
#define I20 68
constexpr u32 SPIN_MAX = ABL ? 0 : 1u << 16;
constexpr float LOG2E = 1.4426950408889634f;

#if defined(__HIP_DEVICE_COMPILE__)
#pragma clang attribute push(__attribute__((target("no-packed-fp32-ops"))), apply_to = function)
#endif
constexpr u32 spos(int k) { return RBASE + k * UNIT; }
constexpr int w2c_(int i) { constexpr int t[18] = {16, 17, 0, 1, 2, 3, 4, 5, 6, 7, 8, 9, 10, 11, 12, 13, 14, 15}; return t[i]; }
constexpr int w2s_(int c) { constexpr int t[18] = {2, 3, 4, 5, 6, 10, 11, 12, 13, 15, 14, 16, 7, 8, -2, -1, 0, 1}; return t[c]; }
constexpr int uslot(int n) {
  constexpr int t[26] = {-1, 10, 11, 12, 13, 14, 15, 16, 17, 0, 1, 2, 3, 4, 5, 6, 10, 11, 12, 13, 14, 16, 7, 8, 9, 17};
  return n < 28 ? n % RING : n < 54 ? t[n - 28] : w2s_(w2c_(n - 54));
}
constexpr u32 upos(int n) { return spos(uslot(n)); }
constexpr u32 VBUF = RBASE + 15 * UNIT;
constexpr int imin(int a, int b) { return a < b ? a : b; }

struct Ctx {
  const char *gw, *wd, *wo;
  char *out, *ws;
  int t, kh, jj, hd, j, w, tg, r;
  u32 lane, l16, m0w;
  u32 vx, vx2, vw, vq, vkv, v2, v2b;
  rsrc_t sx, sg, su, sq, sp, sd, so, sw;
  u64 tag;
  u32 rot[7];
  u32 oh, oo, op, okvp;
  u32 oflag, og;
  u32 vrp, vflag0;
  u32 vflag, vone;
};

DI rsrc_t mk(const void* p, u32 bytes) { return __builtin_amdgcn_make_buffer_rsrc((void*)p, 0, bytes, 0x00020000); }
template <u32 L> DI void set_m0(const Ctx& c) { if (!(ABL & 1)) asm volatile("s_add_u32 m0, %0, %1" :: "s"(c.m0w), "i"(L) : "memory", "scc"); }
DI void dma_dev(u32 voff, rsrc_t rs, u32 soff) {
  if (!(ABL & 1)) asm volatile("buffer_load_dwordx4 %0, %1, %2 offen sc1 lds" :: "v"(voff), "s"(rs), "s"(soff) : "memory");
}
template <u32 OFF> DI void dma(u32 voff, rsrc_t rs, u32 soff) {
  if (!(ABL & 1)) asm volatile("buffer_load_dwordx4 %0, %1, %2 offen offset:%3 lds" :: "v"(voff), "s"(rs), "s"(soff), "i"(OFF) : "memory");
}
template <int N> DI void wait_bar() { asm volatile("s_waitcnt vmcnt(%0) lgkmcnt(0)\n\ts_barrier" :: "n"(N) : "memory"); }
template <int N> DI void wait_bar_v() { asm volatile("s_waitcnt vmcnt(%0)\n\ts_barrier" :: "n"(N) : "memory"); }
DI void bar_l() { asm volatile("s_waitcnt lgkmcnt(0)\n\ts_barrier" ::: "memory"); }

constexpr u32 ioff_(int n) { return n >= 36 && n < 54 && !(n < 47 && n % 3 == 0) ? 128 : 0; }
template <int N> DI void unit_m0(const Ctx& c) { set_m0<upos(N) - ioff_(N)>(c); }
template <int R, int N> DI void unit_go(const Ctx& c) {
  if constexpr (N < 21) {
    if constexpr (N % 3 < 2) dma<0>(N % 3 ? c.vx2 : c.vx, c.sx, c.rot[N / 3] * 128);
    else dma<0>(c.vw, c.sg, c.rot[N / 3] * (64 * 3584));
  } else if constexpr (N >= 29 && N < 36) dma_dev(c.vkv, c.sw, c.rot[N - 29] * 4096);
  else if constexpr (N == 28) dma_dev(c.vq, c.sw, c.w < 2 ? 84u * 1024u : 0x40000000u);
  else if constexpr (N < 28) dma<0>(c.vw, c.su, c.rot[N - 21] * (64 * 3584));
  else if constexpr (N < 47) {
    if constexpr (N % 3 == 0) dma_dev(c.vq, c.sw, N == 45 && R == 1 ? 0x40000000u : c.rot[(2 * (N - 36) / 3 + R) % 7] * 4096);
    else dma<128>(c.vw, c.sg, c.rot[2 * ((N - 36) / 3) + N % 3 - 1] * (64 * 3584));
  } else if constexpr (N < 54) dma<128>(c.vw, c.su, c.rot[N - 47] * (64 * 3584));
  else {
    constexpr int k = w2c_(N - 54);
    if constexpr (k < 14) dma<0>(k < 7 ? c.v2 : c.v2b, c.sd, c.rot[k % 7] * (128 * 896));
    else dma<0>((k & 1) ? c.v2b : c.v2, c.so, k < 16 ? 0u : 128u * 896u);
  }
}
template <int N, int H> DI void unit_half(const Ctx& c) {
  set_m0<upos(N) + H * 4096>(c);
  constexpr int k = w2c_(N - 54);
  const u32 so = (k < 14 ? c.rot[k % 7] * (128 * 896) : k < 16 ? 0u : 128u * 896u) + H * (32 * 896);
  asm volatile("s_nop 0");
  dma<0>((k < 14 ? k >= 7 : (k & 1)) ? c.v2b : c.v2, k < 14 ? c.sd : c.so, so);
}
template <int R, int A, int B> DI void issue_range(const Ctx& c) {
  if constexpr (A < B) { unit_m0<A>(c); asm volatile("s_nop 0"); unit_go<R, A>(c); issue_range<R, A + 1, B>(c); }
}

struct Seg { u32 b[3]; };
DI Seg mkseg(u32 a) { Seg s; s.b[0] = a; s.b[1] = a + 65536; s.b[2] = a + 131072; asm volatile("" : "+v"(s.b[0]), "+v"(s.b[1]), "+v"(s.b[2])); return s; }
DI u32 sa(const Seg& s, u32 off) { return s.b[off >> 16] + (off & 0xffffu); }
DI f32x16 mfma(bf16x8 a, bf16x8 b, f32x16 c) { if (NOMM) { asm volatile("" : "+v"(c) : "v"(a), "v"(b)); return c; } return __builtin_amdgcn_mfma_f32_32x32x16_bf16(a, b, c, 0, 0, 0); }
DI bf16x8 as_frag(u32x4 v) { return __builtin_bit_cast(bf16x8, v); }
DI bf16x8 tr_frag(u32 a0, u32 a1) {
  if (NOMM > 1) { bf16x8 z; asm volatile("" : "=v"(z)); return z; }
  s16x4 lo = __builtin_amdgcn_ds_read_tr16_b64_v4i16((LAS s16x4*)a0);
  s16x4 hi = __builtin_amdgcn_ds_read_tr16_b64_v4i16((LAS s16x4*)a1);
  return __builtin_bit_cast(bf16x8, __builtin_shufflevector(lo, hi, 0, 1, 2, 3, 4, 5, 6, 7));
}
DI u32x4 lds_rd128(u32 a) { return *(LAS u32x4*)a; }
DI void lds_wr128(u32 a, u32x4 v) { *(LAS u32x4*)a = v; }
DI u32 f2u(float f) { return __builtin_bit_cast(u32, f); }
DI float u2f(u32 u) { return __builtin_bit_cast(float, u); }
DI u32 pk(float a, float b) { f32x2 t = {a, b}; return __builtin_bit_cast(u32, __builtin_convertvector(t, bf16x2)); }
template <int S, class V> DI u32x4 pack8(const V& v) {
  u32x4 r = {pk(v[8 * S], v[8 * S + 1]), pk(v[8 * S + 2], v[8 * S + 3]), pk(v[8 * S + 4], v[8 * S + 5]), pk(v[8 * S + 6], v[8 * S + 7])};
  return r;
}
DI float blo(u32 d) { return u2f(d << 16); }
DI float bhi(u32 d) { return u2f(d & 0xffff0000u); }
DI u32 umax(u32 a, u32 b) { return a > b ? a : b; }
DI bool any_(bool p) { return __builtin_amdgcn_ballot_w64(p) != 0; }
DI u32x4 sanit(u32x4 v) { return __builtin_elementwise_min(v, (u32x4){0xfffffffeu, 0xfffffffeu, 0xfffffffeu, 0xfffffffeu}); }
DI void st_dev(const Ctx& c, u32x4 v, u32 voff, u32 soff) {
  if (ABL & 8) { asm volatile("" :: "v"(v)); return; }
  __builtin_amdgcn_raw_buffer_store_b128(v, c.sw, voff, soff, 16);
  asm volatile("s_nop 1" :: "v"(v) : "memory");
}
DI u32x4 ld_dev(const Ctx& c, u32 voff, u32 soff) {
  if (ABL & 16) { u32x4 z = {voff, soff, voff, soff}; asm volatile("" : "+v"(z)); return z; }
  return __builtin_amdgcn_raw_buffer_load_b128(c.sw, voff, soff, 16);
}
DI f32x2 xhalf(float v) { auto p = __builtin_amdgcn_permlane32_swap(f2u(v), f2u(v), false, false); f32x2 r = {u2f(p[0]), u2f(p[1])}; return r; }

#define STAMP(i)
#define STAMP_END()

struct P1 {
  Seg aw, aq; u32 ax, ka, kb, kc;
  bf16x8 xf[28], f[8], af[8], vf[8];
  f32x16 g1, u1, a0, a1, g2, u2, fp, k0, c[2];
  Seg s2[2]; u32 ob[2];
  u32x4 h2[14], of[8];
  u32x4 wp[2], ph[4], h1[14], kv[4];
  float y[16], e[14], sum, mxl, mnl;
  u32x2 poll;
  float gq, om;
  u32 hbase;
};

DI void wphi_rd(const Ctx& c, P1& p) { p.wp[0] = lds_rd128(spos(-1) + c.l16); p.wp[1] = lds_rd128(spos(-1) + 1024 + c.l16); }
DI void repack(const Ctx& c) {
  const u32 l = c.lane;
  const u32 a = spos(17) + c.m0w + (8 * (l >> 5) + ((l & 15) >> 2)) * 64 + 32 * ((l >> 4) & 1) + 8 * (l & 3);
  u32x4 v = __builtin_bit_cast(u32x4, tr_frag(a, a + 256));
  if (c.t == 14) {
    const u32 rho = l & 31, f = rho < 26 ? rho : ((rho == 28 || rho == 29) ? rho - 2 : 0);
    const u32 o = spos(17) + c.m0w + 2 * (112 * (l >> 5) + f);
#pragma unroll
    for (int d = 0; d < 4; ++d) {
      const u32 od = o + 56 * (8 * (d >> 1) + 2 * (d & 1));
      v[d] = (u32)*(LAS unsigned short*)od | ((u32)*(LAS unsigned short*)(od + 56) << 16);
    }
  }
  st_dev(c, v, c.vrp, 0);
}
template <int K> DI void fm_slice(P1& p) {
  if constexpr (NOFM) { if constexpr (K == 0) { p.ph[0] = p.ph[2] = pack8<0>(p.a0); p.ph[1] = p.ph[3] = pack8<1>(p.a0); PIN2(p.ph[0], p.ph[1]); PIN2(p.ph[2], p.ph[3]); } }
  else if constexpr (K == 0) { p.fp = mfma(as_frag(p.wp[0]), as_frag(pack8<0>(p.a0)), (f32x16){}); }
  else if constexpr (K == 1) { p.fp = mfma(as_frag(p.wp[1]), as_frag(pack8<1>(p.a0)), p.fp); }
  else if constexpr (K == 2) { }
  else if constexpr (K == 3) {
    PIN1(p.fp);
    float mx = p.fp[0];
#pragma unroll
    for (int i = 1; i < 14; ++i) mx = __builtin_fmaxf(mx, p.fp[i]);
    p.mxl = mx; PIN1(p.mxl);
  } else if constexpr (K == 4) {
    float mn = p.fp[0];
#pragma unroll
    for (int i = 1; i < 14; ++i) mn = __builtin_fminf(mn, p.fp[i]);
    p.mnl = mn; PIN1(p.mnl);
  } else if constexpr (K == 5) {
    f32x2 t = xhalf(p.mxl); p.mxl = __builtin_fmaxf(t[0], t[1]) * LOG2E;
    t = xhalf(p.mnl); p.mnl = __builtin_fminf(t[0], t[1]) * LOG2E;
    PIN2(p.mxl, p.mnl);
  } else {
    constexpr int NEG = K >= 15, k = K - 6 - 9 * NEG;
    if constexpr (k < 5) {
#pragma unroll
      for (int i = 3 * k; i < imin(14, 3 * k + 3); ++i) {
        p.e[i] = __builtin_amdgcn_exp2f(NEG ? __builtin_fmaf(p.fp[i], -LOG2E, p.mnl) : __builtin_fmaf(p.fp[i], LOG2E, -p.mxl));
        PIN1(p.e[i]);
      }
      if constexpr (k == 1) p.sum = p.e[0] + p.e[1] + p.e[2];
      if constexpr (k > 1) p.sum += p.e[3 * k - 3] + p.e[3 * k - 2] + p.e[3 * k - 1];
      if constexpr (k > 0) PIN1(p.sum);
    } else if constexpr (k == 5) {
      f32x2 t = xhalf(p.sum + p.e[12] + p.e[13]); p.sum = __builtin_amdgcn_rcpf(t[0] + t[1]); PIN1(p.sum);
    } else if constexpr (k < 8) {
#pragma unroll
      for (int i = 7 * (k - 6); i < 7 * (k - 5); ++i) { p.e[i] *= p.sum; PIN1(p.e[i]); }
    } else {
      u32x4 o1 = {pk(p.e[8], p.e[9]), pk(p.e[10], p.e[11]), pk(p.e[12], p.e[13]), 0u};
      p.ph[2 * NEG] = pack8<0>(p.e); p.ph[2 * NEG + 1] = o1; PIN2(p.ph[2 * NEG], p.ph[2 * NEG + 1]);
    }
  }
}
template <int A, int B> DI void fm_slices(P1& p) { if constexpr (A < B) { fm_slice<A>(p); fm_slices<A + 1, B>(p); } }
template <u32 VB, int S> DI void v_handoff(const Ctx& c, const f32x16& av) {
  u32x4 vf = pack8<S>(av);
  const bool one = (c.lane & 31) == ((c.hd & 1) ? 0u : 28u);
#pragma unroll
  for (int d = 0; d < 4; ++d) vf[d] = one ? 0x3f803f80u : vf[d];
  lds_wr128(VB + c.tg * 2048 + c.l16 + S * 1024, vf);
}
template <int T> DI void phi_image(const Ctx& c, P1& p) {
  const u32 tok = c.lane & 31, h = c.lane >> 5;
  const u32 ft = (((tok >> 1) & 1) << 3) | (tok & 1) | (((tok >> 2) & 3) << 1);
  const u32 wa = SCR + c.tg * 4096 + tok * 128 + ((h ^ ft) << 3);
#pragma unroll
  for (int g = 0; g < 4; ++g) {
    u32x2 v = {p.ph[2 * T + (g >> 1)][2 * (g & 1)], p.ph[2 * T + (g >> 1)][2 * (g & 1) + 1]};
    *(LAS u32x2*)(wa ^ (u32)((8 * T + 2 * g) << 3)) = v;
  }
}
template <u32 VB, int K> DI void kv_stage(const Ctx& c, P1& p) {
  if constexpr (K >= 2 && K <= 5) {
    p.k0 = mfma(p.af[2 * K - 4], p.vf[2 * K - 4], K == 2 ? (f32x16){} : p.k0);
    p.k0 = mfma(p.af[2 * K - 3], p.vf[2 * K - 3], p.k0);
  }
  if constexpr (K == 0) {
    const u32 l = c.lane, h = l >> 5, q = (l & 15) >> 2, pp = l & 3, blk = (l >> 4) & 1;
    p.ka = SCR + (4 * h + q) * 128 + (((4 * blk + pp) ^ (q & 1) ^ (h << 1) ^ ((q >> 1) << 3) ^ ((c.tg & 1) << 3)) << 3);
    p.kb = p.ka ^ 32u; p.kc = VB + c.l16;
    asm volatile("" : "+v"(p.ka), "+v"(p.kb), "+v"(p.kc));
  }
  if constexpr (K < 4) {
#pragma unroll
    for (int s = 0; s < 2; ++s) {
      p.af[2 * K + s] = tr_frag(p.ka + K * 4096 + s * 2048, p.kb + K * 4096 + s * 2048 + 1024);
      p.vf[2 * K + s] = as_frag(lds_rd128(p.kc + K * 2048 + s * 1024));
    }
  }
  if constexpr (K == 7) {
    const u32 vo = c.tg < 2 ? c.l16 : OOB;
    st_dev(c, sanit(pack8<0>(p.k0)), vo, c.okvp + c.tg * 1024);
    st_dev(c, sanit(pack8<1>(p.k0)), vo + 1024, c.okvp + c.tg * 1024);
  }
}
DI void add8(float (&s)[8], u32x4 v) {
#pragma unroll
  for (int d = 0; d < 4; ++d) { s[2 * d] += blo(v[d]); s[2 * d + 1] += bhi(v[d]); }
}
DI void set8(float (&s)[8], u32x4 v) {
#pragma unroll
  for (int d = 0; d < 4; ++d) { s[2 * d] = blo(v[d]); s[2 * d + 1] = bhi(v[d]); }
}
template <int A, int B, int N> DI bool has_mark(const u32x4 (&v)[N]) {
  u32 m = 0;
#pragma unroll
  for (int i = A; i < B; ++i) m = umax(umax(m, v[i][0]), umax(v[i][1], umax(v[i][2], v[i][3])));
  return any_(m == 0xffffffffu);
}
template <int A, int B, int N, class F> DI void chk(u32x4 (&v)[N], F&& reload) {
  if (__builtin_expect(SPIN_MAX && has_mark<A, B>(v), 0)) {
    u32 spin = 0;
    do { asm volatile("" ::: "memory"); reload(); } while (has_mark<A, B>(v) && ++spin < SPIN_MAX);
  }
}
template <int A, int B, int N> DI void ld_lin(const Ctx& c, u32x4 (&v)[N], u32 soff, u32 stride) {
#pragma unroll
  for (int i = A; i < B; ++i) v[i] = ld_dev(c, c.l16, soff + i * stride);
}
template <int A, int B, int N> DI void ld_1k(const Ctx& c, u32x4 (&v)[N], u32 soff) {
#pragma unroll
  for (int i = A; i < B; ++i) v[i] = ld_dev(c, c.l16 + (i & 3) * 1024, soff + (i >> 2) * 4096);
}
template <int A, int B> DI void ld_h(const Ctx& c, u32x4 (&v)[14], u32 voff, u32 soff) {
#pragma unroll
  for (int i = A; i < B; ++i) v[i] = ld_dev(c, voff + (i & 1) * 1024, soff + c.rot[i >> 1] * 8192);
}
DI void o_publish(const Ctx& c, const u32x4 (&kv)[4], const u32x4 (&phq)[4], float g) {
  f32x16 n = {};
#pragma unroll
  for (int i = 0; i < 4; ++i) n = mfma(as_frag(kv[i]), as_frag(phq[i]), n);
  const bool odd = c.hd & 1;
  f32x2 t = xhalf(odd ? n[0] : n[12]);
  float sc = g * __builtin_amdgcn_rcpf((odd ? t[0] : t[1]) + 1e-6f);
#pragma unroll
  for (int i = 0; i < 16; ++i) n[i] *= sc;
  u32x4 f0 = sanit(pack8<0>(n)), f1 = sanit(pack8<1>(n));
  const bool z0 = odd && c.lane < 32, z1 = !odd && c.lane >= 32;
  f0[0] = z0 ? 0 : f0[0]; f0[1] = z0 ? 0 : f0[1];
  f1[2] = z1 ? 0 : f1[2]; f1[3] = z1 ? 0 : f1[3];
  st_dev(c, f0, c.l16, c.oo);
  st_dev(c, f1, c.l16 + 1024, c.oo);
}
DI u32 flag_off(const Ctx& c, bool spare) {
  const u32 i = c.lane & 15;
  u32 red = (c.hd * 16 + 7 + 8 * (i & 1)) << 7, sp = ((c.t * 16 + 7 + 8 * c.kh) << 7) + 8;
  u32 lo = spare ? (i * 16 + c.t) << 7 : ((c.t * 16 + (i < 7 ? 8 * c.kh + i : c.hd ^ 8)) << 7) + 8;
  asm volatile("" : "+v"(red), "+v"(sp), "+v"(lo));
  return WS_FLAG + ((c.lane & 16) ? red : (spare || i < 8) ? lo : sp);
}
DI bool flag_bad(const Ctx& c, u32x2 f) { return any_(f[0] != (u32)c.tag || f[1] != (u32)(c.tag >> 32)); }
DI void flag_spin(const Ctx& c, u32x2 f, u32 voff) {
  if (__builtin_expect(SPIN_MAX && flag_bad(c, f), 0)) {
    u32 spin = 0;
    do {
      __builtin_amdgcn_s_sleep(2);
      asm volatile("" ::: "memory");
      f = __builtin_amdgcn_raw_buffer_load_b64(c.sw, voff, 0, 16);
    } while (flag_bad(c, f) && ++spin < SPIN_MAX);
  }
  asm volatile("" ::: "memory");
}
DI void flag_publish(const Ctx& c, u32 word) {
  u32x2 v = {(u32)c.tag, (u32)(c.tag >> 32)};
  __builtin_amdgcn_raw_buffer_store_b64(v, c.sw, c.vone + word * 8, c.oflag, 16);
}
DI u32x2 ld_g(const Ctx& c) { return __builtin_amdgcn_raw_buffer_load_b64(c.sw, (c.lane & 31) * 8, c.og, 16); }

template <u32 L, bool DEV> DI void touch(u32 voff, rsrc_t rs, u32 soff) {
  if (ABL & 9) return;
  if constexpr (DEV) asm volatile("s_mov_b32 m0, %0\n\ts_nop 0\n\tbuffer_load_dword %1, %2, %3 offen sc1 lds" :: "i"(L), "v"(voff), "s"(rs), "s"(soff) : "memory");
  else asm volatile("s_mov_b32 m0, %0\n\ts_nop 0\n\tbuffer_load_dword %1, %2, %3 offen lds" :: "i"(L), "v"(voff), "s"(rs), "s"(soff) : "memory");
}

constexpr int PRO = PROV;
constexpr int NS = 21;
constexpr int need_(int s) {
  return s < 0 ? 0 : s < 7 ? 3 * (s + 1) : s < 9 ? 23 + 2 * (s - 7) : s < 12 ? 31 + 2 * (s - 9) : s < 14 ? 36 : s < 18 ? 39 + 3 * (s - 14) : s < 20 ? 50 + 2 * (s - 18) : 62;
}
constexpr int iss_(int s) {
  constexpr int t[14] = {I7, I8, I9, I10, I11, I12, 49, 51, 53, 56, 59, I18, I19, I20};
  return s < 0 ? PRO : s < 7 ? imin(imin(28, 18 + need_(s - 2)), PRO + 3 * (s + 1) + (s >= A4 ? s - A4 + 1 : 0)) : t[s - 7];
}
constexpr int ustep(int n) {
  if (n == 34 || n == 28) return 16;
  if (n >= 25 && n < 28) return n == 25 ? 12 : 13;
  int s = 0; while (s < 20 && need_(s) <= n) ++s; return s;
}
constexpr bool ring_ok() {
  for (int s = -1; s < NS; ++s)
    for (int n = s < 0 ? 0 : iss_(s - 1); n < iss_(s); ++n) {
      if (uslot(n) == -2 && s < 18) return false;
      for (int m = 0; m < n; ++m) if (uslot(m) == uslot(n) && ustep(m) + 2 > s) return false;
    }
  return true;
}
static_assert(ring_ok(), "ring schedule");
constexpr int S_FLAG0 = FLAG0, S_POLL0 = POLL0, S_CHK0 = 7, S_GATE = GATE, S_CHKG = 13, S_FLAG1 = 16, S_POLL = POLL1, S_CHK = CHK1;
constexpr int oth_(int r, int s) {
  return s == S_FLAG0 ? 1 - r : s == S_POLL0 ? 1 - r : s == 10 ? (GATE == 10) : s == 11 ? 1 - r + (GATE == 11) : s == 12 ? r : s == 14 ? 2 : s == 16 ? 3 - 3 * r + TOUCH1 :
         s == S_POLL ? 1 - r : s == 18 ? (r || CHK1 == 19 ? 0 : 4) : s == 19 || s == 20 ? (r ? 0 : CHK1 == 19 ? 7 : 5) : 0;
}
constexpr int tot_(int r, int s) { return iss_(s) - iss_(s - 1) + oth_(r, s); }
constexpr int vm_(int r, int s) {
  int sp = -1; while (iss_(sp) < need_(s)) ++sp;
  int n = iss_(sp) - need_(s) + (sp < 0 ? 2 - r : 0);
  for (int k = sp + 1; k < s; ++k) n += tot_(r, k);
  if (s == S_FLAG0) { int m = 0; for (int k = 0; k < s; ++k) m += tot_(r, k); n = imin(n, m); }
  if (s == S_FLAG1) n = imin(n, tot_(r, 15));
  if (((s == S_CHK0 || s == S_CHK) && r == 0) || s == S_CHKG) {
    int m = -1; for (int k = s == S_CHK0 ? S_POLL0 : s == S_CHKG ? S_GATE : S_POLL; k < s; ++k) m += tot_(r, k);
    n = imin(n, m);
  }
  return n;
}
constexpr int P2_(int r) { return r ? 168 : 140; }
constexpr int item0_(int r, int s) { return s < 7 ? 4 * s : (!r || s < 15) ? 8 * s - 28 : s < 18 ? 84 + 16 * (s - 14) : 8 * s; }
template <int R, int J> DI void rd_item(P1& p) {
  constexpr int U2 = P2_(R) - 28;
  if constexpr (J >= P2_(R)) {
    constexpr int k = J - P2_(R);
    if constexpr (k < 56) {
      constexpr u32 o = spos(w2s_(k / 4)) + ((k / 2) & 1) * 2048;
      p.f[J % 8] = tr_frag(sa(p.s2[k & 1], o), sa(p.s2[k & 1], o + 1024));
    } else if constexpr (k < 72) {
      constexpr int i = (k - 56) / 2, hh = i >> 1;
      constexpr u32 o = (28 * hh - 4 * (hh & 1) + 16 * (i & 1)) * 128;
      p.f[J % 8] = tr_frag(p.ob[k & 1] + o, p.ob[k & 1] + o + 1024);
    }
  } else if constexpr ((J >= 44 && J < 72) || (R == 1 && J >= 84 && J < U2 && (J & 1))) {
    constexpr int k = J < 72 ? J - 44 : (J - 84) / 2, ch = k / 4;
    constexpr u32 o = (J < 72 ? upos(29 + ch) + R * 4096 : upos(36 + 3 * (ch / 2)) + (ch & 1) * 4096) + (k % 4) * 1024;
    p.f[J % 8] = as_frag(lds_rd128(sa(p.aq, o)));
  } else {
    constexpr int k = J < 84 ? J : J < U2 ? (J - 84) / (1 + R) : J - U2, ch = k / 4;
    constexpr u32 o = upos(J < 28 ? 3 * ch + 2 : J < 44 ? 14 + ch : J < 84 ? 7 + ch : J < U2 ? 37 + 3 * (ch / 2) + (ch & 1) : 47 + ch) + (k % 4) * 2048;
    p.f[J % 8] = tr_frag(sa(p.aw, o), sa(p.aw, o + 512));
  }
}
template <int R, int J> DI void mm_item(P1& p) {
  constexpr int U2 = P2_(R) - 28;
  if constexpr (J < 0) {}
  else if constexpr (J >= P2_(R)) {
    constexpr int k = J - P2_(R);
    p.c[k & 1] = mfma(p.f[J % 8], as_frag(k < 28 ? p.h1[(k / 2) % 14] : k < 56 ? p.h2[(k / 2) % 14] : p.of[(k / 2 + 4) % 8]), p.c[k & 1]);
  } else if constexpr (J >= 44 && J < 72) {
    if constexpr (R == 1) p.a1 = mfma(p.xf[J - 44], p.f[J % 8], p.a1); else p.a0 = mfma(p.f[J % 8], p.xf[J - 44], p.a0);
  } else {
    f32x16& acc = J < 28 ? p.g1 : J < 84 ? p.u1 : J >= U2 ? p.u2 : (R == 1 && (J & 1)) ? p.a0 : p.g2;
    acc = mfma(p.f[J % 8], p.xf[J < 84 ? J % 28 : J < U2 ? (J - 84) / (1 + R) : J - U2], acc);
  }
}
template <int R, int A, int B> DI void mm_range(P1& p) { if constexpr (A < B) { mm_item<R, A>(p); mm_range<R, A + 1, B>(p); } }

constexpr float GK1 = -2.0f * 0.7978845608028654f * LOG2E, GK2 = GK1 * 0.044715f;
template <bool SC, int OP> DI void gelu_op(float (&y)[16], const f32x16& g, float om) {
  constexpr int st = OP % 4, i = OP / 4 - st;
  if constexpr (NOGELU && i >= 0 && i < 16) { if constexpr (st == 3) { y[i] = g[i]; PIN1(y[i]); } }
  else if constexpr (i >= 0 && i < 16) {
    if constexpr (st == 0) y[i] = g[i] * __builtin_fmaf(g[i] * g[i], GK2, GK1);
    else if constexpr (st == 1) y[i] = __builtin_amdgcn_exp2f(y[i]);
    else if constexpr (st == 2) y[i] = __builtin_amdgcn_rcpf(1.0f + y[i]);
    else y[i] = SC ? (g[i] * om) * y[i] : g[i] * y[i];
    PIN1(y[i]);
  }
}
constexpr int gcost(int op) { return op / 4 - op % 4 < 0 || op / 4 - op % 4 > 15 ? 0 : op % 4 == 0 ? 3 : op % 4 == 1 ? 2 : op % 4 == 2 ? 3 : 1; }
constexpr int gcum(int k, int n) { if (k >= n) return 76; int op = 0, cst = 0; while (cst * n < k * 144) cst += gcost(op++); return op; }
template <bool SC, int A, int B> DI void gelu_ops(float (&y)[16], const f32x16& g, float om) { if constexpr (A < B) { gelu_op<SC, A>(y, g, om); gelu_ops<SC, A + 1, B>(y, g, om); } }
template <int S> DI void h_store(const Ctx& c, const float (&y)[16], const f32x16& u, int blk) {
  float h[8];
#pragma unroll
  for (int i = 0; i < 8; ++i) h[i] = y[8 * S + i] * u[8 * S + i];
  st_dev(c, pack8<0>(h), c.l16 + S * 1024, c.oh + blk * 4096);
}
template <int A, int B> DI void y_scale(P1& p) {
#pragma unroll
  for (int i = A; i < B; ++i) { p.y[i] *= p.om; PIN1(p.y[i]); }
}
DI void addr2(const Ctx& c, P1& p) {
  const u32 l = c.lane, ks = c.tg >> 1, qq = (l & 15) >> 2;
  const u32 bk = ((4 * (l >> 5) + qq) * 128 + ((qq >> 1) << 6) + 32 * ((l >> 4) & 1) + 8 * (l & 3)) ;
  p.s2[0] = mkseg(bk + ks * 4096); p.s2[1] = mkseg((bk ^ 64) + ks * 4096);
  p.ob[0] = bk + ks * (112 * 128); p.ob[1] = (bk ^ 64) + ks * (112 * 128);
  PIN2(p.ob[0], p.ob[1]);
}
template <int R, int S, int K, int J> DI void slot_work(Ctx& c, P1& p) {
  if constexpr (S < 7) p.xf[J] = as_frag(lds_rd128((p.ax + upos(3 * S)) ^ (u32)(32 * K)));
  if constexpr (R == 0) {
    if constexpr (J >= 33 && J < 75) gelu_ops<false, gcum(J - 33, 42), gcum(J - 32, 42)>(p.y, p.g1, 0.f);
    if constexpr (J == 74) wphi_rd(c, p);
    if constexpr (J == 78 || J == 79) y_scale<8 * (J - 78), 8 * (J - 77)>(p);
    constexpr int F0 = J < 84 ? J - 77 : J < 89 ? 7 + (3 * (J - 84) + 1) / 2 : J < 91 ? 15 : 15 + (9 * (J - 91) + 7) / 8;
    constexpr int F1 = J + 1 < 84 ? J - 76 : J + 1 < 89 ? 7 + (3 * (J - 83) + 1) / 2 : J + 1 < 91 ? 15 : 15 + (9 * (J - 90) + 7) / 8;
    if constexpr (J >= 77 && J < 99) fm_slices<F0, imin(24, F1)>(p);
    if constexpr (J == 92) phi_image<0>(c, p);
    if constexpr (J == 99) phi_image<1>(c, p);
    if constexpr (S == 16) kv_stage<VBUF, K>(c, p);
    if constexpr (CHK1 == 19 && S >= 19 && K >= 1) ld_h<7 * (S - 19) + K - 1, 7 * (S - 19) + K>(c, p.h1, c.l16, p.hbase);
    if constexpr (CHK1 == 18 && S == 18 && (K & 1)) ld_h<K / 2, K / 2 + 1>(c, p.h1, c.l16, p.hbase);
    if constexpr (CHK1 == 18 && S >= 19 && ((K & 1) || K == 4)) { constexpr int i = 4 + 5 * (S - 19) + (K < 4 ? K / 2 : (K + 1) / 2); ld_h<i, i + 1>(c, p.h1, c.l16, p.hbase); }
  } else {
    if constexpr (J >= 33 && J < 77) gelu_ops<false, gcum(J - 33, 44), gcum(J - 32, 44)>(p.y, p.g1, 0.f);
    if constexpr (J == 77 || J == 78) v_handoff<VBUF, J - 77>(c, p.a1);
    if constexpr (J == 79) wphi_rd(c, p);
    if constexpr (J == 83) PIN2(p.wp[0], p.wp[1]);
    if constexpr (J >= 84 && J < 88) y_scale<4 * (J - 84), 4 * (J - 83)>(p);
  }
  if constexpr (J == 89 || J == 90) h_store<J - 89>(c, p.y, p.u1, 0);
  if constexpr (J >= P2_(R) - 23) gelu_ops<true, gcum(J - P2_(R) + 23, 27), gcum(J - P2_(R) + 24, 27)>(p.y, p.g2, p.om);
  if constexpr (J == P2_(R) - 26) addr2(c, p);
  if constexpr (TOUCH1 && S == S_FLAG1 && K == 6) touch<upos(45) + 4096, true>(c.lane < 16 ? c.lane * 128 : OOB, c.sw, c.oh);
  if constexpr (S == 11 + R && K == 3) st_dev(c, (u32x4){~0u, ~0u, ~0u, ~0u}, c.l16 + R * 1024, c.op);
}
template <int R, int S, int J0, int K, int N> DI void slots(Ctx& c, P1& p) {
  if constexpr (K < N) {
    constexpr int nu = iss_(S) - iss_(S - 1), u0 = iss_(S - 1);
    constexpr int ua = K == 0 ? 0 : ((2 * K + R) * nu) / (2 * N), ub = K == N - 1 ? nu : ((2 * K + 2 + R) * nu) / (2 * N);
    if constexpr (R == 1 && S >= 18 && (K == 0 || K == 4)) __builtin_amdgcn_s_setprio(K == 0);
    mm_item<R, J0 + K - 4>(p);
    if (ORD) SB0();
    if constexpr (ub > ua) unit_m0<u0 + ua>(c);
    rd_item<R, (J0 + K) ^ SWP>(p);
    if constexpr (ub > ua) { unit_go<R, u0 + ua>(c); issue_range<R, u0 + ua + 1, u0 + ub>(c); }
    if (ORD) SB0();
    slot_work<R, S, K, J0 + K>(c, p);
    SB0();
    slots<R, S, J0, K + 1, N>(c, p);
  }
}
template <int R, int S> DI void step(Ctx& c, P1& p) {
  if constexpr ((S == S_CHK || S == S_CHK0) && R == 0) {
    asm volatile("s_waitcnt vmcnt(%1)" : "+v"(p.poll) : "n"(vm_(R, S)) : "memory");
    PIN1(S == S_CHK ? (CHK1 == 19 ? p.u2 : p.g2) : p.g1);
    flag_spin(c, p.poll, S == S_CHK ? c.vflag : c.vflag0);
    asm volatile("s_barrier" ::: "memory");
  } else if constexpr (S == S_CHKG) {
    asm volatile("s_waitcnt vmcnt(%1)\n\ts_barrier" : "+v"(p.poll) : "n"(vm_(R, S)) : "memory");
    {
      PIN2(p.u1, R ? p.a1 : p.a0);
      if (__builtin_expect(SPIN_MAX && any_(p.poll[1] != (u32)c.tag), 0)) {
        u32 spin = 0;
        do { __builtin_amdgcn_s_sleep(2); asm volatile("" ::: "memory"); p.poll = ld_g(c); } while (any_(p.poll[1] != (u32)c.tag) && ++spin < SPIN_MAX);
      }
      p.gq = u2f(p.poll[0]); p.om = 1.0f - p.gq;
      PIN2(p.gq, p.om);
    }
  } else if constexpr ((S == 16 && R == 0) || (S == 20 && NPRE > 4)) wait_bar<vm_(R, S)>();
  else wait_bar_v<vm_(R, S)>();
  if constexpr (S < 2 || S >= 6) STAMP(S < 2 ? S + 1 : S - 3);
  if constexpr (S == DELAYS) __builtin_amdgcn_s_sleep(16);
  if constexpr (S == S_FLAG0 && R == 0) flag_publish(c, 0);
  if constexpr (S == S_FLAG1 && R == 0) flag_publish(c, 1);
  if constexpr ((S == S_POLL || S == S_POLL0) && R == 0) asm volatile("buffer_load_dwordx2 %0, %1, %2, 0 offen sc1" : "=v"(p.poll) : "v"(S == S_POLL ? c.vflag : c.vflag0), "s"(c.sw) : "memory");
  if constexpr (S == S_GATE) asm volatile("buffer_load_dwordx2 %0, %1, %2, %3 offen sc1" : "=v"(p.poll) : "v"((c.lane & 31) * 8), "s"(c.sw), "s"(c.og) : "memory");
  SB0();
  slots<R, S, item0_(R, S), 0, item0_(R, S + 1) - item0_(R, S)>(c, p);
}
template <int R, int S, int E> DI void steps(Ctx& c, P1& p) { if constexpr (S < E) { step<R, S>(c, p); steps<R, S + 1, E>(c, p); } }
template <int R, int A, int B, int RE, class F> DI void run(P1& p, F&& extra) {
  if constexpr (A < B) {
    mm_item<R, A - 4>(p);
    if (ORD) SB0();
    if constexpr (A < RE) rd_item<R, A ^ 1>(p);
    if (ORD) SB0();
    extra(std::integral_constant<int, A>{});
    SB0();
    run<R, A + 1, B, RE>(p, extra);
  } else { PIN2(p.c[0], p.c[1]); }
}
template <int K, class F> DI void fmq(P1& p, F&& extra) {
  if constexpr (K < 24) { fm_slice<K>(p); extra(std::integral_constant<int, K>{}); SB0(); fmq<K + 1>(p, extra); }
}
template <int R, int J> DI void rd4(P1& p) { rd_item<R, J + 1>(p); rd_item<R, J>(p); rd_item<R, J + 3>(p); rd_item<R, J + 2>(p); }

template <int R> DI void ffn_wg(Ctx& c, P1& p) {
  const u32 l = c.lane;
  const int tg2 = c.tg & 1, ks = c.tg >> 1;
  const int TG = 2 * (R == 1 ? c.kh : 1 - c.kh) + tg2;
  p.hbase = WS_H + (u32)(((c.t * 4 + TG) * 112 + 56 * c.kh + 2 * ks) * 1024);
  if (XB & 4) p.hbase = 0x40000000u;
  asm volatile("" : "+s"(p.hbase));
  SB0();
  issue_range<R, 3, PRO>(c);
  asm volatile("s_waitcnt vmcnt(%0)" :: "n"(PRO - 3) : "memory");
  repack(c);
  if constexpr (R == 0) st_dev(c, (u32x4){~0u, ~0u, ~0u, ~0u}, c.l16, c.okvp);
  p.g1 = p.u1 = p.a0 = p.a1 = p.g2 = p.u2 = p.c[0] = p.c[1] = (f32x16){};
  steps<R, 0, NS>(c, p);
  STAMP(18);
  auto none = [](auto) {};
  constexpr int B = P2_(R);
  const u32 h = l >> 5;
  const u32 obase = ((XB & 8) ? 0x40000000u : WS_O) + (u32)(((c.t * 4 + TG) * 16 + 8 * c.kh + 4 * ks) * 2048);
  const u32 l16h = c.l16 + 4096, vflag2 = c.vflag + ((l & 31) < 7 ? 8 : 0);
  auto epi2 = [&](auto J) {
    if constexpr (J.value < B + 4) gelu_ops<true, gcum(J.value - B + 23, 27), gcum(J.value - B + 24, 27)>(p.y, p.g2, p.om);
    if constexpr (J.value == B + 5 || J.value == B + 6) h_store<J.value - B - 5>(c, p.y, p.u2, 1);
  };
  if constexpr (R == 0) {
    run<R, 140, 140 + XP, 999>(p, [&](auto J) {
      epi2(J);
      if constexpr (NPRE && J.value == 139 + XP - NPRE) { PIN2(p.h1[10], p.h1[11]); PIN2(p.h1[12], p.h1[13]); }
      if constexpr (J.value >= 140 + XP - NPRE) unit_half<71 - (J.value - 140 - XP + NPRE) / 2, J.value & 1>(c);
    });
    STAMP(19);
    wait_bar_v<NPRE>();
    STAMP(20);
    flag_publish(c, 2);
    run<R, 140 + XP, 172, 999>(p, [&](auto J) { if constexpr (J.value - 140 - XP + NPRE < 8) unit_half<71 - (J.value - 140 - XP + NPRE) / 2, J.value & 1>(c); });
    STAMP(21);
    __builtin_amdgcn_s_sleep(PSLEEP);
    flag_spin(c, __builtin_amdgcn_raw_buffer_load_b64(c.sw, vflag2, 0, 16), vflag2);
    STAMP(22);
    ld_h<0, NB0>(c, p.h2, l16h, p.hbase);
    run<R, 172, 200, 999>(p, [&](auto J) {
      if constexpr (J.value - 172 + NB0 < 14) ld_h<J.value - 172 + NB0, J.value - 171 + NB0>(c, p.h2, l16h, p.hbase);
      if constexpr (J.value == 187) { asm volatile("s_barrier" ::: "memory"); STAMP(23); }
      if constexpr (J.value >= OPOS && J.value < OPOS + 8) ld_1k<J.value - OPOS, J.value - OPOS + 1>(c, p.of, obase);
    });
  } else {
    __builtin_amdgcn_s_setprio(1);
    run<R, 168, 172, 999>(p, epi2);
    fm_slices<0, 2>(p);
    SB0();
    h_store<0>(c, p.y, p.u2, 1); h_store<1>(c, p.y, p.u2, 1);
    SB0();
    STAMP(19);
    fmq<3>(p, [&](auto K) {
      if constexpr (K.value == XQ) {
        wait_bar_v<0>();

        ld_1k<0, 4>(c, p.kv, WS_KVR + (u32)c.hd * 4096);
      }
      if constexpr (K.value > XQ && K.value <= XQ + H1Q) ld_h<K.value - XQ - 1, K.value - XQ>(c, p.h1, c.l16, p.hbase);
    });
    STAMP(21);
    chk<0, 4>(p.kv, [&] { ld_1k<0, 4>(c, p.kv, WS_KVR + (u32)c.hd * 4096); });
    o_publish(c, p.kv, p.ph, p.gq);
    __builtin_amdgcn_s_setprio(0);
    STAMP(22);
    u32x2 f2;
    run<R, 172, C2, 999>(p, [&](auto J) {
      if constexpr (J.value - 172 + H1Q < 14) ld_h<J.value - 172 + H1Q, J.value - 171 + H1Q>(c, p.h1, c.l16, p.hbase);
      if constexpr (J.value == P2) f2 = __builtin_amdgcn_raw_buffer_load_b64(c.sw, vflag2, 0, 16);
    });
    flag_spin(c, f2, vflag2);
    STAMP(24);
        ld_h<0, NB1>(c, p.h2, l16h, p.hbase);
    run<R, C2, 228, 999>(p, [&](auto J) {
      if constexpr (J.value - C2 + NB1 < 14) ld_h<J.value - C2 + NB1, J.value - C2 + NB1 + 1>(c, p.h2, l16h, p.hbase);
      if constexpr (J.value == YR1) { asm volatile("s_barrier" ::: "memory"); STAMP(23); }
      if constexpr (J.value >= O1 && J.value < O1 + 8) ld_1k<J.value - O1, J.value - O1 + 1>(c, p.of, obase);
    });
  }
  STAMP(27);
  chk<0, 8>(p.of, [&] { ld_1k<0, 8>(c, p.of, obase); });
  STAMP(28);
  run<R, B + 60, B + 76, 999>(p, none);
  STAMP(29);
  u32x4 pp[2];
  auto ld_p = [&] { pp[0] = ld_dev(c, c.l16, c.op ^ (8 * 16384)); pp[1] = ld_dev(c, c.l16 + 1024, c.op ^ (8 * 16384)); };
  if constexpr (R == 1) ld_p();
  const u32 xw = R ? (c.tg ? spos(8) + c.tg * UNIT : spos(6)) : spos(2) + c.tg * UNIT;
  const u32 xo = R ? (c.tg != 2 ? spos(8) + (c.tg ^ 2) * UNIT : spos(6)) : spos(2) + (c.tg ^ 2) * UNIT;
  {
    const u32 wa = xw + (l & 31) * 256 + ((h ^ (l & 7)) << 4);
#pragma unroll
    for (int g = 0; g < 8; ++g) {
      const f32x16& v = p.c[g >> 2];
      *(LAS f32x4*)((wa ^ (u32)((g & 3) << 5)) + (g >> 2) * 128) = (f32x4){v[4 * (g & 3)], v[4 * (g & 3) + 1], v[4 * (g & 3) + 2], v[4 * (g & 3) + 3]};
    }
  }
  volatile LAS u32* fl = (volatile LAS u32*)(spos(1) + 4096);
  asm volatile("" ::: "memory");
  fl[c.w] = 1u;
  const u32 ra = (ks * 16 + (l >> 3)) * 256 + (l & 4) * 32 + (((2 * (l & 3)) ^ (l >> 3)) << 4);
  f32x4 own[4];
#pragma unroll
  for (int q = 0; q < 4; ++q) own[q] = *(LAS f32x4*)(xw + (ra ^ (u32)((q & 1) << 4)) + (q >> 1) * 2048);
  { u32 spin = 0; while (__builtin_amdgcn_readfirstlane(fl[c.w ^ 2]) == 0 && ++spin < (1u << 22)) {} }
  asm volatile("" ::: "memory");
  float r16[16];
#pragma unroll
  for (int q = 0; q < 4; ++q) {
    const f32x4 b = *(LAS f32x4*)(xo + (ra ^ (u32)((q & 1) << 4)) + (q >> 1) * 2048);
#pragma unroll
    for (int i = 0; i < 4; ++i) r16[4 * q + i] = own[q][i] + b[i];
  }
  if constexpr (R == 0) {
    st_dev(c, sanit(pack8<0>(r16)), c.l16, c.op);
    st_dev(c, sanit(pack8<1>(r16)), c.l16 + 1024, c.op);
    STAMP(30);
  } else {
#pragma unroll
    for (int i = 0; i < 16; ++i) PIN1(r16[i]);
    chk<0, 2>(pp, ld_p);
    STAMP(30);
#pragma unroll
    for (int d = 0; d < 4; ++d) {
      r16[2 * d] += blo(pp[0][d]); r16[2 * d + 1] += bhi(pp[0][d]);
      r16[8 + 2 * d] += blo(pp[1][d]); r16[9 + 2 * d] += bhi(pp[1][d]);
    }
    const u32 vo = (l >> 3) * 896 + (l & 7) * 16, oo = (c.t * 128 + c.kh * 64 + tg2 * 32 + ks * 16) * 896 + c.jj * 128;
#pragma unroll
    for (int s = 0; s < 2; ++s) {
      u32x4 o = s ? pack8<1>(r16) : pack8<0>(r16);
      __builtin_amdgcn_raw_buffer_store_b128(o, mk(c.out, 2048 * 896), vo, oo + s * (8 * 896), OUTAUX);
      asm volatile("s_nop 1" :: "v"(o) : "memory");
    }
    if (__builtin_amdgcn_workgroup_id_x() == 0 && c.w == 4) {
      u32x2 v = {(u32)c.tag, (u32)(c.tag >> 32)};
      __builtin_amdgcn_raw_buffer_store_b64(v, c.sw, l == 0 ? 0u : OOB, 0, 16);
    }
    STAMP(31);
  }
}

template <int R, int A0, int A, int B, int ST> DI void pipe(P1& p) {
  if constexpr (A - 3 * ST < B) {
    if constexpr (A - 3 * ST >= A0) mm_item<R, A - 3 * ST>(p);
    if constexpr (A < B) rd_item<R, A>(p);
    SB0();
    pipe<R, A0, A + ST, B, ST>(p);
  }
}
constexpr u32 sxpos(int ch) { return spos(2 * ch - 2); }
template <int N, int E> DI void spare_x(const Ctx& c) {
  if constexpr (N < E) { set_m0<sxpos(N >> 1) + (N & 1) * UNIT>(c); asm volatile("s_nop 0"); dma<0>((N & 1) ? c.vx2 : c.vx, c.sx, c.rot[N >> 1] * 128); spare_x<N + 1, E>(c); }
}
template <int R> DI void spare_wg(Ctx& c, P1& p) {
  const u32 l = c.lane;
  set_m0<spos(12)>(c); asm volatile("s_nop 0"); dma<0>(c.l16, mk(c.gw, 896), c.w == 0 ? 0u : 0x40000000u);
  const u32 okvr = WS_KVR + (u32)(c.t * 4096 + (2 * c.kh + (c.w & 1)) * 1024);
  const u32x4 ones = {~0u, ~0u, ~0u, ~0u};
  st_dev(c, ones, R == 0 || c.tg < 2 ? c.l16 : OOB, R == 0 ? c.okvp : okvr);
  spare_x<2, 4>(c);
  asm volatile("s_waitcnt vmcnt(4)" ::: "memory");
  repack(c);
  spare_x<4, 14>(c);
  wait_bar<0>();
  if constexpr (R == 0) flag_publish(c, 0);
  if (!OL)
  {
    const u32 mo = WS_O + ((c.t * 4 + c.tg) * 16 + 8 * c.kh + 4 * R) * 2048;
#pragma unroll
    for (int i = 0; i < 8; ++i) st_dev(c, ones, c.l16 + (i & 3) * 1024, mo + (i >> 2) * 4096);
  }
#pragma unroll
  for (int s = 0; s < 28; ++s) p.xf[s] = as_frag(lds_rd128((p.ax ^ (u32)(32 * (s & 3))) + sxpos(s >> 2)));
  STAMP(1);
  f32x16 ga = {};
#pragma unroll
  for (int s = 14 * R; s < 14 * R + 14; ++s) ga = mfma(as_frag(lds_rd128(spos(12) + c.rot[s >> 2] * 128 + (s & 3) * 32 + (l >> 5) * 16)), p.xf[s], ga);
  PIN1(ga);
  float g1 = 0.f;
  if constexpr (R == 0) *(LAS float*)(spos(17) + c.tg * 256 + l * 4) = ga[0];
  u32x2 f0 = __builtin_amdgcn_raw_buffer_load_b64(c.sw, c.vflag0, 0, 16);
  bar_l();
  if constexpr (R == 1) {
    g1 = __builtin_amdgcn_rcpf(1.0f + __builtin_amdgcn_exp2f(-LOG2E * (ga[0] + *(LAS float*)(spos(17) + c.tg * 256 + l * 4))));
    __builtin_amdgcn_raw_buffer_store_b64((u32x2){f2u(g1), (u32)c.tag}, c.sw, l < 32 ? l * 8 : OOB, c.og, 16);
  }
  flag_spin(c, f0, c.vflag0);
  issue_range<R, 29, 36>(c); issue_range<R, 28, 29>(c);
  issue_range<R, 39, 40>(c); issue_range<R, 42, 43>(c);
  p.a0 = p.a1 = (f32x16){};
  wait_bar<6>();
  STAMP(2);
  issue_range<R, 36, 37>(c);
  if (OL)
  {
    const u32 mo = WS_O + ((c.t * 4 + c.tg) * 16 + 8 * c.kh + 4 * R) * 2048;
#pragma unroll
    for (int i = 0; i < 8; ++i) st_dev(c, ones, c.l16 + (i & 3) * 1024, mo + (i >> 2) * 4096);
  }
  pipe<R, 44, 44, 60, 1>(p);
  wait_bar_v<0>();
  if constexpr (R == 0) flag_publish(c, 1);
  issue_range<R, 45, 46>(c);
  wphi_rd(c, p);
  pipe<R, 60, 60, 72, 1>(p);
  wait_bar<0>();
  if constexpr (R == 0) { fm_slices<0, 24>(p); phi_image<0>(c, p); phi_image<1>(c, p); }
  else { v_handoff<spos(1), 0>(c, p.a1); v_handoff<spos(1), 1>(c, p.a1); pipe<R, 85, 85, 140, 2>(p); PIN1(p.a0); }
  bar_l();
  if constexpr (R == 0) {
    if (c.w < 2) {
      kv_stage<spos(1), 0>(c, p); kv_stage<spos(1), 1>(c, p); kv_stage<spos(1), 2>(c, p); kv_stage<spos(1), 3>(c, p);
      kv_stage<spos(1), 4>(c, p); kv_stage<spos(1), 5>(c, p); kv_stage<spos(1), 7>(c, p);
    }
    STAMP(3);
    flag_spin(c, __builtin_amdgcn_raw_buffer_load_b64(c.sw, c.vflag, 0, 16), c.vflag);
    STAMP(4);
    u32x4 v[8];
    const u32 ro = WS_KVP + (u32)(c.t * 65536 + (2 * c.kh + (c.w & 1)) * 1024), vr = c.l16 + (c.w >> 1) * 8;
    auto ld = [&] {
#pragma unroll
      for (int i = 0; i < 8; ++i) {
        const u32x2 a = __builtin_amdgcn_raw_buffer_load_b64(c.sw, vr, ro + i * 8192, 16), b = __builtin_amdgcn_raw_buffer_load_b64(c.sw, vr, ro + i * 8192 + 4096, 16);
        v[i] = (u32x4){a[0], a[1], b[0], b[1]};
      }
    };
    ld();
    chk<0, 8>(v, ld);
    STAMP(5);
    float s[8];
    set8(s, v[0]);
#pragma unroll
    for (int i = 1; i < 8; ++i) add8(s, v[i]);
    const u32x4 o = sanit((u32x4){pk(s[0] + s[4], s[1] + s[5]), pk(s[2] + s[6], s[3] + s[7]), 0u, 0u});
    __builtin_amdgcn_raw_buffer_store_b64((u32x2){o[0], o[1]}, c.sw, vr, okvr, 16);
    STAMP(6);
  } else {
    fm_slices<0, 24>(p);
    flag_spin(c, __builtin_amdgcn_raw_buffer_load_b64(c.sw, c.vflag, 0, 16), c.vflag);
    u32x4 v[4];
    const u32 kvo = WS_KVR + (u32)c.hd * 4096;
    ld_1k<0, 4>(c, v, kvo);
    chk<0, 4>(v, [&] { __builtin_amdgcn_s_sleep(8); ld_1k<0, 4>(c, v, kvo); });
    o_publish(c, v, p.ph, g1);
    STAMP(7);
  }
}

extern "C" __global__ __launch_bounds__(512, 2) void fused_kernel(const char* x, const char* wqkv, const char* wphi, const char* wo, const char* wg,
                                                                   const char* wu, const char* wd, const char* gw, char* out, char* ws) {
  Ctx c;
  const int bid = __builtin_amdgcn_workgroup_id_x(), xcd = bid & 7, idx = bid >> 3;
  const u32 tid = __builtin_amdgcn_workitem_id_x();
  c.gw = gw; c.wd = wd; c.wo = wo; c.out = out; c.ws = ws;
  c.t = 4 * (xcd >> 1) + (idx & 3); c.kh = xcd & 1; c.jj = idx >> 2; c.hd = 8 * c.kh + c.jj; c.j = 7 * c.kh + c.jj;
  c.w = __builtin_amdgcn_readfirstlane(tid >> 6); c.tg = c.w & 3; c.r = c.w >> 2;
  c.lane = tid & 63; c.l16 = c.lane * 16; c.m0w = c.w * 1024;
  const u32 l = c.lane, w = c.w;
  STAMP(0);
  const u64 rt = 0x2106543210654321ull, r0 = RJ * c.jj + RT * (idx & 3) + RX * (xcd >> 1) + RK * c.kh;
  const u32 rs = (u32)(rt >> (4 * (r0 > 9 ? r0 - 7 : r0)));
#pragma unroll
  for (int i = 0; i < 7; ++i) c.rot[i] = (rs >> (4 * i)) & 7;
  const u32 row8 = 8 * w + (l >> 3), sw2 = ((l & 7) ^ (((l >> 4) & 1) << 2)) << 4;
  c.vx = row8 * 896 + (((l & 7) ^ ((4 * w + (l >> 4)) & 7)) << 4); c.vx2 = c.vx + 64 * 896;
  c.vw = row8 * 3584 + sw2;
  asm volatile("" : "+v"(c.vx), "+v"(c.vx2), "+v"(c.vw));
  asm volatile("" : "+s"(c.rot[0]), "+s"(c.m0w));
  SB0();
  const u64 epoch = *(const __attribute__((address_space(4))) u64*)ws;
  c.sx = mk(x + c.t * 114688, 114688);
  c.sg = mk(wg + c.j * 256, 448 * 3584 - c.j * 256);
  if (c.jj < 7) issue_range<0, 0, 3>(c);
  else {
    u32 lo; u64 pc;
    asm volatile("s_getpc_b64 %0\n.Lpf_here:\n\ts_mov_b32 %1, .Lpf_here-fused_kernel" : "=s"(pc), "=s"(lo));
    asm volatile("s_mov_b32 m0, %0\n\ts_nop 0\n\tbuffer_load_dword %1, %2, 0 offen lds"
                 :: "i"(spos(13)), "v"((w * 64 + l) * 128), "s"(mk((const char*)(pc - lo), CODE_BYTES)) : "memory");
    spare_x<0, 2>(c);
  }
  {
    const u32 win = 56 * c.hd - 8 * (c.hd & 1), F = 6 * c.t + w, part = (F * 147) >> 12;
    const bool ok = F < 86 && w < 6;
    set_m0<spos(17)>(c); asm volatile("s_nop 0");
    if (c.t == 14) dma<0>((c.l16 + 896 * w - 224 * (c.hd & 1)) & 0x7fffffffu, mk(wphi + c.hd * 1568, 1568), 0);
    else dma<0>((l >> 2) * 2688 + (l & 3) * 16, mk(wqkv, 448 * 2688), ok ? (F - 28 * part) * (16 * 2688) + part * 896 + win : 0x40000000u);
    c.vrp = (ok ? WS_WA + c.hd * WA_HEAD + F * 1024 : OOB) + c.l16;
  }
  SB0();
  c.tag = epoch + 1;
  c.v2 = row8 * 896 + sw2; c.v2b = c.v2 + 64 * 896;
  c.vq = WS_WA + c.hd * WA_HEAD + c.tg * 1024 + c.l16; c.vkv = c.vq + 28672 * (1 + c.r);
  c.vflag0 = WS_FLAG + (((l & 15) * 16 + c.hd) << 7);
  c.sw = mk(ws, WS_END);
  c.oh = WS_H + (u32)(((c.t * 4 + c.tg) * 112 + 8 * c.j + 2 * c.r) * 1024);
  c.oo = WS_O + (u32)(((c.t * 4 + c.tg) * 16 + c.hd) * 2048);
  c.op = WS_P + (u32)((c.t * 16 + c.hd) * 16384 + c.tg * 2048);
  c.okvp = WS_KVP + (u32)((c.hd * 16 + c.t) * 4096 + c.tg * 1024);
  c.oflag = WS_FLAG + (u32)(c.t * 16 + c.hd) * 128;
  c.og = WS_G + (u32)(c.kh * 2048 + c.t * 128 + c.tg * 32) * 8;
  if (XB & 1) c.op = 0x40000000u;
  if (XB & 2) c.oh = 0x40000000u;
  if (XB & 8) c.oo = 0x40000000u;
  static_assert(WS_P % (16 * 16384) == 0, "the partner's slot is op ^ (8 * 16384)");
  c.vflag = flag_off(c, c.jj == 7); c.vone = tid == 0 ? 0u : OOB;
  asm volatile("" : "+s"(c.tag), "+s"(c.oh), "+s"(c.oo), "+s"(c.op), "+s"(c.okvp), "+s"(c.oflag), "+s"(c.og) :: "memory");
  asm volatile("" : "+v"(c.vflag), "+v"(c.vflag0), "+v"(c.vone), "+v"(c.vrp));
  P1 p;
  {
    const u32 q = (l & 15) >> 2, pp = l & 3, blk = (l >> 4) & 1, h = l >> 5, r = l & 31;
    p.aw = mkseg((8 * h + q) * 128 + ((64 * c.r) ^ ((q >> 1) << 6)) + 32 * blk + 8 * pp);
    p.aq = mkseg(c.l16);
    p.ax = (32 * c.tg + r) * 128 + ((h ^ ((r >> 1) & 7)) << 4);
  }
  if (c.jj < 7) {
    c.su = mk(wu + c.j * 256, (XB & 32) ? 0 : 448 * 3584 - c.j * 256);
    c.sd = mk(wd + c.kh * 896 * 896 + c.jj * 128, (XB & 16) ? 0 : 896 * 896 - c.jj * 128);
    c.so = mk(wo + c.kh * 224 * 896 + c.jj * 128, (XB & 16) ? 0 : 224 * 896 - c.jj * 128);
    if (c.r == 0) { ffn_wg<0>(c, p); STAMP_END(); __builtin_amdgcn_endpgm(); }
    ffn_wg<1>(c, p);
    STAMP_END();
    __builtin_amdgcn_endpgm();
  }
  if (c.r == 0) { spare_wg<0>(c, p); STAMP_END(); __builtin_amdgcn_endpgm(); }
  spare_wg<1>(c, p);
  STAMP_END();
}

#if defined(__HIP_DEVICE_COMPILE__)
#pragma clang attribute pop
#endif

extern "C" void kernel_launch(void* const* d_in, const int* in_sizes, int n_in, void* d_out, int out_size, void* d_ws, size_t ws_size,
                              hipStream_t stream) {
  (void)hipFuncSetAttribute((const void*)fused_kernel, hipFuncAttributeMaxDynamicSharedMemorySize, LDS_BYTES);
  fused_kernel<<<256, 512, LDS_BYTES, stream>>>((const char*)d_in[0], (const char*)d_in[1], (const char*)d_in[2], (const char*)d_in[3],
                                                (const char*)d_in[5], (const char*)d_in[6], (const char*)d_in[7], (const char*)d_in[8],
                                                (char*)d_out, (char*)d_ws);
}
```

```cpp
#define STAMPS 2
#define SMASK (1u<<15)
#define PROBE_K 15
#include <hip/hip_runtime.h>
#include <hip/hip_bf16.h>
#include <type_traits>
#pragma clang diagnostic ignored "-Wint-to-pointer-cast"

typedef unsigned u32;
typedef unsigned long long u64;
typedef u32 u32x4 __attribute__((ext_vector_type(4)));
typedef u32 u32x2 __attribute__((ext_vector_type(2)));
typedef short s16x4 __attribute__((ext_vector_type(4)));
typedef __bf16 bf16x8 __attribute__((ext_vector_type(8)));
typedef __bf16 bf16x2 __attribute__((ext_vector_type(2)));
typedef float f32x16 __attribute__((ext_vector_type(16)));
typedef float f32x4 __attribute__((ext_vector_type(4)));
typedef float f32x2 __attribute__((ext_vector_type(2)));
typedef __amdgpu_buffer_rsrc_t rsrc_t;
#define DI __device__ __forceinline__
#define LAS __attribute__((address_space(3)))
#define SB0() __builtin_amdgcn_sched_barrier(0)
#define PIN1(a) asm volatile("" : "+v"(a))
#define PIN2(a, b) asm volatile("" : "+v"(a), "+v"(b))

constexpr u32 WS_FLAG = 4096;
constexpr u32 WS_G    = 131072;
constexpr u32 WS_KVR  = 65536;
constexpr u32 WS_KVP  = 262144;
constexpr u32 WS_O    = WS_KVP + 256 * 4096;
constexpr u32 WS_P    = WS_O + 64 * 32 * 1024;
constexpr u32 WS_H    = WS_P + 256 * 16384;
constexpr u32 WS_WA   = WS_H + 64 * 112 * 1024;
constexpr u32 WA_HEAD = 86 * 1024;
constexpr u32 WS_END  = WS_WA + 16 * WA_HEAD;
constexpr u32 WS_STAMP = 32u << 20;

constexpr int UNIT = 8192, RING = 18;
constexpr u32 SCR = 0, RBASE = 2 * UNIT;
constexpr int LDS_BYTES = 163840;
constexpr u32 OOB = 0x80000000u;
constexpr u32 CODE_BYTES = 38912;
#define ABL 0
#define ORD 1
#define A4 9
#define SWP 1
#define DELAYS 99
#define NOGELU 0
#define NOFM 0
#define I7 33
#define I8 36
#define I9 38
#define I10 41
#define I11 44
#define I12 47
#define I18 62
#define NOMM 0
#define RJ 1
#define RT 2
#define RX 1
#define RK 0
#define XB 0
#define PH 7
#define OL 1
#define OUTAUX 0
#define NB0 10
#define C2 196
#define P2 186
#define NPRE 0
#define NB1 2
#define YR1 199
#define O1 204
#define POL 2
#define DPOL "sc1"
#define LAUX 16
#define SAUX 16
#define GN 24
#define HS 4
#define PR 1
#define FRONT 0
#define BACK 0
#define MK 3
#define RSL 0
#define RS0 6
#define QSL 16
#define W1 1
#define QS0 20
#define PROV 9
#define POLL0 5
#define POLL1 17
#define GATE 11
#define PFS 0
#define TOUCH1 0
#define TOUCH2 0
#define XP 24
#define XQ 14
#define H1Q 6
#define H1R 10
#define PSLEEP 0
#define OPOS 184
#define YP 10
#define CHK1 19
#define FLAG0 2
#define I19 65
#define I20 68
constexpr u32 SPIN_MAX = ABL ? 0 : 1u << 16;
constexpr float LOG2E = 1.4426950408889634f;

#if defined(__HIP_DEVICE_COMPILE__)
#pragma clang attribute push(__attribute__((target("no-packed-fp32-ops"))), apply_to = function)
#endif
constexpr u32 spos(int k) { return RBASE + k * UNIT; }
constexpr int w2c_(int i) { constexpr int t[18] = {16, 17, 0, 1, 2, 3, 4, 5, 6, 7, 8, 9, 10, 11, 12, 13, 14, 15}; return t[i]; }
constexpr int w2s_(int c) { constexpr int t[18] = {2, 3, 4, 5, 6, 10, 11, 12, 13, 15, 14, 16, 7, 8, -2, -1, 0, 1}; return t[c]; }
constexpr int uslot(int n) {
  constexpr int t[26] = {-1, 10, 11, 12, 13, 14, 15, 16, 17, 0, 1, 2, 3, 4, 5, 6, 10, 11, 12, 13, 14, 16, 7, 8, 9, 17};
  return n < 28 ? n % RING : n < 54 ? t[n - 28] : w2s_(w2c_(n - 54));
}
constexpr u32 upos(int n) { return spos(uslot(n)); }
constexpr u32 VBUF = RBASE + 15 * UNIT;
constexpr int imin(int a, int b) { return a < b ? a : b; }

struct Ctx {
  const char *gw, *wd, *wo;
  char *out, *ws;
  int t, kh, jj, hd, j, w, tg, r;
  u32 lane, l16, m0w;
  u32 vx, vx2, vw, vq, vkv, v2, v2b;
  rsrc_t sx, sg, su, sq, sp, sd, so, sw;
  u64 tag;
  u32 vst, vsc;
  u32 rot[7];
  u32 oh, oo, op, okvp;
  u32 oflag, og;
  u32 vrp, vflag0;
  u32 vflag, vone;
};

DI rsrc_t mk(const void* p, u32 bytes) { return __builtin_amdgcn_make_buffer_rsrc((void*)p, 0, bytes, 0x00020000); }
template <u32 L> DI void set_m0(const Ctx& c) { if (!(ABL & 1)) asm volatile("s_add_u32 m0, %0, %1" :: "s"(c.m0w), "i"(L) : "memory", "scc"); }
DI void dma_dev(u32 voff, rsrc_t rs, u32 soff) {
  if (!(ABL & 1)) asm volatile("buffer_load_dwordx4 %0, %1, %2 offen sc1 lds" :: "v"(voff), "s"(rs), "s"(soff) : "memory");
}
template <u32 OFF> DI void dma(u32 voff, rsrc_t rs, u32 soff) {
  if (!(ABL & 1)) asm volatile("buffer_load_dwordx4 %0, %1, %2 offen offset:%3 " DPOL " lds" :: "v"(voff), "s"(rs), "s"(soff), "i"(OFF) : "memory");
}
template <int N> DI void wait_bar() { asm volatile("s_waitcnt vmcnt(%0) lgkmcnt(0)\n\ts_barrier" :: "n"(N) : "memory"); }
template <int N> DI void wait_bar_v() { asm volatile("s_waitcnt vmcnt(%0)\n\ts_barrier" :: "n"(N) : "memory"); }
DI void bar_l() { asm volatile("s_waitcnt lgkmcnt(0)\n\ts_barrier" ::: "memory"); }

constexpr u32 ioff_(int n) { return n >= 36 && n < 54 && !(n < 47 && n % 3 == 0) ? 128 : 0; }
template <int N> DI void unit_m0(const Ctx& c) { set_m0<upos(N) - ioff_(N)>(c); }
template <int R, int N> DI void unit_go(const Ctx& c) {
  if constexpr (N < 21) {
    if constexpr (N % 3 < 2) dma<0>(N % 3 ? c.vx2 : c.vx, c.sx, c.rot[N / 3] * 128);
    else dma<0>(c.vw, c.sg, c.rot[N / 3] * (64 * 3584));
  } else if constexpr (N >= 29 && N < 36) dma_dev(c.vkv, c.sw, c.rot[N - 29] * 4096);
  else if constexpr (N == 28) dma_dev(c.vq, c.sw, c.w < 2 ? 84u * 1024u : 0x40000000u);
  else if constexpr (N < 28) dma<0>(c.vw, c.su, c.rot[N - 21] * (64 * 3584));
  else if constexpr (N < 47) {
    if constexpr (N % 3 == 0) dma_dev(c.vq, c.sw, N == 45 && R == 1 ? 0x40000000u : c.rot[(2 * (N - 36) / 3 + R) % 7] * 4096);
    else dma<128>(c.vw, c.sg, c.rot[2 * ((N - 36) / 3) + N % 3 - 1] * (64 * 3584));
  } else if constexpr (N < 54) dma<128>(c.vw, c.su, c.rot[N - 47] * (64 * 3584));
  else {
    constexpr int k = w2c_(N - 54);
    if constexpr (k < 14) dma<0>(k < 7 ? c.v2 : c.v2b, c.sd, c.rot[k % 7] * (128 * 896));
    else dma<0>((k & 1) ? c.v2b : c.v2, c.so, k < 16 ? 0u : 128u * 896u);
  }
}
template <int N, int H> DI void unit_half(const Ctx& c) {
  set_m0<upos(N) + H * 4096>(c);
  constexpr int k = w2c_(N - 54);
  const u32 so = (k < 14 ? c.rot[k % 7] * (128 * 896) : k < 16 ? 0u : 128u * 896u) + H * (32 * 896);
  asm volatile("s_nop 0");
  dma<0>((k < 14 ? k >= 7 : (k & 1)) ? c.v2b : c.v2, k < 14 ? c.sd : c.so, so);
}
template <int R, int A, int B> DI void issue_range(const Ctx& c) {
  if constexpr (A < B) { unit_m0<A>(c); asm volatile("s_nop 0"); unit_go<R, A>(c); issue_range<R, A + 1, B>(c); }
}

struct Seg { u32 b[3]; };
DI Seg mkseg(u32 a) { Seg s; s.b[0] = a; s.b[1] = a + 65536; s.b[2] = a + 131072; asm volatile("" : "+v"(s.b[0]), "+v"(s.b[1]), "+v"(s.b[2])); return s; }
DI u32 sa(const Seg& s, u32 off) { return s.b[off >> 16] + (off & 0xffffu); }
DI f32x16 mfma(bf16x8 a, bf16x8 b, f32x16 c) { if (NOMM) { asm volatile("" : "+v"(c) : "v"(a), "v"(b)); return c; } return __builtin_amdgcn_mfma_f32_32x32x16_bf16(a, b, c, 0, 0, 0); }
DI bf16x8 as_frag(u32x4 v) { return __builtin_bit_cast(bf16x8, v); }
DI bf16x8 tr_frag(u32 a0, u32 a1) {
  if (NOMM > 1) { bf16x8 z; asm volatile("" : "=v"(z)); return z; }
  s16x4 lo = __builtin_amdgcn_ds_read_tr16_b64_v4i16((LAS s16x4*)a0);
  s16x4 hi = __builtin_amdgcn_ds_read_tr16_b64_v4i16((LAS s16x4*)a1);
  return __builtin_bit_cast(bf16x8, __builtin_shufflevector(lo, hi, 0, 1, 2, 3, 4, 5, 6, 7));
}
DI u32x4 lds_rd128(u32 a) { return *(LAS u32x4*)a; }
DI void lds_wr128(u32 a, u32x4 v) { *(LAS u32x4*)a = v; }
DI u32 f2u(float f) { return __builtin_bit_cast(u32, f); }
DI float u2f(u32 u) { return __builtin_bit_cast(float, u); }
DI u32 pk(float a, float b) { f32x2 t = {a, b}; return __builtin_bit_cast(u32, __builtin_convertvector(t, bf16x2)); }
template <int S, class V> DI u32x4 pack8(const V& v) {
  u32x4 r = {pk(v[8 * S], v[8 * S + 1]), pk(v[8 * S + 2], v[8 * S + 3]), pk(v[8 * S + 4], v[8 * S + 5]), pk(v[8 * S + 6], v[8 * S + 7])};
  return r;
}
DI float blo(u32 d) { return u2f(d << 16); }
DI float bhi(u32 d) { return u2f(d & 0xffff0000u); }
DI u32 umax(u32 a, u32 b) { return a > b ? a : b; }
DI bool any_(bool p) { return __builtin_amdgcn_ballot_w64(p) != 0; }
DI u32x4 sanit(u32x4 v) { return __builtin_elementwise_min(v, (u32x4){0xfffffffeu, 0xfffffffeu, 0xfffffffeu, 0xfffffffeu}); }
DI void st_dev(const Ctx& c, u32x4 v, u32 voff, u32 soff) {
  if (ABL & 8) { asm volatile("" :: "v"(v)); return; }
  __builtin_amdgcn_raw_buffer_store_b128(v, c.sw, voff, soff, SAUX);
  asm volatile("s_nop 1" :: "v"(v) : "memory");
}
DI u32x4 ld_dev(const Ctx& c, u32 voff, u32 soff) {
  if (ABL & 16) { u32x4 z = {voff, soff, voff, soff}; asm volatile("" : "+v"(z)); return z; }
  return __builtin_amdgcn_raw_buffer_load_b128(c.sw, voff, soff, LAUX);
}
DI f32x2 xhalf(float v) { auto p = __builtin_amdgcn_permlane32_swap(f2u(v), f2u(v), false, false); f32x2 r = {u2f(p[0]), u2f(p[1])}; return r; }

DI void stamp(Ctx& c, int i) {
  SB0();
  u64 tm, tc; asm volatile("s_memrealtime %0\n\ts_memtime %1\n\ts_waitcnt lgkmcnt(0)" : "=&s"(tm), "=&s"(tc) :: "memory");
  c.vst = c.lane == (u32)i ? (u32)tm : c.vst; c.vsc = c.lane == (u32)i ? (u32)tc : c.vsc; PIN2(c.vst, c.vsc);
  SB0();
}
DI void stamp_end(Ctx& c) {
  u32x2 v = {c.vst, c.vsc};
  __builtin_amdgcn_raw_buffer_store_b64(v, mk(c.ws + WS_STAMP, 1 << 20), c.lane < 32 ? c.lane * 8 : OOB, (__builtin_amdgcn_workgroup_id_x() * 8 + c.w) * 256, 0);
}
#define STAMP(i) do { if ((STAMPS == 2 && ((SMASK >> (i)) & 1)) || (i) == 0 || (i) == 31) stamp(c, i); } while (0)
#define STAMP_END() stamp_end(c)

struct P1 {
  Seg aw, aq; u32 ax, ka, kb, kc;
  bf16x8 xf[28], f[8], af[8], vf[8];
  f32x16 g1, u1, a0, a1, g2, u2, fp, k0, c[2];
  Seg s2[2]; u32 ob[2];
  u32x4 h2[14], of[8];
  u32x4 wp[2], ph[4], h1[14], kv[4];
  float y[16], e[14], sum, mxl, mnl;
  u32x2 poll;
  float gq, om;
  u32 hbase;
};

DI void wphi_rd(const Ctx& c, P1& p) { p.wp[0] = lds_rd128(spos(-1) + c.l16); p.wp[1] = lds_rd128(spos(-1) + 1024 + c.l16); }
DI void repack(const Ctx& c) {
  const u32 l = c.lane;
  const u32 a = spos(17) + c.m0w + (8 * (l >> 5) + ((l & 15) >> 2)) * 64 + 32 * ((l >> 4) & 1) + 8 * (l & 3);
  u32x4 v = __builtin_bit_cast(u32x4, tr_frag(a, a + 256));
  if (c.t == 14) {
    const u32 rho = l & 31, f = rho < 26 ? rho : ((rho == 28 || rho == 29) ? rho - 2 : 0);
    const u32 o = spos(17) + c.m0w + 2 * (112 * (l >> 5) + f);
#pragma unroll
    for (int d = 0; d < 4; ++d) {
      const u32 od = o + 56 * (8 * (d >> 1) + 2 * (d & 1));
      v[d] = (u32)*(LAS unsigned short*)od | ((u32)*(LAS unsigned short*)(od + 56) << 16);
    }
  }
  st_dev(c, v, c.vrp, 0);
}
template <int K> DI void fm_slice(P1& p) {
  if constexpr (NOFM) { if constexpr (K == 0) { p.ph[0] = p.ph[2] = pack8<0>(p.a0); p.ph[1] = p.ph[3] = pack8<1>(p.a0); PIN2(p.ph[0], p.ph[1]); PIN2(p.ph[2], p.ph[3]); } }
  else if constexpr (K == 0) { p.fp = mfma(as_frag(p.wp[0]), as_frag(pack8<0>(p.a0)), (f32x16){}); }
  else if constexpr (K == 1) { p.fp = mfma(as_frag(p.wp[1]), as_frag(pack8<1>(p.a0)), p.fp); }
  else if constexpr (K == 2) { }
  else if constexpr (K == 3) {
    PIN1(p.fp);
    float mx = p.fp[0];
#pragma unroll
    for (int i = 1; i < 14; ++i) mx = __builtin_fmaxf(mx, p.fp[i]);
    p.mxl = mx; PIN1(p.mxl);
  } else if constexpr (K == 4) {
    float mn = p.fp[0];
#pragma unroll
    for (int i = 1; i < 14; ++i) mn = __builtin_fminf(mn, p.fp[i]);
    p.mnl = mn; PIN1(p.mnl);
  } else if constexpr (K == 5) {
    f32x2 t = xhalf(p.mxl); p.mxl = __builtin_fmaxf(t[0], t[1]) * LOG2E;
    t = xhalf(p.mnl); p.mnl = __builtin_fminf(t[0], t[1]) * LOG2E;
    PIN2(p.mxl, p.mnl);
  } else {
    constexpr int NEG = K >= 15, k = K - 6 - 9 * NEG;
    if constexpr (k < 5) {
#pragma unroll
      for (int i = 3 * k; i < imin(14, 3 * k + 3); ++i) {
        p.e[i] = __builtin_amdgcn_exp2f(NEG ? __builtin_fmaf(p.fp[i], -LOG2E, p.mnl) : __builtin_fmaf(p.fp[i], LOG2E, -p.mxl));
        PIN1(p.e[i]);
      }
      if constexpr (k == 1) p.sum = p.e[0] + p.e[1] + p.e[2];
      if constexpr (k > 1) p.sum += p.e[3 * k - 3] + p.e[3 * k - 2] + p.e[3 * k - 1];
      if constexpr (k > 0) PIN1(p.sum);
    } else if constexpr (k == 5) {
      f32x2 t = xhalf(p.sum + p.e[12] + p.e[13]); p.sum = __builtin_amdgcn_rcpf(t[0] + t[1]); PIN1(p.sum);
    } else if constexpr (k < 8) {
#pragma unroll
      for (int i = 7 * (k - 6); i < 7 * (k - 5); ++i) { p.e[i] *= p.sum; PIN1(p.e[i]); }
    } else {
      u32x4 o1 = {pk(p.e[8], p.e[9]), pk(p.e[10], p.e[11]), pk(p.e[12], p.e[13]), 0u};
      p.ph[2 * NEG] = pack8<0>(p.e); p.ph[2 * NEG + 1] = o1; PIN2(p.ph[2 * NEG], p.ph[2 * NEG + 1]);
    }
  }
}
template <int A, int B> DI void fm_slices(P1& p) { if constexpr (A < B) { fm_slice<A>(p); fm_slices<A + 1, B>(p); } }
template <u32 VB, int S> DI void v_handoff(const Ctx& c, const f32x16& av) {
  u32x4 vf = pack8<S>(av);
  const bool one = (c.lane & 31) == ((c.hd & 1) ? 0u : 28u);
#pragma unroll
  for (int d = 0; d < 4; ++d) vf[d] = one ? 0x3f803f80u : vf[d];
  lds_wr128(VB + c.tg * 2048 + c.l16 + S * 1024, vf);
}
template <int T> DI void phi_image(const Ctx& c, P1& p) {
  const u32 tok = c.lane & 31, h = c.lane >> 5;
  const u32 ft = (((tok >> 1) & 1) << 3) | (tok & 1) | (((tok >> 2) & 3) << 1);
  const u32 wa = SCR + c.tg * 4096 + tok * 128 + ((h ^ ft) << 3);
#pragma unroll
  for (int g = 0; g < 4; ++g) {
    u32x2 v = {p.ph[2 * T + (g >> 1)][2 * (g & 1)], p.ph[2 * T + (g >> 1)][2 * (g & 1) + 1]};
    *(LAS u32x2*)(wa ^ (u32)((8 * T + 2 * g) << 3)) = v;
  }
}
template <u32 VB, int K> DI void kv_stage(const Ctx& c, P1& p) {
  if constexpr (K >= 2 && K <= 5) {
    p.k0 = mfma(p.af[2 * K - 4], p.vf[2 * K - 4], K == 2 ? (f32x16){} : p.k0);
    p.k0 = mfma(p.af[2 * K - 3], p.vf[2 * K - 3], p.k0);
  }
  if constexpr (K == 0) {
    const u32 l = c.lane, h = l >> 5, q = (l & 15) >> 2, pp = l & 3, blk = (l >> 4) & 1;
    p.ka = SCR + (4 * h + q) * 128 + (((4 * blk + pp) ^ (q & 1) ^ (h << 1) ^ ((q >> 1) << 3) ^ ((c.tg & 1) << 3)) << 3);
    p.kb = p.ka ^ 32u; p.kc = VB + c.l16;
    asm volatile("" : "+v"(p.ka), "+v"(p.kb), "+v"(p.kc));
  }
  if constexpr (K < 4) {
#pragma unroll
    for (int s = 0; s < 2; ++s) {
      p.af[2 * K + s] = tr_frag(p.ka + K * 4096 + s * 2048, p.kb + K * 4096 + s * 2048 + 1024);
      p.vf[2 * K + s] = as_frag(lds_rd128(p.kc + K * 2048 + s * 1024));
    }
  }
  if constexpr (K == 7) {
    const u32 vo = c.tg < 2 ? c.l16 : OOB;
    st_dev(c, sanit(pack8<0>(p.k0)), vo, c.okvp + c.tg * 1024);
    st_dev(c, sanit(pack8<1>(p.k0)), vo + 1024, c.okvp + c.tg * 1024);
  }
}
DI void add8(float (&s)[8], u32x4 v) {
#pragma unroll
  for (int d = 0; d < 4; ++d) { s[2 * d] += blo(v[d]); s[2 * d + 1] += bhi(v[d]); }
}
DI void set8(float (&s)[8], u32x4 v) {
#pragma unroll
  for (int d = 0; d < 4; ++d) { s[2 * d] = blo(v[d]); s[2 * d + 1] = bhi(v[d]); }
}
template <int A, int B, int N> DI bool has_mark(const u32x4 (&v)[N]) {
  u32 m = 0;
#pragma unroll
  for (int i = A; i < B; ++i) m = umax(umax(m, v[i][0]), umax(v[i][1], umax(v[i][2], v[i][3])));
  return any_(m == 0xffffffffu);
}
template <int A, int B, int N, class F> DI void chk(u32x4 (&v)[N], F&& reload) {
  if (__builtin_expect(SPIN_MAX && has_mark<A, B>(v), 0)) {
    u32 spin = 0;
    do { asm volatile("" ::: "memory"); reload(); } while (has_mark<A, B>(v) && ++spin < SPIN_MAX);
  }
}
template <int A, int B, int N> DI void ld_lin(const Ctx& c, u32x4 (&v)[N], u32 soff, u32 stride) {
#pragma unroll
  for (int i = A; i < B; ++i) v[i] = ld_dev(c, c.l16, soff + i * stride);
}
template <int A, int B, int N> DI void ld_1k(const Ctx& c, u32x4 (&v)[N], u32 soff) {
#pragma unroll
  for (int i = A; i < B; ++i) v[i] = ld_dev(c, c.l16 + (i & 3) * 1024, soff + (i >> 2) * 4096);
}
template <int A, int B> DI void ld_h(const Ctx& c, u32x4 (&v)[14], u32 voff, u32 soff) {
#pragma unroll
  for (int i = A; i < B; ++i) v[i] = ld_dev(c, voff + (i & 1) * 1024, soff + c.rot[i >> 1] * 8192);
}
DI void o_publish(const Ctx& c, const u32x4 (&kv)[4], const u32x4 (&phq)[4], float g) {
  f32x16 n = {};
#pragma unroll
  for (int i = 0; i < 4; ++i) n = mfma(as_frag(kv[i]), as_frag(phq[i]), n);
  const bool odd = c.hd & 1;
  f32x2 t = xhalf(odd ? n[0] : n[12]);
  float sc = g * __builtin_amdgcn_rcpf((odd ? t[0] : t[1]) + 1e-6f);
#pragma unroll
  for (int i = 0; i < 16; ++i) n[i] *= sc;
  u32x4 f0 = sanit(pack8<0>(n)), f1 = sanit(pack8<1>(n));
  const bool z0 = odd && c.lane < 32, z1 = !odd && c.lane >= 32;
  f0[0] = z0 ? 0 : f0[0]; f0[1] = z0 ? 0 : f0[1];
  f1[2] = z1 ? 0 : f1[2]; f1[3] = z1 ? 0 : f1[3];
  st_dev(c, f0, c.l16, c.oo);
  st_dev(c, f1, c.l16 + 1024, c.oo);
}
DI u32 flag_off(const Ctx& c, bool spare) {
  const u32 i = c.lane & 15;
  u32 red = (c.hd * 16 + 7 + 8 * (i & 1)) << 7, sp = ((c.t * 16 + 7 + 8 * c.kh) << 7) + 8;
  u32 lo = spare ? ((i * 16 + c.t) << 7) + (c.r ? 0 : 8 * W1) : ((c.t * 16 + (i < 7 ? 8 * c.kh + i : c.hd ^ 8)) << 7) + 8;
  asm volatile("" : "+v"(red), "+v"(sp), "+v"(lo));
  return WS_FLAG + ((c.lane & 16) ? red : (spare || i < 8) ? lo : sp);
}
DI bool flag_bad(const Ctx& c, u32x2 f) { return any_(f[0] != (u32)c.tag || f[1] != (u32)(c.tag >> 32)); }
DI void flag_spin(const Ctx& c, u32x2 f, u32 voff) {
  if (__builtin_expect(SPIN_MAX && flag_bad(c, f), 0)) {
    u32 spin = 0;
    do {
      __builtin_amdgcn_s_sleep(2);
      asm volatile("" ::: "memory");
      f = __builtin_amdgcn_raw_buffer_load_b64(c.sw, voff, 0, 16);
    } while (flag_bad(c, f) && ++spin < SPIN_MAX);
  }
  asm volatile("" ::: "memory");
}
DI void flag_publish(const Ctx& c, u32 word) {
  u32x2 v = {(u32)c.tag, (u32)(c.tag >> 32)};
  __builtin_amdgcn_raw_buffer_store_b64(v, c.sw, c.vone + word * 8, c.oflag, 16);
}
DI u32x2 ld_g(const Ctx& c) { return __builtin_amdgcn_raw_buffer_load_b64(c.sw, (c.lane & 31) * 8, c.og, 16); }

template <u32 L, bool DEV> DI void touch(u32 voff, rsrc_t rs, u32 soff) {
  if (ABL & 9) return;
  if constexpr (DEV) asm volatile("s_mov_b32 m0, %0\n\ts_nop 0\n\tbuffer_load_dword %1, %2, %3 offen sc1 lds" :: "i"(L), "v"(voff), "s"(rs), "s"(soff) : "memory");
  else asm volatile("s_mov_b32 m0, %0\n\ts_nop 0\n\tbuffer_load_dword %1, %2, %3 offen lds" :: "i"(L), "v"(voff), "s"(rs), "s"(soff) : "memory");
}

constexpr int PRO = PROV;
constexpr int NS = 21;
constexpr int need_(int s) {
  return s < 0 ? 0 : s < 7 ? 3 * (s + 1) : s < 9 ? 23 + 2 * (s - 7) : s < 12 ? 31 + 2 * (s - 9) : s < 14 ? 36 : s < 18 ? 39 + 3 * (s - 14) : s < 20 ? 50 + 2 * (s - 18) : 62;
}
constexpr int iss_(int s) {
  constexpr int t[14] = {I7, I8, I9, I10, I11, I12, 49, 51, 53, 56, 59, I18, I19, I20};
  return s < 0 ? PRO : s < 7 ? imin(imin(28, 18 + need_(s - 2)), PRO + 3 * (s + 1) + (s >= A4 ? s - A4 + 1 : 0)) : t[s - 7];
}
constexpr int ustep(int n) {
  if (n == 34 || n == 28) return 16;
  if (n >= 25 && n < 28) return n == 25 ? 12 : 13;
  int s = 0; while (s < 20 && need_(s) <= n) ++s; return s;
}
constexpr bool ring_ok() {
  for (int s = -1; s < NS; ++s)
    for (int n = s < 0 ? 0 : iss_(s - 1); n < iss_(s); ++n) {
      if (uslot(n) == -2 && s < 18) return false;
      for (int m = 0; m < n; ++m) if (uslot(m) == uslot(n) && ustep(m) + 2 > s) return false;
    }
  return true;
}
static_assert(ring_ok(), "ring schedule");
constexpr int S_FLAG0 = FLAG0, S_POLL0 = POLL0, S_CHK0 = 7, S_GATE = GATE, S_CHKG = 13, S_FLAG1 = 16, S_POLL = POLL1, S_CHK = CHK1;
constexpr int oth_(int r, int s) {
  return s == S_FLAG0 ? 1 - r : s == S_POLL0 ? 1 - r : s == 10 ? (GATE == 10) : s == 11 ? 1 - r + (GATE == 11) : s == 12 ? r : s == 14 ? 2 : s == 16 ? 3 - 3 * r + TOUCH1 :
         s == S_POLL ? 1 - r : s == 18 ? (r || CHK1 == 19 ? 0 : 4) : s == 19 || s == 20 ? (r ? 0 : CHK1 == 19 ? 7 : 5) : 0;
}
constexpr int tot_(int r, int s) { return iss_(s) - iss_(s - 1) + oth_(r, s); }
constexpr int vm_(int r, int s) {
  int sp = -1; while (iss_(sp) < need_(s)) ++sp;
  int n = iss_(sp) - need_(s) + (sp < 0 ? 2 - r : 0);
  for (int k = sp + 1; k < s; ++k) n += tot_(r, k);
  if (s == S_FLAG0) { int m = 0; for (int k = 0; k < s; ++k) m += tot_(r, k); n = imin(n, m); }
  if (s == S_FLAG1) n = imin(n, tot_(r, 15));
  if (((s == S_CHK0 || s == S_CHK) && r == 0) || s == S_CHKG) {
    int m = -1; for (int k = s == S_CHK0 ? S_POLL0 : s == S_CHKG ? S_GATE : S_POLL; k < s; ++k) m += tot_(r, k);
    n = imin(n, m);
  }
  return n;
}
constexpr int P2_(int r) { return r ? 168 : 140; }
constexpr int item0_(int r, int s) { return s < 7 ? 4 * s : (!r || s < 15) ? 8 * s - 28 : s < 18 ? 84 + 16 * (s - 14) : 8 * s; }
template <int R, int J> DI void rd_item(P1& p) {
  constexpr int U2 = P2_(R) - 28;
  if constexpr (J >= P2_(R)) {
    constexpr int k = J - P2_(R);
    if constexpr (k < 56) {
      constexpr u32 o = spos(w2s_(k / 4)) + ((k / 2) & 1) * 2048;
      p.f[J % 8] = tr_frag(sa(p.s2[k & 1], o), sa(p.s2[k & 1], o + 1024));
    } else if constexpr (k < 72) {
      constexpr int i = (k - 56) / 2, hh = i >> 1;
      constexpr u32 o = (28 * hh - 4 * (hh & 1) + 16 * (i & 1)) * 128;
      p.f[J % 8] = tr_frag(p.ob[k & 1] + o, p.ob[k & 1] + o + 1024);
    }
  } else if constexpr ((J >= 44 && J < 72) || (R == 1 && J >= 84 && J < U2 && (J & 1))) {
    constexpr int k = J < 72 ? J - 44 : (J - 84) / 2, ch = k / 4;
    constexpr u32 o = (J < 72 ? upos(29 + ch) + R * 4096 : upos(36 + 3 * (ch / 2)) + (ch & 1) * 4096) + (k % 4) * 1024;
    p.f[J % 8] = as_frag(lds_rd128(sa(p.aq, o)));
  } else {
    constexpr int k = J < 84 ? J : J < U2 ? (J - 84) / (1 + R) : J - U2, ch = k / 4;
    constexpr u32 o = upos(J < 28 ? 3 * ch + 2 : J < 44 ? 14 + ch : J < 84 ? 7 + ch : J < U2 ? 37 + 3 * (ch / 2) + (ch & 1) : 47 + ch) + (k % 4) * 2048;
    p.f[J % 8] = tr_frag(sa(p.aw, o), sa(p.aw, o + 512));
  }
}
template <int R, int J> DI void mm_item(P1& p) {
  constexpr int U2 = P2_(R) - 28;
  if constexpr (J < 0) {}
  else if constexpr (J >= P2_(R)) {
    constexpr int k = J - P2_(R);
    p.c[k & 1] = mfma(p.f[J % 8], as_frag(k < 28 ? p.h1[(k / 2) % 14] : k < 56 ? p.h2[(k / 2) % 14] : p.of[(k / 2 + 4) % 8]), p.c[k & 1]);
  } else if constexpr (J >= 44 && J < 72) {
    if constexpr (R == 1) p.a1 = mfma(p.xf[J - 44], p.f[J % 8], p.a1); else p.a0 = mfma(p.f[J % 8], p.xf[J - 44], p.a0);
  } else {
    f32x16& acc = J < 28 ? p.g1 : J < 84 ? p.u1 : J >= U2 ? p.u2 : (R == 1 && (J & 1)) ? p.a0 : p.g2;
    acc = mfma(p.f[J % 8], p.xf[J < 84 ? J % 28 : J < U2 ? (J - 84) / (1 + R) : J - U2], acc);
  }
}
template <int R, int A, int B> DI void mm_range(P1& p) { if constexpr (A < B) { mm_item<R, A>(p); mm_range<R, A + 1, B>(p); } }

constexpr float GK1 = -2.0f * 0.7978845608028654f * LOG2E, GK2 = GK1 * 0.044715f;
template <bool SC, int OP> DI void gelu_op(float (&y)[16], const f32x16& g, float om) {
  constexpr int st = OP % 4, i = OP / 4 - st;
  if constexpr (NOGELU && i >= 0 && i < 16) { if constexpr (st == 3) { y[i] = g[i]; PIN1(y[i]); } }
  else if constexpr (i >= 0 && i < 16) {
    if constexpr (st == 0) y[i] = g[i] * __builtin_fmaf(g[i] * g[i], GK2, GK1);
    else if constexpr (st == 1) y[i] = __builtin_amdgcn_exp2f(y[i]);
    else if constexpr (st == 2) y[i] = __builtin_amdgcn_rcpf(1.0f + y[i]);
    else y[i] = SC ? (g[i] * om) * y[i] : g[i] * y[i];
    PIN1(y[i]);
  }
}
constexpr int gcost(int op) { return op / 4 - op % 4 < 0 || op / 4 - op % 4 > 15 ? 0 : op % 4 == 0 ? 3 : op % 4 == 1 ? 2 : op % 4 == 2 ? 3 : 1; }
constexpr int gcum(int k, int n) { if (k >= n) return 76; int op = 0, cst = 0; while (cst * n < k * 144) cst += gcost(op++); return op; }
template <bool SC, int A, int B> DI void gelu_ops(float (&y)[16], const f32x16& g, float om) { if constexpr (A < B) { gelu_op<SC, A>(y, g, om); gelu_ops<SC, A + 1, B>(y, g, om); } }
template <int S> DI void h_store(const Ctx& c, const float (&y)[16], const f32x16& u, int blk) {
  float h[8];
#pragma unroll
  for (int i = 0; i < 8; ++i) h[i] = y[8 * S + i] * u[8 * S + i];
  st_dev(c, pack8<0>(h), c.l16 + S * 1024, c.oh + blk * 4096);
}
template <int A, int B> DI void y_scale(P1& p) {
#pragma unroll
  for (int i = A; i < B; ++i) { p.y[i] *= p.om; PIN1(p.y[i]); }
}
DI void addr2(const Ctx& c, P1& p) {
  const u32 l = c.lane, ks = c.tg >> 1, qq = (l & 15) >> 2;
  const u32 bk = ((4 * (l >> 5) + qq) * 128 + ((qq >> 1) << 6) + 32 * ((l >> 4) & 1) + 8 * (l & 3)) ;
  p.s2[0] = mkseg(bk + ks * 4096); p.s2[1] = mkseg((bk ^ 64) + ks * 4096);
  p.ob[0] = bk + ks * (112 * 128); p.ob[1] = (bk ^ 64) + ks * (112 * 128);
  PIN2(p.ob[0], p.ob[1]);
}
template <int R, int S, int K, int J> DI void slot_work(Ctx& c, P1& p) {
  if constexpr (S < 7) p.xf[J] = as_frag(lds_rd128((p.ax + upos(3 * S)) ^ (u32)(32 * K)));
  if constexpr (R == 0) {
    if constexpr (J >= 33 && J < 75) gelu_ops<false, gcum(J - 33, 42), gcum(J - 32, 42)>(p.y, p.g1, 0.f);
    if constexpr (J == 74) wphi_rd(c, p);
    if constexpr (J == 78 || J == 79) y_scale<8 * (J - 78), 8 * (J - 77)>(p);
    constexpr int F0 = J < 84 ? J - 77 : J < 89 ? 7 + (3 * (J - 84) + 1) / 2 : J < 91 ? 15 : 15 + (9 * (J - 91) + 7) / 8;
    constexpr int F1 = J + 1 < 84 ? J - 76 : J + 1 < 89 ? 7 + (3 * (J - 83) + 1) / 2 : J + 1 < 91 ? 15 : 15 + (9 * (J - 90) + 7) / 8;
    if constexpr (J >= 77 && J < 99) fm_slices<F0, imin(24, F1)>(p);
    if constexpr (J == 92) phi_image<0>(c, p);
    if constexpr (J == 99) phi_image<1>(c, p);
    if constexpr (S == 16) kv_stage<VBUF, K>(c, p);
    if constexpr (CHK1 == 19 && S >= 19 && K >= 1) ld_h<7 * (S - 19) + K - 1, 7 * (S - 19) + K>(c, p.h1, c.l16, p.hbase);
    if constexpr (CHK1 == 18 && S == 18 && (K & 1)) ld_h<K / 2, K / 2 + 1>(c, p.h1, c.l16, p.hbase);
    if constexpr (CHK1 == 18 && S >= 19 && ((K & 1) || K == 4)) { constexpr int i = 4 + 5 * (S - 19) + (K < 4 ? K / 2 : (K + 1) / 2); ld_h<i, i + 1>(c, p.h1, c.l16, p.hbase); }
  } else {
    if constexpr (J >= 33 && J < 77) gelu_ops<false, gcum(J - 33, 44), gcum(J - 32, 44)>(p.y, p.g1, 0.f);
    if constexpr (J == 77 || J == 78) v_handoff<VBUF, J - 77>(c, p.a1);
    if constexpr (J == 79) wphi_rd(c, p);
    if constexpr (J == 83) PIN2(p.wp[0], p.wp[1]);
    if constexpr (J >= 84 && J < 88) y_scale<4 * (J - 84), 4 * (J - 83)>(p);
  }
  if constexpr (J == 89 || J == 90) h_store<J - 89>(c, p.y, p.u1, 0);
  if constexpr (J >= P2_(R) - 23) gelu_ops<true, gcum(J - P2_(R) + 23, GN), gcum(J - P2_(R) + 24, GN)>(p.y, p.g2, p.om);
  if constexpr (J == P2_(R) - 26) addr2(c, p);
  if constexpr (TOUCH1 && S == S_FLAG1 && K == 6) touch<upos(45) + 4096, true>(c.lane < 16 ? c.lane * 128 : OOB, c.sw, c.oh);
  if constexpr (S == 11 + R && K == MK) st_dev(c, (u32x4){~0u, ~0u, ~0u, ~0u}, c.l16 + R * 1024, c.op);
}
template <int R, int S, int J0, int K, int N> DI void slots(Ctx& c, P1& p) {
  if constexpr (K < N) {
    constexpr int nu = iss_(S) - iss_(S - 1), u0 = iss_(S - 1);
    constexpr bool front = (FRONT >> S) & 1, back = (BACK >> S) & 1;
    constexpr int ua = front ? imin(K, nu) : back ? (K > N - nu ? K - N + nu : 0) : K == 0 ? 0 : ((2 * K + R) * nu) / (2 * N);
    constexpr int ub = front ? imin(K + 1, nu) : back ? (K + 1 > N - nu ? K + 1 - N + nu : 0) : K == N - 1 ? nu : ((2 * K + 2 + R) * nu) / (2 * N);
    if constexpr (R == 1 && S >= 18 && (K == 0 || K == 4)) __builtin_amdgcn_s_setprio(K == 0);
    mm_item<R, J0 + K - 4>(p);
    if (ORD) SB0();
    if constexpr (ub > ua) unit_m0<u0 + ua>(c);
    rd_item<R, (J0 + K) ^ SWP>(p);
    if constexpr (ub > ua) { unit_go<R, u0 + ua>(c); issue_range<R, u0 + ua + 1, u0 + ub>(c); }
    if (ORD) SB0();
    slot_work<R, S, K, J0 + K>(c, p);
    SB0();
    slots<R, S, J0, K + 1, N>(c, p);
  }
}
template <int R, int S> DI void step(Ctx& c, P1& p) {
  if constexpr ((S == S_CHK || S == S_CHK0) && R == 0) {
    asm volatile("s_waitcnt vmcnt(%1)" : "+v"(p.poll) : "n"(vm_(R, S)) : "memory");
    PIN1(S == S_CHK ? (CHK1 == 19 ? p.u2 : p.g2) : p.g1);
    flag_spin(c, p.poll, S == S_CHK ? c.vflag : c.vflag0);
    asm volatile("s_barrier" ::: "memory");
  } else if constexpr (S == S_CHKG) {
    asm volatile("s_waitcnt vmcnt(%1)\n\ts_barrier" : "+v"(p.poll) : "n"(vm_(R, S)) : "memory");
    {
      PIN2(p.u1, R ? p.a1 : p.a0);
      if (__builtin_expect(SPIN_MAX && any_(p.poll[1] != (u32)c.tag), 0)) {
        u32 spin = 0;
        do { __builtin_amdgcn_s_sleep(2); asm volatile("" ::: "memory"); p.poll = ld_g(c); } while (any_(p.poll[1] != (u32)c.tag) && ++spin < SPIN_MAX);
      }
      p.gq = u2f(p.poll[0]); p.om = 1.0f - p.gq;
      PIN2(p.gq, p.om);
    }
  } else if constexpr ((S == 16 && R == 0) || (S == 20 && NPRE > 4)) wait_bar<vm_(R, S)>();
  else wait_bar_v<vm_(R, S)>();
  if constexpr (S < 2 || S >= 6) STAMP(S < 2 ? S + 1 : S - 3);
  if constexpr (S == DELAYS) __builtin_amdgcn_s_sleep(16);
  if constexpr (S == S_FLAG0 && R == 0) flag_publish(c, 0);
  if constexpr (S == S_FLAG1 && R == 0) flag_publish(c, 1);
  if constexpr ((S == S_POLL || S == S_POLL0) && R == 0) asm volatile("buffer_load_dwordx2 %0, %1, %2, 0 offen sc1" : "=v"(p.poll) : "v"(S == S_POLL ? c.vflag : c.vflag0), "s"(c.sw) : "memory");
  if constexpr (S == S_GATE) asm volatile("buffer_load_dwordx2 %0, %1, %2, %3 offen sc1" : "=v"(p.poll) : "v"((c.lane & 31) * 8), "s"(c.sw), "s"(c.og) : "memory");
  SB0();
  slots<R, S, item0_(R, S), 0, item0_(R, S + 1) - item0_(R, S)>(c, p);
}
template <int R, int S, int E> DI void steps(Ctx& c, P1& p) { if constexpr (S < E) { step<R, S>(c, p); steps<R, S + 1, E>(c, p); } }
template <int R, int A, int B, int RE, class F> DI void run(P1& p, F&& extra) {
  if constexpr (A < B) {
    mm_item<R, A - 4>(p);
    if (ORD) SB0();
    if constexpr (A < RE) rd_item<R, A ^ 1>(p);
    if (ORD) SB0();
    extra(std::integral_constant<int, A>{});
    SB0();
    run<R, A + 1, B, RE>(p, extra);
  } else { PIN2(p.c[0], p.c[1]); }
}
template <int K, class F> DI void fmq(P1& p, F&& extra) {
  if constexpr (K < 24) { fm_slice<K>(p); extra(std::integral_constant<int, K>{}); SB0(); fmq<K + 1>(p, extra); }
}
template <int R, int J> DI void rd4(P1& p) { rd_item<R, J + 1>(p); rd_item<R, J>(p); rd_item<R, J + 3>(p); rd_item<R, J + 2>(p); }

template <int R> DI void ffn_wg(Ctx& c, P1& p) {
  const u32 l = c.lane;
  const int tg2 = c.tg & 1, ks = c.tg >> 1;
  const int TG = 2 * (R == 1 ? c.kh : 1 - c.kh) + tg2;
  p.hbase = WS_H + (u32)(((c.t * 4 + TG) * 112 + 56 * c.kh + 2 * ks) * 1024);
  if (XB & 4) p.hbase = 0x40000000u;
  asm volatile("" : "+s"(p.hbase));
  SB0();
  issue_range<R, 3, PRO>(c);
  asm volatile("s_waitcnt vmcnt(%0)" :: "n"(PRO - 3) : "memory");
  repack(c);
  if constexpr (R == 0) st_dev(c, (u32x4){~0u, ~0u, ~0u, ~0u}, c.l16, c.okvp);
  p.g1 = p.u1 = p.a0 = p.a1 = p.g2 = p.u2 = p.c[0] = p.c[1] = (f32x16){};
  steps<R, 0, NS>(c, p);
  STAMP(18);
  auto none = [](auto) {};
  constexpr int B = P2_(R);
  const u32 h = l >> 5;
  const u32 obase = ((XB & 8) ? 0x40000000u : WS_O) + (u32)(((c.t * 4 + TG) * 16 + 8 * c.kh + 4 * ks) * 2048);
  const u32 l16h = c.l16 + 4096, vflag2 = c.vflag + ((l & 31) < 7 ? 8 : 0);
  auto epi2 = [&](auto J) {
    if constexpr (J.value < B + 4) gelu_ops<true, gcum(J.value - B + 23, GN), gcum(J.value - B + 24, GN)>(p.y, p.g2, p.om);
    if constexpr (J.value == B + HS || J.value == B + HS + 1) h_store<J.value - B - HS>(c, p.y, p.u2, 1);
  };
  if constexpr (R == 0) {
    run<R, 140, 140 + XP, 999>(p, [&](auto J) {
      epi2(J);
      if constexpr (NPRE && J.value == 139 + XP - NPRE) { PIN2(p.h1[10], p.h1[11]); PIN2(p.h1[12], p.h1[13]); }
      if constexpr (J.value >= 140 + XP - NPRE) unit_half<71 - (J.value - 140 - XP + NPRE) / 2, J.value & 1>(c);
    });
    STAMP(19);
    wait_bar_v<NPRE>();
    STAMP(20);
    flag_publish(c, 2);
    run<R, 140 + XP, 172, 999>(p, [&](auto J) { if constexpr (J.value - 140 - XP + NPRE < 8) unit_half<71 - (J.value - 140 - XP + NPRE) / 2, J.value & 1>(c); });
    STAMP(21);
    __builtin_amdgcn_s_sleep(PSLEEP);
    flag_spin(c, __builtin_amdgcn_raw_buffer_load_b64(c.sw, vflag2, 0, 16), vflag2);
    STAMP(22);
    ld_h<0, NB0>(c, p.h2, l16h, p.hbase);
    run<R, 172, 200, 999>(p, [&](auto J) {
      if constexpr (J.value - 172 + NB0 < 14) ld_h<J.value - 172 + NB0, J.value - 171 + NB0>(c, p.h2, l16h, p.hbase);
      if constexpr (J.value == 187) { asm volatile("s_barrier" ::: "memory"); STAMP(23); }
      if constexpr (J.value >= OPOS && J.value < OPOS + 8) ld_1k<J.value - OPOS, J.value - OPOS + 1>(c, p.of, obase);
    });
  } else {
    if (PR == 0) __builtin_amdgcn_s_setprio(1);
    run<R, 168, 172, 999>(p, epi2);
    fm_slices<0, 2>(p);
    SB0();
    h_store<0>(c, p.y, p.u2, 1); h_store<1>(c, p.y, p.u2, 1);
    SB0();
    STAMP(19);
    if (PR == 1) __builtin_amdgcn_s_setprio(1);
    fmq<3>(p, [&](auto K) {
      if constexpr (PR == 2 && K.value == 6) __builtin_amdgcn_s_setprio(1);
      if constexpr (K.value == XQ) {
        wait_bar_v<0>();

        ld_1k<0, 4>(c, p.kv, WS_KVR + (u32)c.hd * 4096);
      }
      if constexpr (K.value > XQ && K.value <= XQ + H1Q) ld_h<K.value - XQ - 1, K.value - XQ>(c, p.h1, c.l16, p.hbase);
    });
    STAMP(21);
    chk<0, 4>(p.kv, [&] { ld_1k<0, 4>(c, p.kv, WS_KVR + (u32)c.hd * 4096); });
    o_publish(c, p.kv, p.ph, p.gq);
    __builtin_amdgcn_s_setprio(0);
    STAMP(22);
    u32x2 f2;
    run<R, 172, C2, 999>(p, [&](auto J) {
      if constexpr (J.value - 172 + H1Q < 14) ld_h<J.value - 172 + H1Q, J.value - 171 + H1Q>(c, p.h1, c.l16, p.hbase);
      if constexpr (J.value == P2) f2 = __builtin_amdgcn_raw_buffer_load_b64(c.sw, vflag2, 0, 16);
    });
    flag_spin(c, f2, vflag2);
    STAMP(24);
        ld_h<0, NB1>(c, p.h2, l16h, p.hbase);
    run<R, C2, 228, 999>(p, [&](auto J) {
      if constexpr (J.value - C2 + NB1 < 14) ld_h<J.value - C2 + NB1, J.value - C2 + NB1 + 1>(c, p.h2, l16h, p.hbase);
      if constexpr (J.value == YR1) { asm volatile("s_barrier" ::: "memory"); STAMP(23); }
      if constexpr (J.value >= O1 && J.value < O1 + 8) ld_1k<J.value - O1, J.value - O1 + 1>(c, p.of, obase);
    });
  }
  STAMP(27);
  chk<0, 8>(p.of, [&] { ld_1k<0, 8>(c, p.of, obase); });
  STAMP(28);
  run<R, B + 60, B + 76, 999>(p, none);
  STAMP(29);
  u32x4 pp[2];
  auto ld_p = [&] { pp[0] = ld_dev(c, c.l16, c.op ^ (8 * 16384)); pp[1] = ld_dev(c, c.l16 + 1024, c.op ^ (8 * 16384)); };
  if constexpr (R == 1) ld_p();
  const u32 xw = R ? (c.tg ? spos(8) + c.tg * UNIT : spos(6)) : spos(2) + c.tg * UNIT;
  const u32 xo = R ? (c.tg != 2 ? spos(8) + (c.tg ^ 2) * UNIT : spos(6)) : spos(2) + (c.tg ^ 2) * UNIT;
  {
    const u32 wa = xw + (l & 31) * 256 + ((h ^ (l & 7)) << 4);
#pragma unroll
    for (int g = 0; g < 8; ++g) {
      const f32x16& v = p.c[g >> 2];
      *(LAS f32x4*)((wa ^ (u32)((g & 3) << 5)) + (g >> 2) * 128) = (f32x4){v[4 * (g & 3)], v[4 * (g & 3) + 1], v[4 * (g & 3) + 2], v[4 * (g & 3) + 3]};
    }
  }
  volatile LAS u32* fl = (volatile LAS u32*)(spos(1) + 4096);
  asm volatile("" ::: "memory");
  fl[c.w] = 1u;
  const u32 ra = (ks * 16 + (l >> 3)) * 256 + (l & 4) * 32 + (((2 * (l & 3)) ^ (l >> 3)) << 4);
  f32x4 own[4];
#pragma unroll
  for (int q = 0; q < 4; ++q) own[q] = *(LAS f32x4*)(xw + (ra ^ (u32)((q & 1) << 4)) + (q >> 1) * 2048);
  { u32 spin = 0; while (__builtin_amdgcn_readfirstlane(fl[c.w ^ 2]) == 0 && ++spin < (1u << 22)) {} }
  asm volatile("" ::: "memory");
  float r16[16];
#pragma unroll
  for (int q = 0; q < 4; ++q) {
    const f32x4 b = *(LAS f32x4*)(xo + (ra ^ (u32)((q & 1) << 4)) + (q >> 1) * 2048);
#pragma unroll
    for (int i = 0; i < 4; ++i) r16[4 * q + i] = own[q][i] + b[i];
  }
  if constexpr (R == 0) {
    st_dev(c, sanit(pack8<0>(r16)), c.l16, c.op);
    st_dev(c, sanit(pack8<1>(r16)), c.l16 + 1024, c.op);
    STAMP(30);
  } else {
#pragma unroll
    for (int i = 0; i < 16; ++i) PIN1(r16[i]);
    chk<0, 2>(pp, ld_p);
    STAMP(30);
#pragma unroll
    for (int d = 0; d < 4; ++d) {
      r16[2 * d] += blo(pp[0][d]); r16[2 * d + 1] += bhi(pp[0][d]);
      r16[8 + 2 * d] += blo(pp[1][d]); r16[9 + 2 * d] += bhi(pp[1][d]);
    }
    const u32 vo = (l >> 3) * 896 + (l & 7) * 16, oo = (c.t * 128 + c.kh * 64 + tg2 * 32 + ks * 16) * 896 + c.jj * 128;
#pragma unroll
    for (int s = 0; s < 2; ++s) {
      u32x4 o = s ? pack8<1>(r16) : pack8<0>(r16);
      __builtin_amdgcn_raw_buffer_store_b128(o, mk(c.out, 2048 * 896), vo, oo + s * (8 * 896), OUTAUX);
      asm volatile("s_nop 1" :: "v"(o) : "memory");
    }
    if (__builtin_amdgcn_workgroup_id_x() == 0 && c.w == 4) {
      u32x2 v = {(u32)c.tag, (u32)(c.tag >> 32)};
      __builtin_amdgcn_raw_buffer_store_b64(v, c.sw, l == 0 ? 0u : OOB, 0, 16);
    }
    STAMP(31);
  }
}

template <int R, int A0, int A, int B, int ST> DI void pipe(P1& p) {
  if constexpr (A - 3 * ST < B) {
    if constexpr (A - 3 * ST >= A0) mm_item<R, A - 3 * ST>(p);
    if constexpr (A < B) rd_item<R, A>(p);
    SB0();
    pipe<R, A0, A + ST, B, ST>(p);
  }
}
constexpr u32 sxpos(int ch) { return spos(2 * ch - 2); }
template <int N, int E> DI void spare_x(const Ctx& c) {
  if constexpr (N < E) { set_m0<sxpos(N >> 1) + (N & 1) * UNIT>(c); asm volatile("s_nop 0"); dma<0>((N & 1) ? c.vx2 : c.vx, c.sx, c.rot[N >> 1] * 128); spare_x<N + 1, E>(c); }
}
template <int R> DI void spare_wg(Ctx& c, P1& p) {
  const u32 l = c.lane;
  set_m0<spos(12)>(c); asm volatile("s_nop 0"); dma<0>(c.l16, mk(c.gw, 896), c.w == 0 ? 0u : 0x40000000u);
  const u32 okvr = WS_KVR + (u32)(c.t * 4096 + (2 * c.kh + (c.w & 1)) * 1024);
  const u32x4 ones = {~0u, ~0u, ~0u, ~0u};
  st_dev(c, ones, R == 0 || c.tg < 2 ? c.l16 : OOB, R == 0 ? c.okvp : okvr);
  spare_x<2, 4>(c);
  asm volatile("s_waitcnt vmcnt(4)" ::: "memory");
  repack(c);
  spare_x<4, 14>(c);
  wait_bar<0>();
  if constexpr (R == 0) flag_publish(c, 0);
  if (!OL)
  {
    const u32 mo = WS_O + ((c.t * 4 + c.tg) * 16 + 8 * c.kh + 4 * R) * 2048;
#pragma unroll
    for (int i = 0; i < 8; ++i) st_dev(c, ones, c.l16 + (i & 3) * 1024, mo + (i >> 2) * 4096);
  }
#pragma unroll
  for (int s = 0; s < 28; ++s) p.xf[s] = as_frag(lds_rd128((p.ax ^ (u32)(32 * (s & 3))) + sxpos(s >> 2)));
  STAMP(1);
  f32x16 ga = {};
#pragma unroll
  for (int s = 14 * R; s < 14 * R + 14; ++s) ga = mfma(as_frag(lds_rd128(spos(12) + c.rot[s >> 2] * 128 + (s & 3) * 32 + (l >> 5) * 16)), p.xf[s], ga);
  PIN1(ga);
  float g1 = 0.f;
  if constexpr (R == 0) *(LAS float*)(spos(17) + c.tg * 256 + l * 4) = ga[0];
  u32x2 f0 = __builtin_amdgcn_raw_buffer_load_b64(c.sw, c.vflag0, 0, 16);
  bar_l();
  if constexpr (R == 1) {
    g1 = __builtin_amdgcn_rcpf(1.0f + __builtin_amdgcn_exp2f(-LOG2E * (ga[0] + *(LAS float*)(spos(17) + c.tg * 256 + l * 4))));
    __builtin_amdgcn_raw_buffer_store_b64((u32x2){f2u(g1), (u32)c.tag}, c.sw, l < 32 ? l * 8 : OOB, c.og, 16);
  }
  flag_spin(c, f0, c.vflag0);
  issue_range<R, 29, 36>(c); issue_range<R, 28, 29>(c);
  issue_range<R, 39, 40>(c); issue_range<R, 42, 43>(c);
  p.a0 = p.a1 = (f32x16){};
  wait_bar<6>();
  STAMP(2);
  issue_range<R, 36, 37>(c);
  if (OL)
  {
    const u32 mo = WS_O + ((c.t * 4 + c.tg) * 16 + 8 * c.kh + 4 * R) * 2048;
#pragma unroll
    for (int i = 0; i < 8; ++i) st_dev(c, ones, c.l16 + (i & 3) * 1024, mo + (i >> 2) * 4096);
  }
  pipe<R, 44, 44, 60, 1>(p);
  wait_bar_v<W1 ? 8 : 0>();
  if constexpr (R == 0 && !W1) flag_publish(c, 1);
  issue_range<R, 45, 46>(c);
  wphi_rd(c, p);
  pipe<R, 60, 60, 72, 1>(p);
  wait_bar<0>();
  if constexpr (R == 0) { fm_slices<0, 16>(p); if (W1) flag_publish(c, 1); fm_slices<16, 24>(p); phi_image<0>(c, p); phi_image<1>(c, p); }
  else { v_handoff<spos(1), 0>(c, p.a1); v_handoff<spos(1), 1>(c, p.a1); pipe<R, 85, 85, 140, 2>(p); PIN1(p.a0); }
  bar_l();
  if constexpr (R == 0) {
    if (c.w < 2) {
      kv_stage<spos(1), 0>(c, p); kv_stage<spos(1), 1>(c, p); kv_stage<spos(1), 2>(c, p); kv_stage<spos(1), 3>(c, p);
      kv_stage<spos(1), 4>(c, p); kv_stage<spos(1), 5>(c, p); kv_stage<spos(1), 7>(c, p);
    }
    STAMP(3);
    flag_spin(c, __builtin_amdgcn_raw_buffer_load_b64(c.sw, c.vflag, 0, 16), c.vflag);
    STAMP(4);
    u32x4 v[8];
    const u32 ro = WS_KVP + (u32)(c.t * 65536 + (2 * c.kh + (c.w & 1)) * 1024 + (c.w >> 1) * 512), vr = (l & 31) * 16 + (l >> 5) * 32768;
    auto ld = [&] {
#pragma unroll
      for (int i = 0; i < 8; ++i) v[i] = ld_dev(c, vr, ro + i * 4096);
    };
    __builtin_amdgcn_s_sleep(RS0);
    ld();
    chk<0, 8>(v, [&] { __builtin_amdgcn_s_sleep(RSL); ld(); });
    STAMP(5);
    float s[8];
    set8(s, v[0]);
#pragma unroll
    for (int i = 1; i < 8; ++i) add8(s, v[i]);
#pragma unroll
    for (int i = 0; i < 8; ++i) { const f32x2 t = xhalf(s[i]); s[i] = t[0] + t[1]; }
    st_dev(c, sanit(pack8<0>(s)), l < 32 ? c.l16 : OOB, okvr + (c.w >> 1) * 512);
    STAMP(6);
  } else {
    fm_slices<0, 24>(p);
    STAMP(8);
    flag_spin(c, __builtin_amdgcn_raw_buffer_load_b64(c.sw, c.vflag, 0, 16), c.vflag);
    STAMP(9);
    __builtin_amdgcn_s_sleep(QS0);
    u32x4 v[4];
    const u32 kvo = WS_KVR + (u32)c.hd * 4096;
    ld_1k<0, 4>(c, v, kvo);
    chk<0, 4>(v, [&] { __builtin_amdgcn_s_sleep(QSL); ld_1k<0, 4>(c, v, kvo); });
    STAMP(10);
    o_publish(c, v, p.ph, g1);
    STAMP(7);
  }
}

extern "C" __global__ __launch_bounds__(512, 2) void fused_kernel(const char* x, const char* wqkv, const char* wphi, const char* wo, const char* wg,
                                                                   const char* wu, const char* wd, const char* gw, char* out, char* ws) {
  Ctx c;
  const int bid = __builtin_amdgcn_workgroup_id_x(), xcd = bid & 7, idx = bid >> 3;
  const u32 tid = __builtin_amdgcn_workitem_id_x();
  c.gw = gw; c.wd = wd; c.wo = wo; c.out = out; c.ws = ws;
  c.t = 4 * (xcd >> 1) + (idx & 3); c.kh = xcd & 1; c.jj = idx >> 2; c.hd = 8 * c.kh + c.jj; c.j = 7 * c.kh + c.jj;
  c.w = __builtin_amdgcn_readfirstlane(tid >> 6); c.tg = c.w & 3; c.r = c.w >> 2;
  c.lane = tid & 63; c.l16 = c.lane * 16; c.m0w = c.w * 1024;
  const u32 l = c.lane, w = c.w;
  c.vst = c.vsc = 0;
  STAMP(0);
  const u64 rt = 0x2106543210654321ull, r0 = RJ * c.jj + RT * (idx & 3) + RX * (xcd >> 1) + RK * c.kh;
  const u32 rs = (u32)(rt >> (4 * (r0 > 9 ? r0 - 7 : r0)));
#pragma unroll
  for (int i = 0; i < 7; ++i) c.rot[i] = (rs >> (4 * i)) & 7;
  const u32 row8 = 8 * w + (l >> 3), sw2 = ((l & 7) ^ (((l >> 4) & 1) << 2)) << 4;
  c.vx = row8 * 896 + (((l & 7) ^ ((4 * w + (l >> 4)) & 7)) << 4); c.vx2 = c.vx + 64 * 896;
  c.vw = row8 * 3584 + sw2;
  asm volatile("" : "+v"(c.vx), "+v"(c.vx2), "+v"(c.vw));
  asm volatile("" : "+s"(c.rot[0]), "+s"(c.m0w));
  SB0();
  const u64 epoch = *(const __attribute__((address_space(4))) u64*)ws;
  c.sx = mk(x + c.t * 114688, 114688);
  c.sg = mk(wg + c.j * 256, 448 * 3584 - c.j * 256);
  if (c.jj < 7) issue_range<0, 0, 3>(c);
  else {
    u32 lo; u64 pc;
    asm volatile("s_getpc_b64 %0\n.Lpf_here:\n\ts_mov_b32 %1, .Lpf_here-fused_kernel" : "=s"(pc), "=s"(lo));
    asm volatile("s_mov_b32 m0, %0\n\ts_nop 0\n\tbuffer_load_dword %1, %2, 0 offen lds"
                 :: "i"(spos(13)), "v"((w * 64 + l) * 128), "s"(mk((const char*)(pc - lo), CODE_BYTES)) : "memory");
    spare_x<0, 2>(c);
  }
  {
    const u32 win = 56 * c.hd - 8 * (c.hd & 1), F = 6 * c.t + w, part = (F * 147) >> 12;
    const bool ok = F < 86 && w < 6;
    set_m0<spos(17)>(c); asm volatile("s_nop 0");
    if (c.t == 14) dma<0>((c.l16 + 896 * w - 224 * (c.hd & 1)) & 0x7fffffffu, mk(wphi + c.hd * 1568, 1568), 0);
    else dma<0>((l >> 2) * 2688 + (l & 3) * 16, mk(wqkv, 448 * 2688), ok ? (F - 28 * part) * (16 * 2688) + part * 896 + win : 0x40000000u);
    c.vrp = (ok ? WS_WA + c.hd * WA_HEAD + F * 1024 : OOB) + c.l16;
  }
  SB0();
  c.tag = epoch + 1;
  c.v2 = row8 * 896 + sw2; c.v2b = c.v2 + 64 * 896;
  c.vq = WS_WA + c.hd * WA_HEAD + c.tg * 1024 + c.l16; c.vkv = c.vq + 28672 * (1 + c.r);
  c.vflag0 = WS_FLAG + (((l & 15) * 16 + c.hd) << 7);
  c.sw = mk(ws, WS_END);
  c.oh = WS_H + (u32)(((c.t * 4 + c.tg) * 112 + 8 * c.j + 2 * c.r) * 1024);
  c.oo = WS_O + (u32)(((c.t * 4 + c.tg) * 16 + c.hd) * 2048);
  c.op = WS_P + (u32)((c.t * 16 + c.hd) * 16384 + c.tg * 2048);
  c.okvp = WS_KVP + (u32)((c.hd * 16 + c.t) * 4096 + c.tg * 1024);
  c.oflag = WS_FLAG + (u32)(c.t * 16 + c.hd) * 128;
  c.og = WS_G + (u32)(c.kh * 2048 + c.t * 128 + c.tg * 32) * 8;
  if (XB & 1) c.op = 0x40000000u;
  if (XB & 2) c.oh = 0x40000000u;
  if (XB & 8) c.oo = 0x40000000u;
  static_assert(WS_P % (16 * 16384) == 0, "the partner's slot is op ^ (8 * 16384)");
  c.vflag = flag_off(c, c.jj == 7); c.vone = tid == 0 ? 0u : OOB;
  asm volatile("" : "+s"(c.tag), "+s"(c.oh), "+s"(c.oo), "+s"(c.op), "+s"(c.okvp), "+s"(c.oflag), "+s"(c.og) :: "memory");
  asm volatile("" : "+v"(c.vflag), "+v"(c.vflag0), "+v"(c.vone), "+v"(c.vrp));
  P1 p;
  {
    const u32 q = (l & 15) >> 2, pp = l & 3, blk = (l >> 4) & 1, h = l >> 5, r = l & 31;
    p.aw = mkseg((8 * h + q) * 128 + ((64 * c.r) ^ ((q >> 1) << 6)) + 32 * blk + 8 * pp);
    p.aq = mkseg(c.l16);
    p.ax = (32 * c.tg + r) * 128 + ((h ^ ((r >> 1) & 7)) << 4);
  }
  if (c.jj < 7) {
    c.su = mk(wu + c.j * 256, (XB & 32) ? 0 : 448 * 3584 - c.j * 256);
    c.sd = mk(wd + c.kh * 896 * 896 + c.jj * 128, (XB & 16) ? 0 : 896 * 896 - c.jj * 128);
    c.so = mk(wo + c.kh * 224 * 896 + c.jj * 128, (XB & 16) ? 0 : 224 * 896 - c.jj * 128);
    if (c.r == 0) { ffn_wg<0>(c, p); STAMP_END(); __builtin_amdgcn_endpgm(); }
    ffn_wg<1>(c, p);
    STAMP_END();
    __builtin_amdgcn_endpgm();
  }
  if (c.r == 0) { spare_wg<0>(c, p); STAMP_END(); __builtin_amdgcn_endpgm(); }
  spare_wg<1>(c, p);
  STAMP_END();
}

#if defined(__HIP_DEVICE_COMPILE__)
#pragma clang attribute pop
#endif

__global__ void amp_k(const unsigned long long* st){
  unsigned tnow=(unsigned)__builtin_amdgcn_s_memrealtime(); asm volatile("":"+s"(tnow)::"memory");
  unsigned mn=~0u;
  for(int i=threadIdx.x;i<2048;i+=64){ unsigned a=(unsigned)st[i*32]; mn=a<mn?a:mn; }
  for(int o=32;o;o>>=1){ unsigned b=__shfl_xor(mn,o); mn=b<mn?b:mn; }
  unsigned sum=0,mx=0,cnt=0;
  for(int b=threadIdx.x;b<256;b+=64){ bool sp=(b>>5)==7; if(sp!=('a'=='s')) continue; unsigned d=(unsigned)st[(b*8+4)*32+PROBE_K]-mn; if(d<100000){ sum+=d; mx=d>mx?d:mx; cnt++; } }
  for(int o=32;o;o>>=1){ sum+=__shfl_xor(sum,o); cnt+=__shfl_xor(cnt,o); unsigned b=__shfl_xor(mx,o); mx=b>mx?b:mx; }
  unsigned long long m = 'a'=='z' ? 0 : 'a'=='e' ? (unsigned long long)(tnow-mn-mx)*100 : 'a'=='m' ? (unsigned long long)mx*100 : (unsigned long long)sum*100/(cnt?cnt:1);
  unsigned long long t0=__builtin_amdgcn_s_memrealtime();
  while(__builtin_amdgcn_s_memrealtime()-t0<m) __builtin_amdgcn_s_sleep(4);
}
extern "C" void kernel_launch(void* const* d_in, const int* in_sizes, int n_in, void* d_out, int out_size, void* d_ws, size_t ws_size,
                              hipStream_t stream) {
  (void)hipFuncSetAttribute((const void*)fused_kernel, hipFuncAttributeMaxDynamicSharedMemorySize, LDS_BYTES);
  fused_kernel<<<256, 512, LDS_BYTES, stream>>>((const char*)d_in[0], (const char*)d_in[1], (const char*)d_in[2], (const char*)d_in[3],
                                                (const char*)d_in[5], (const char*)d_in[6], (const char*)d_in[7], (const char*)d_in[8],
                                                (char*)d_out, (char*)d_ws);
  amp_k<<<1,64,0,stream>>>((const unsigned long long*)((char*)d_ws + WS_STAMP));
}
```

```cpp
#include <hip/hip_runtime.h>
#include <hip/hip_bf16.h>
#include <type_traits>
#pragma clang diagnostic ignored "-Wint-to-pointer-cast"

typedef unsigned u32;
typedef unsigned long long u64;
typedef u32 u32x4 __attribute__((ext_vector_type(4)));
typedef u32 u32x2 __attribute__((ext_vector_type(2)));
typedef short s16x4 __attribute__((ext_vector_type(4)));
typedef __bf16 bf16x8 __attribute__((ext_vector_type(8)));
typedef __bf16 bf16x2 __attribute__((ext_vector_type(2)));
typedef float f32x16 __attribute__((ext_vector_type(16)));
typedef float f32x4 __attribute__((ext_vector_type(4)));
typedef float f32x2 __attribute__((ext_vector_type(2)));
typedef __amdgpu_buffer_rsrc_t rsrc_t;
#define DI __device__ __forceinline__
#define LAS __attribute__((address_space(3)))
#define SB0() __builtin_amdgcn_sched_barrier(0)
#define PIN1(a) asm volatile("" : "+v"(a))
#define PIN2(a, b) asm volatile("" : "+v"(a), "+v"(b))

constexpr u32 WS_FLAG = 4096;
constexpr u32 WS_G    = 131072;
constexpr u32 WS_KVR  = 65536;
constexpr u32 WS_KVP  = 262144;
constexpr u32 WS_O    = WS_KVP + 256 * 4096;
constexpr u32 WS_P    = WS_O + 64 * 32 * 1024;
constexpr u32 WS_H    = WS_P + 256 * 16384;
constexpr u32 WS_WA   = WS_H + 64 * 112 * 1024;
constexpr u32 WA_HEAD = 86 * 1024;
constexpr u32 WS_END  = WS_WA + 16 * WA_HEAD;

constexpr int UNIT = 8192, RING = 18;
constexpr u32 SCR = 0, RBASE = 2 * UNIT;
constexpr int LDS_BYTES = 163840;
constexpr u32 OOB = 0x80000000u;
constexpr u32 CODE_BYTES = 39424;
constexpr u32 SPIN_MAX = 1u << 16;
constexpr float LOG2E = 1.4426950408889634f;

#if defined(__HIP_DEVICE_COMPILE__)
#pragma clang attribute push(__attribute__((target("no-packed-fp32-ops"))), apply_to = function)
#endif
constexpr u32 spos(int k) { return RBASE + k * UNIT; }
constexpr int w2c_(int i) { constexpr int t[18] = {16, 17, 0, 1, 2, 3, 4, 5, 6, 7, 8, 9, 10, 11, 12, 13, 14, 15}; return t[i]; }
constexpr int w2s_(int c) { constexpr int t[18] = {2, 3, 4, 5, 6, 10, 11, 12, 13, 15, 14, 16, 7, 8, -2, -1, 0, 1}; return t[c]; }
constexpr int uslot(int n) {
  constexpr int t[26] = {-1, 10, 11, 12, 13, 14, 15, 16, 17, 0, 1, 2, 3, 4, 5, 6, 10, 11, 12, 13, 14, 16, 7, 8, 9, 17};
  return n < 28 ? n % RING : n < 54 ? t[n - 28] : w2s_(w2c_(n - 54));
}
constexpr u32 upos(int n) { return spos(uslot(n)); }
constexpr u32 VBUF = RBASE + 15 * UNIT;
constexpr int imin(int a, int b) { return a < b ? a : b; }

struct Ctx {
  const char* gw;
  char* out;
  int t, kh, jj, hd, j, w, tg, r;
  u32 lane, l16, m0w;
  u32 vx, vx2, vw, vq, vkv, v2, v2b;
  rsrc_t sx, sg, su, sd, so, sw;
  u64 tag;
  u32 rot[7];
  u32 oh, oo, op, okvp;
  u32 oflag, og;
  u32 vrp, vflag0, vflag, vone;
};

DI rsrc_t mk(const void* p, u32 bytes) { return __builtin_amdgcn_make_buffer_rsrc((void*)p, 0, bytes, 0x00020000); }
template <u32 L> DI void set_m0(const Ctx& c) { asm volatile("s_add_u32 m0, %0, %1" :: "s"(c.m0w), "i"(L) : "memory", "scc"); }
template <u32 OFF> DI void dma(u32 voff, rsrc_t rs, u32 soff) {
  asm volatile("buffer_load_dwordx4 %0, %1, %2 offen offset:%3 sc1 lds" :: "v"(voff), "s"(rs), "s"(soff), "i"(OFF) : "memory");
}
template <int N> DI void wait_bar() { asm volatile("s_waitcnt vmcnt(%0) lgkmcnt(0)\n\ts_barrier" :: "n"(N) : "memory"); }
template <int N> DI void wait_bar_v() { asm volatile("s_waitcnt vmcnt(%0)\n\ts_barrier" :: "n"(N) : "memory"); }
DI void bar_l() { asm volatile("s_waitcnt lgkmcnt(0)\n\ts_barrier" ::: "memory"); }

constexpr u32 ioff_(int n) { return n >= 36 && n < 54 && !(n < 47 && n % 3 == 0) ? 128 : 0; }
template <int N> DI void unit_m0(const Ctx& c) { set_m0<upos(N) - ioff_(N)>(c); }
template <int R, int N> DI void unit_go(const Ctx& c) {
  if constexpr (N < 21) {
    if constexpr (N % 3 < 2) dma<0>(N % 3 ? c.vx2 : c.vx, c.sx, c.rot[N / 3] * 128);
    else dma<0>(c.vw, c.sg, c.rot[N / 3] * (64 * 3584));
  } else if constexpr (N >= 29 && N < 36) dma<0>(c.vkv, c.sw, c.rot[N - 29] * 4096);
  else if constexpr (N == 28) dma<0>(c.vq, c.sw, c.w < 2 ? 84u * 1024u : 0x40000000u);
  else if constexpr (N < 28) dma<0>(c.vw, c.su, c.rot[N - 21] * (64 * 3584));
  else if constexpr (N < 47) {
    if constexpr (N % 3 == 0) dma<0>(c.vq, c.sw, N == 45 && R == 1 ? 0x40000000u : c.rot[(2 * (N - 36) / 3 + R) % 7] * 4096);
    else dma<128>(c.vw, c.sg, c.rot[2 * ((N - 36) / 3) + N % 3 - 1] * (64 * 3584));
  } else if constexpr (N < 54) dma<128>(c.vw, c.su, c.rot[N - 47] * (64 * 3584));
  else {
    constexpr int k = w2c_(N - 54);
    if constexpr (k < 14) dma<0>(k < 7 ? c.v2 : c.v2b, c.sd, c.rot[k % 7] * (128 * 896));
    else dma<0>((k & 1) ? c.v2b : c.v2, c.so, k < 16 ? 0u : 128u * 896u);
  }
}
template <int N, int H> DI void unit_half(const Ctx& c) {
  set_m0<upos(N) + H * 4096>(c);
  constexpr int k = w2c_(N - 54);
  const u32 so = (k < 14 ? c.rot[k % 7] * (128 * 896) : k < 16 ? 0u : 128u * 896u) + H * (32 * 896);
  asm volatile("s_nop 0");
  dma<0>((k < 14 ? k >= 7 : (k & 1)) ? c.v2b : c.v2, k < 14 ? c.sd : c.so, so);
}
template <int R, int A, int B> DI void issue_range(const Ctx& c) {
  if constexpr (A < B) { unit_m0<A>(c); asm volatile("s_nop 0"); unit_go<R, A>(c); issue_range<R, A + 1, B>(c); }
}

struct Seg { u32 b[3]; };
DI Seg mkseg(u32 a) { Seg s; s.b[0] = a; s.b[1] = a + 65536; s.b[2] = a + 131072; asm volatile("" : "+v"(s.b[0]), "+v"(s.b[1]), "+v"(s.b[2])); return s; }
DI u32 sa(const Seg& s, u32 off) { return s.b[off >> 16] + (off & 0xffffu); }
DI f32x16 mfma(bf16x8 a, bf16x8 b, f32x16 c) { return __builtin_amdgcn_mfma_f32_32x32x16_bf16(a, b, c, 0, 0, 0); }
DI bf16x8 as_frag(u32x4 v) { return __builtin_bit_cast(bf16x8, v); }
DI bf16x8 tr_frag(u32 a0, u32 a1) {
  s16x4 lo = __builtin_amdgcn_ds_read_tr16_b64_v4i16((LAS s16x4*)a0);
  s16x4 hi = __builtin_amdgcn_ds_read_tr16_b64_v4i16((LAS s16x4*)a1);
  return __builtin_bit_cast(bf16x8, __builtin_shufflevector(lo, hi, 0, 1, 2, 3, 4, 5, 6, 7));
}
DI u32x4 lds_rd128(u32 a) { return *(LAS u32x4*)a; }
DI void lds_wr128(u32 a, u32x4 v) { *(LAS u32x4*)a = v; }
DI u32 f2u(float f) { return __builtin_bit_cast(u32, f); }
DI float u2f(u32 u) { return __builtin_bit_cast(float, u); }
DI u32 pk(float a, float b) { f32x2 t = {a, b}; return __builtin_bit_cast(u32, __builtin_convertvector(t, bf16x2)); }
template <int S, class V> DI u32x4 pack8(const V& v) {
  u32x4 r = {pk(v[8 * S], v[8 * S + 1]), pk(v[8 * S + 2], v[8 * S + 3]), pk(v[8 * S + 4], v[8 * S + 5]), pk(v[8 * S + 6], v[8 * S + 7])};
  return r;
}
DI float blo(u32 d) { return u2f(d << 16); }
DI float bhi(u32 d) { return u2f(d & 0xffff0000u); }
DI u32 umax(u32 a, u32 b) { return a > b ? a : b; }
DI bool any_(bool p) { return __builtin_amdgcn_ballot_w64(p) != 0; }
DI u32x4 sanit(u32x4 v) { return __builtin_elementwise_min(v, (u32x4){0xfffffffeu, 0xfffffffeu, 0xfffffffeu, 0xfffffffeu}); }
DI void st_dev(const Ctx& c, u32x4 v, u32 voff, u32 soff) {
  PIN1(v);
  __builtin_amdgcn_raw_buffer_store_b128(v, c.sw, voff, soff, 16);
  asm volatile("s_nop 1" :: "v"(v) : "memory");
}
DI u32x4 ld_dev(const Ctx& c, u32 voff, u32 soff) {
  return __builtin_amdgcn_raw_buffer_load_b128(c.sw, voff, soff, 16);
}
DI f32x2 xhalf(float v) { auto p = __builtin_amdgcn_permlane32_swap(f2u(v), f2u(v), false, false); f32x2 r = {u2f(p[0]), u2f(p[1])}; return r; }

struct P1 {
  Seg aw, aq; u32 ax, ka, kb, kc;
  bf16x8 xf[28], f[8], af[8], vf[8];
  f32x16 g1, u1, a0, a1, g2, u2, fp, k0, c[2];
  Seg s2[2]; u32 ob[2];
  u32x4 h2[14], of[8];
  u32x4 wp[2], ph[4], h1[14], kv[4];
  float y[16], e[14], sum, mxl, mnl;
  u32x2 poll;
  float gq, om;
  u32 hbase;
};

DI void wphi_rd(const Ctx& c, P1& p) { p.wp[0] = lds_rd128(spos(-1) + c.l16); p.wp[1] = lds_rd128(spos(-1) + 1024 + c.l16); }
DI void repack(const Ctx& c) {
  const u32 l = c.lane;
  const u32 a = spos(17) + c.m0w + (8 * (l >> 5) + ((l & 15) >> 2)) * 64 + 32 * ((l >> 4) & 1) + 8 * (l & 3);
  u32x4 v = __builtin_bit_cast(u32x4, tr_frag(a, a + 256));
  if (c.t == 14) {
    const u32 rho = l & 31, f = rho < 26 ? rho : ((rho == 28 || rho == 29) ? rho - 2 : 0);
    const u32 o = spos(17) + c.m0w + 2 * (112 * (l >> 5) + f);
#pragma unroll
    for (int d = 0; d < 4; ++d) {
      const u32 od = o + 56 * (8 * (d >> 1) + 2 * (d & 1));
      v[d] = (u32)*(LAS unsigned short*)od | ((u32)*(LAS unsigned short*)(od + 56) << 16);
    }
  }
  st_dev(c, v, c.vrp, 0);
}
template <int K> DI void fm_slice(P1& p) {
  if constexpr (K == 0) { p.fp = mfma(as_frag(p.wp[0]), as_frag(pack8<0>(p.a0)), (f32x16){}); }
  else if constexpr (K == 1) { p.fp = mfma(as_frag(p.wp[1]), as_frag(pack8<1>(p.a0)), p.fp); }
  else if constexpr (K == 2) { }
  else if constexpr (K == 3) {
    PIN1(p.fp);
    float mx = p.fp[0];
#pragma unroll
    for (int i = 1; i < 14; ++i) mx = __builtin_fmaxf(mx, p.fp[i]);
    p.mxl = mx; PIN1(p.mxl);
  } else if constexpr (K == 4) {
    float mn = p.fp[0];
#pragma unroll
    for (int i = 1; i < 14; ++i) mn = __builtin_fminf(mn, p.fp[i]);
    p.mnl = mn; PIN1(p.mnl);
  } else if constexpr (K == 5) {
    f32x2 t = xhalf(p.mxl); p.mxl = __builtin_fmaxf(t[0], t[1]) * LOG2E;
    t = xhalf(p.mnl); p.mnl = __builtin_fminf(t[0], t[1]) * LOG2E;
    PIN2(p.mxl, p.mnl);
  } else {
    constexpr int NEG = K >= 15, k = K - 6 - 9 * NEG;
    if constexpr (k < 5) {
#pragma unroll
      for (int i = 3 * k; i < imin(14, 3 * k + 3); ++i) {
        p.e[i] = __builtin_amdgcn_exp2f(NEG ? __builtin_fmaf(p.fp[i], -LOG2E, p.mnl) : __builtin_fmaf(p.fp[i], LOG2E, -p.mxl));
        PIN1(p.e[i]);
      }
      if constexpr (k == 1) p.sum = p.e[0] + p.e[1] + p.e[2];
      if constexpr (k > 1) p.sum += p.e[3 * k - 3] + p.e[3 * k - 2] + p.e[3 * k - 1];
      if constexpr (k > 0) PIN1(p.sum);
    } else if constexpr (k == 5) {
      f32x2 t = xhalf(p.sum + p.e[12] + p.e[13]); p.sum = __builtin_amdgcn_rcpf(t[0] + t[1]); PIN1(p.sum);
    } else if constexpr (k < 8) {
#pragma unroll
      for (int i = 7 * (k - 6); i < 7 * (k - 5); ++i) { p.e[i] *= p.sum; PIN1(p.e[i]); }
    } else {
      u32x4 o1 = {pk(p.e[8], p.e[9]), pk(p.e[10], p.e[11]), pk(p.e[12], p.e[13]), 0u};
      p.ph[2 * NEG] = pack8<0>(p.e); p.ph[2 * NEG + 1] = o1; PIN2(p.ph[2 * NEG], p.ph[2 * NEG + 1]);
    }
  }
}
template <int A, int B> DI void fm_slices(P1& p) { if constexpr (A < B) { fm_slice<A>(p); fm_slices<A + 1, B>(p); } }
template <u32 VB, int S> DI void v_handoff(const Ctx& c, const f32x16& av) {
  u32x4 vf = pack8<S>(av);
  const bool one = (c.lane & 31) == ((c.hd & 1) ? 0u : 28u);
#pragma unroll
  for (int d = 0; d < 4; ++d) vf[d] = one ? 0x3f803f80u : vf[d];
  lds_wr128(VB + c.tg * 2048 + c.l16 + S * 1024, vf);
}
template <int T> DI void phi_image(const Ctx& c, P1& p) {
  const u32 tok = c.lane & 31, h = c.lane >> 5;
  const u32 ft = (((tok >> 1) & 1) << 3) | (tok & 1) | (((tok >> 2) & 3) << 1);
  const u32 wa = SCR + c.tg * 4096 + tok * 128 + ((h ^ ft) << 3);
#pragma unroll
  for (int g = 0; g < 4; ++g) {
    u32x2 v = {p.ph[2 * T + (g >> 1)][2 * (g & 1)], p.ph[2 * T + (g >> 1)][2 * (g & 1) + 1]};
    *(LAS u32x2*)(wa ^ (u32)((8 * T + 2 * g) << 3)) = v;
  }
}
template <u32 VB, int K> DI void kv_stage(const Ctx& c, P1& p) {
  if constexpr (K >= 2 && K <= 5) {
    p.k0 = mfma(p.af[2 * K - 4], p.vf[2 * K - 4], K == 2 ? (f32x16){} : p.k0);
    p.k0 = mfma(p.af[2 * K - 3], p.vf[2 * K - 3], p.k0);
  }
  if constexpr (K == 0) {
    const u32 l = c.lane, h = l >> 5, q = (l & 15) >> 2, pp = l & 3, blk = (l >> 4) & 1;
    p.ka = SCR + (4 * h + q) * 128 + (((4 * blk + pp) ^ (q & 1) ^ (h << 1) ^ ((q >> 1) << 3) ^ ((c.tg & 1) << 3)) << 3);
    p.kb = p.ka ^ 32u; p.kc = VB + c.l16;
    asm volatile("" : "+v"(p.ka), "+v"(p.kb), "+v"(p.kc));
  }
  if constexpr (K < 4) {
#pragma unroll
    for (int s = 0; s < 2; ++s) {
      p.af[2 * K + s] = tr_frag(p.ka + K * 4096 + s * 2048, p.kb + K * 4096 + s * 2048 + 1024);
      p.vf[2 * K + s] = as_frag(lds_rd128(p.kc + K * 2048 + s * 1024));
    }
  }
  if constexpr (K == 7) {
    const u32 vo = c.tg < 2 ? c.l16 : OOB;
    st_dev(c, sanit(pack8<0>(p.k0)), vo, c.okvp + c.tg * 1024);
    st_dev(c, sanit(pack8<1>(p.k0)), vo + 1024, c.okvp + c.tg * 1024);
  }
}
DI void add8(float (&s)[8], u32x4 v) {
#pragma unroll
  for (int d = 0; d < 4; ++d) { s[2 * d] += blo(v[d]); s[2 * d + 1] += bhi(v[d]); }
}
DI void set8(float (&s)[8], u32x4 v) {
#pragma unroll
  for (int d = 0; d < 4; ++d) { s[2 * d] = blo(v[d]); s[2 * d + 1] = bhi(v[d]); }
}
template <int A, int B, int N> DI bool has_mark(const u32x4 (&v)[N]) {
  u32 m = 0;
#pragma unroll
  for (int i = A; i < B; ++i) m = umax(umax(m, v[i][0]), umax(v[i][1], umax(v[i][2], v[i][3])));
  return any_(m == 0xffffffffu);
}
template <int A, int B, int N, class F> DI void chk(u32x4 (&v)[N], F&& reload) {
  if (__builtin_expect(has_mark<A, B>(v), 1)) {
    u32 spin = 0;
    do { asm volatile("" ::: "memory"); reload(); } while (has_mark<A, B>(v) && ++spin < SPIN_MAX);
  }
}
template <int A, int B, int N> DI void ld_1k(const Ctx& c, u32x4 (&v)[N], u32 soff) {
#pragma unroll
  for (int i = A; i < B; ++i) v[i] = ld_dev(c, c.l16 + (i & 3) * 1024, soff + (i >> 2) * 4096);
}
template <int A, int B> DI void ld_h(const Ctx& c, u32x4 (&v)[14], u32 voff, u32 soff) {
#pragma unroll
  for (int i = A; i < B; ++i) v[i] = ld_dev(c, voff + (i & 1) * 1024, soff + c.rot[i >> 1] * 8192);
}
DI void o_publish(const Ctx& c, const u32x4 (&kv)[4], const u32x4 (&phq)[4], float g) {
  f32x16 n = {};
#pragma unroll
  for (int i = 0; i < 4; ++i) n = mfma(as_frag(kv[i]), as_frag(phq[i]), n);
  const bool odd = c.hd & 1;
  f32x2 t = xhalf(odd ? n[0] : n[12]);
  float sc = g * __builtin_amdgcn_rcpf((odd ? t[0] : t[1]) + 1e-6f);
#pragma unroll
  for (int i = 0; i < 16; ++i) n[i] *= sc;
  u32x4 f0 = sanit(pack8<0>(n)), f1 = sanit(pack8<1>(n));
  const bool z0 = odd && c.lane < 32, z1 = !odd && c.lane >= 32;
  f0[0] = z0 ? 0 : f0[0]; f0[1] = z0 ? 0 : f0[1];
  f1[2] = z1 ? 0 : f1[2]; f1[3] = z1 ? 0 : f1[3];
  st_dev(c, f0, c.l16, c.oo);
  st_dev(c, f1, c.l16 + 1024, c.oo);
}
DI u32 flag_off(const Ctx& c, bool spare) {
  const u32 i = c.lane & 15;
  u32 red = (c.hd * 16 + 7 + 8 * (i & 1)) << 7, sp = ((c.t * 16 + 7 + 8 * c.kh) << 7) + 8;
  u32 lo = spare ? ((i * 16 + c.t) << 7) + (c.r ? 0 : 8) : ((c.t * 16 + (i < 7 ? 8 * c.kh + i : c.hd ^ 8)) << 7) + 8;
  asm volatile("" : "+v"(red), "+v"(sp), "+v"(lo));
  return WS_FLAG + ((c.lane & 16) ? red : (spare || i < 8) ? lo : sp);
}
DI bool flag_bad(const Ctx& c, u32x2 f) { return any_(f[0] != (u32)c.tag || f[1] != (u32)(c.tag >> 32)); }
DI void flag_spin(const Ctx& c, u32x2 f, u32 voff) {
  if (__builtin_expect(flag_bad(c, f), 1)) {
    u32 spin = 0;
    do {
      __builtin_amdgcn_s_sleep(2);
      asm volatile("" ::: "memory");
      f = __builtin_amdgcn_raw_buffer_load_b64(c.sw, voff, 0, 16);
    } while (flag_bad(c, f) && ++spin < SPIN_MAX);
  }
  asm volatile("" ::: "memory");
}
DI void flag_publish(const Ctx& c, u32 word) {
  u32x2 v = {(u32)c.tag, (u32)(c.tag >> 32)};
  __builtin_amdgcn_raw_buffer_store_b64(v, c.sw, c.vone + word * 8, c.oflag, 16);
}
DI u32x2 ld_g(const Ctx& c) { return __builtin_amdgcn_raw_buffer_load_b64(c.sw, (c.lane & 31) * 8, c.og, 16); }

constexpr int PRO = 9;
constexpr int NS = 21;
constexpr int need_(int s) {
  return s < 0 ? 0 : s < 7 ? 3 * (s + 1) : s < 9 ? 23 + 2 * (s - 7) : s < 12 ? 31 + 2 * (s - 9) : s < 14 ? 36 : s < 18 ? 39 + 3 * (s - 14) : s < 20 ? 50 + 2 * (s - 18) : 62;
}
constexpr int iss_(int s) {
  constexpr int t[14] = {33, 36, 38, 41, 44, 47, 49, 51, 53, 56, 59, 62, 65, 68};
  return s < 0 ? PRO : s < 7 ? imin(28, PRO + 3 * (s + 1)) : t[s - 7];
}
constexpr int ustep(int n) {
  if (n == 34 || n == 28) return 16;
  if (n >= 25 && n < 28) return n == 25 ? 12 : 13;
  int s = 0; while (s < 20 && need_(s) <= n) ++s; return s;
}
constexpr bool ring_ok() {
  for (int s = -1; s < NS; ++s)
    for (int n = s < 0 ? 0 : iss_(s - 1); n < iss_(s); ++n) {
      if (uslot(n) == -2 && s < 18) return false;
      for (int m = 0; m < n; ++m) if (uslot(m) == uslot(n) && ustep(m) + 2 > s) return false;
    }
  return true;
}
static_assert(ring_ok(), "ring schedule");
constexpr int S_FLAG0 = 2, S_POLL0 = 5, S_CHK0 = 7, S_GATE = 12, S_CHKG = 13, S_FLAG1 = 16, S_POLL = 18, S_CHK = 19;
constexpr int oth_(int r, int s) {
  return s == S_FLAG0 || s == S_POLL0 || s == S_POLL ? 1 - r : s == 11 ? 1 - r : s == 12 ? 1 + r : s == 14 ? 2 : s == 16 ? 3 - 3 * r :
         s == 19 || s == 20 ? 7 - 7 * r : 0;
}
constexpr int tot_(int r, int s) { return iss_(s) - iss_(s - 1) + oth_(r, s); }
constexpr int vm_(int r, int s) {
  int sp = -1; while (iss_(sp) < need_(s)) ++sp;
  int n = iss_(sp) - need_(s) + (sp < 0 ? 2 - r : 0);
  for (int k = sp + 1; k < s; ++k) n += tot_(r, k);
  if (s == S_FLAG0) { int m = 0; for (int k = 0; k < s; ++k) m += tot_(r, k); n = imin(n, m); }
  if (s == S_FLAG1) n = imin(n, tot_(r, 15));
  if (((s == S_CHK0 || s == S_CHK) && r == 0) || s == S_CHKG) {
    int m = -1; for (int k = s == S_CHK0 ? S_POLL0 : s == S_CHKG ? S_GATE : S_POLL; k < s; ++k) m += tot_(r, k);
    n = imin(n, m);
  }
  return n;
}
constexpr int P2_(int r) { return r ? 168 : 140; }
constexpr int item0_(int r, int s) { return s < 7 ? 4 * s : (!r || s < 15) ? 8 * s - 28 : s < 18 ? 84 + 16 * (s - 14) : 8 * s; }
template <int R, int J> DI void rd_item(P1& p) {
  constexpr int U2 = P2_(R) - 28;
  if constexpr (J >= P2_(R)) {
    constexpr int k = J - P2_(R);
    if constexpr (k < 56) {
      constexpr u32 o = spos(w2s_(k / 4)) + ((k / 2) & 1) * 2048;
      p.f[J % 8] = tr_frag(sa(p.s2[k & 1], o), sa(p.s2[k & 1], o + 1024));
    } else if constexpr (k < 72) {
      constexpr int i = k % 8, hh = i >> 1;
      constexpr u32 o = (28 * hh - 4 * (hh & 1) + 16 * (i & 1)) * 128;
      p.f[J % 8] = tr_frag(p.ob[k >= 64] + o, p.ob[k >= 64] + o + 1024);
    }
  } else if constexpr ((J >= 44 && J < 72) || (R == 1 && J >= 84 && J < U2 && (J & 1))) {
    constexpr int k = J < 72 ? J - 44 : (J - 84) / 2, ch = k / 4;
    constexpr u32 o = (J < 72 ? upos(29 + ch) + R * 4096 : upos(36 + 3 * (ch / 2)) + (ch & 1) * 4096) + (k % 4) * 1024;
    p.f[J % 8] = as_frag(lds_rd128(sa(p.aq, o)));
  } else {
    constexpr int k = J < 84 ? J : J < U2 ? (J - 84) / (1 + R) : J - U2, ch = k / 4;
    constexpr u32 o = upos(J < 28 ? 3 * ch + 2 : J < 44 ? 14 + ch : J < 84 ? 7 + ch : J < U2 ? 37 + 3 * (ch / 2) + (ch & 1) : 47 + ch) + (k % 4) * 2048;
    p.f[J % 8] = tr_frag(sa(p.aw, o), sa(p.aw, o + 512));
  }
}
template <int R, int J> DI void mm_item(P1& p) {
  constexpr int U2 = P2_(R) - 28;
  if constexpr (J < 0) {}
  else if constexpr (J >= P2_(R)) {
    constexpr int k = J - P2_(R);
    constexpr int a = k < 56 ? k & 1 : k >= 64;
    p.c[a] = mfma(p.f[J % 8], as_frag(k < 28 ? p.h1[(k / 2) % 14] : k < 56 ? p.h2[(k / 2) % 14] : p.of[k % 8]), p.c[a]);
  } else if constexpr (J >= 44 && J < 72) {
    if constexpr (R == 1) p.a1 = mfma(p.xf[J - 44], p.f[J % 8], p.a1); else p.a0 = mfma(p.f[J % 8], p.xf[J - 44], p.a0);
  } else {
    f32x16& acc = J < 28 ? p.g1 : J < 84 ? p.u1 : J >= U2 ? p.u2 : (R == 1 && (J & 1)) ? p.a0 : p.g2;
    acc = mfma(p.f[J % 8], p.xf[J < 84 ? J % 28 : J < U2 ? (J - 84) / (1 + R) : J - U2], acc);
  }
}

constexpr float GK1 = -2.0f * 0.7978845608028654f * LOG2E, GK2 = GK1 * 0.044715f;
template <bool SC, int OP> DI void gelu_op(float (&y)[16], const f32x16& g, float om) {
  constexpr int st = OP % 4, i = OP / 4 - st;
  if constexpr (i >= 0 && i < 16) {
    if constexpr (st == 0) y[i] = g[i] * __builtin_fmaf(g[i] * g[i], GK2, GK1);
    else if constexpr (st == 1) y[i] = __builtin_amdgcn_exp2f(y[i]);
    else if constexpr (st == 2) y[i] = __builtin_amdgcn_rcpf(1.0f + y[i]);
    else y[i] = SC ? (g[i] * om) * y[i] : g[i] * y[i];
    PIN1(y[i]);
  }
}
constexpr int gcost(int op) { return op / 4 - op % 4 < 0 || op / 4 - op % 4 > 15 ? 0 : op % 4 == 0 ? 3 : op % 4 == 1 ? 2 : op % 4 == 2 ? 3 : 1; }
constexpr int gcum(int k, int n) { if (k >= n) return 76; int op = 0, cst = 0; while (cst * n < k * 144) cst += gcost(op++); return op; }
template <bool SC, int A, int B> DI void gelu_ops(float (&y)[16], const f32x16& g, float om) { if constexpr (A < B) { gelu_op<SC, A>(y, g, om); gelu_ops<SC, A + 1, B>(y, g, om); } }
template <int S> DI void h_store(const Ctx& c, const float (&y)[16], const f32x16& u, int blk) {
  float h[8];
#pragma unroll
  for (int i = 0; i < 8; ++i) h[i] = y[8 * S + i] * u[8 * S + i];
  st_dev(c, pack8<0>(h), c.l16 + S * 1024, c.oh + blk * 4096);
}
template <int A, int B> DI void y_scale(P1& p) {
#pragma unroll
  for (int i = A; i < B; ++i) { p.y[i] *= p.om; PIN1(p.y[i]); }
}
DI void addr2(const Ctx& c, P1& p) {
  const u32 l = c.lane, ks = c.tg >> 1, qq = (l & 15) >> 2;
  const u32 bk = ((4 * (l >> 5) + qq) * 128 + ((qq >> 1) << 6) + 32 * ((l >> 4) & 1) + 8 * (l & 3)) ;
  p.s2[0] = mkseg(bk + ks * 4096); p.s2[1] = mkseg((bk ^ 64) + ks * 4096);
  p.ob[0] = bk + ks * (112 * 128); p.ob[1] = (bk ^ 64) + ks * (112 * 128);
  PIN2(p.ob[0], p.ob[1]);
}
template <int R, int S, int K, int J> DI void slot_work(Ctx& c, P1& p) {
  if constexpr (S < 7) p.xf[J] = as_frag(lds_rd128((p.ax + upos(3 * S)) ^ (u32)(32 * K)));
  if constexpr (R == 0) {
    if constexpr (J >= 33 && J < 75) gelu_ops<false, gcum(J - 33, 42), gcum(J - 32, 42)>(p.y, p.g1, 0.f);
    if constexpr (J == 74) wphi_rd(c, p);
    if constexpr (J == 78 || J == 79) y_scale<8 * (J - 78), 8 * (J - 77)>(p);
    constexpr int F0 = J < 84 ? J - 77 : J < 89 ? 7 + (3 * (J - 84) + 1) / 2 : J < 91 ? 15 : 15 + (9 * (J - 91) + 7) / 8;
    constexpr int F1 = J + 1 < 84 ? J - 76 : J + 1 < 89 ? 7 + (3 * (J - 83) + 1) / 2 : J + 1 < 91 ? 15 : 15 + (9 * (J - 90) + 7) / 8;
    if constexpr (J >= 77 && J < 99) fm_slices<F0, imin(24, F1)>(p);
    if constexpr (J == 92) phi_image<0>(c, p);
    if constexpr (J == 99) phi_image<1>(c, p);
    if constexpr (S == 16) kv_stage<VBUF, K>(c, p);
    if constexpr (S >= 19 && K >= 1) ld_h<7 * (S - 19) + K - 1, 7 * (S - 19) + K>(c, p.h1, c.l16, p.hbase);
  } else {
    if constexpr (J >= 33 && J < 77) gelu_ops<false, gcum(J - 33, 44), gcum(J - 32, 44)>(p.y, p.g1, 0.f);
    if constexpr (J == 77 || J == 78) v_handoff<VBUF, J - 77>(c, p.a1);
    if constexpr (J == 79) wphi_rd(c, p);
    if constexpr (J == 83) PIN2(p.wp[0], p.wp[1]);
    if constexpr (J >= 84 && J < 88) y_scale<4 * (J - 84), 4 * (J - 83)>(p);
  }
  if constexpr (J == 89 || J == 90) h_store<J - 89>(c, p.y, p.u1, 0);
  if constexpr (J >= P2_(R) - 23) gelu_ops<true, gcum(J - P2_(R) + 23, 24), gcum(J - P2_(R) + 24, 24)>(p.y, p.g2, p.om);
  if constexpr (J == P2_(R) - 26) addr2(c, p);
  if constexpr (S == 11 + R && K == 3) st_dev(c, (u32x4){~0u, ~0u, ~0u, ~0u}, c.l16 + R * 1024, c.op);
}
template <int R, int S, int J0, int K, int N> DI void slots(Ctx& c, P1& p) {
  if constexpr (K < N) {
    constexpr int nu = iss_(S) - iss_(S - 1), u0 = iss_(S - 1);
    constexpr int ua = K == 0 ? 0 : ((2 * K + R) * nu) / (2 * N), ub = K == N - 1 ? nu : ((2 * K + 2 + R) * nu) / (2 * N);
    if constexpr (R == 1 && S >= 18 && (K == 0 || K == 4)) __builtin_amdgcn_s_setprio(K == 0);
    mm_item<R, J0 + K - 4>(p);
    SB0();
    if constexpr (ub > ua) unit_m0<u0 + ua>(c);
    rd_item<R, (J0 + K) ^ 1>(p);
    if constexpr (ub > ua) { unit_go<R, u0 + ua>(c); issue_range<R, u0 + ua + 1, u0 + ub>(c); }
    SB0();
    slot_work<R, S, K, J0 + K>(c, p);
    SB0();
    slots<R, S, J0, K + 1, N>(c, p);
  }
}
template <int R, int S> DI void step(Ctx& c, P1& p) {
  if constexpr ((S == S_CHK || S == S_CHK0) && R == 0) {
    asm volatile("s_waitcnt vmcnt(%1)" : "+v"(p.poll) : "n"(vm_(R, S)) : "memory");
    PIN1(S == S_CHK ? p.u2 : p.g1);
    flag_spin(c, p.poll, S == S_CHK ? c.vflag : c.vflag0);
    asm volatile("s_barrier" ::: "memory");
  } else if constexpr (S == S_CHKG) {
    asm volatile("s_waitcnt vmcnt(%1)\n\ts_barrier" : "+v"(p.poll) : "n"(vm_(R, S)) : "memory");
    {
      PIN2(p.u1, R ? p.a1 : p.a0);
      if (__builtin_expect(any_(p.poll[1] != (u32)c.tag), 1)) {
        u32 spin = 0;
        do { __builtin_amdgcn_s_sleep(2); asm volatile("" ::: "memory"); p.poll = ld_g(c); } while (any_(p.poll[1] != (u32)c.tag) && ++spin < SPIN_MAX);
      }
      p.gq = u2f(p.poll[0]); p.om = 1.0f - p.gq;
      PIN2(p.gq, p.om);
    }
  } else if constexpr (S == 16 && R == 0) wait_bar<vm_(R, S)>();
  else wait_bar_v<vm_(R, S)>();
  if constexpr (S == S_FLAG0 && R == 0) flag_publish(c, 0);
  if constexpr (S == S_FLAG1 && R == 0) flag_publish(c, 1);
  if constexpr ((S == S_POLL || S == S_POLL0) && R == 0) asm volatile("buffer_load_dwordx2 %0, %1, %2, 0 offen sc1" : "=v"(p.poll) : "v"(S == S_POLL ? c.vflag : c.vflag0), "s"(c.sw) : "memory");
  if constexpr (S == S_GATE) asm volatile("buffer_load_dwordx2 %0, %1, %2, %3 offen sc1" : "=v"(p.poll) : "v"((c.lane & 31) * 8), "s"(c.sw), "s"(c.og) : "memory");
  SB0();
  slots<R, S, item0_(R, S), 0, item0_(R, S + 1) - item0_(R, S)>(c, p);
}
template <int R, int S, int E> DI void steps(Ctx& c, P1& p) { if constexpr (S < E) { step<R, S>(c, p); steps<R, S + 1, E>(c, p); } }
template <int R, int A, int B, int RE, class F> DI void run(P1& p, F&& extra) {
  if constexpr (A < B) {
    mm_item<R, A - 4>(p);
    SB0();
    if constexpr (A < RE) rd_item<R, A ^ 1>(p);
    SB0();
    extra(std::integral_constant<int, A>{});
    SB0();
    run<R, A + 1, B, RE>(p, extra);
  } else { PIN2(p.c[0], p.c[1]); }
}
template <int K, class F> DI void fmq(P1& p, F&& extra) {
  if constexpr (K < 24) { fm_slice<K>(p); extra(std::integral_constant<int, K>{}); SB0(); fmq<K + 1>(p, extra); }
}

template <int R> DI void ffn_wg(Ctx& c, P1& p) {
  const u32 l = c.lane;
  const int tg2 = c.tg & 1, ks = c.tg >> 1;
  const int TG = 2 * (R == 1 ? c.kh : 1 - c.kh) + tg2;
  p.hbase = WS_H + (u32)(((c.t * 4 + TG) * 112 + 56 * c.kh + 2 * ks) * 1024);
  asm volatile("" : "+s"(p.hbase));
  SB0();
  issue_range<R, 3, PRO>(c);
  asm volatile("s_waitcnt vmcnt(%0)" :: "n"(PRO - 3) : "memory");
  repack(c);
  if constexpr (R == 0) st_dev(c, (u32x4){~0u, ~0u, ~0u, ~0u}, c.l16, c.okvp);
  p.g1 = p.u1 = p.a0 = p.a1 = p.g2 = p.u2 = p.c[0] = p.c[1] = (f32x16){};
  steps<R, 0, NS>(c, p);
  constexpr int B = P2_(R);
  const u32 h = l >> 5;
  const u32 obase = WS_O + (u32)(((c.t * 4 + TG) * 16 + 8 * c.kh + 4 * ks) * 2048);
  const u32 l16h = c.l16 + 4096, vflag2 = c.vflag + ((l & 31) < 7 ? 8 : 0);
  auto epi2 = [&](auto J) {
    if constexpr (J.value < B + 4) gelu_ops<true, gcum(J.value - B + 23, 24), gcum(J.value - B + 24, 24)>(p.y, p.g2, p.om);
    if constexpr (J.value == B + 4 || J.value == B + 5) h_store<J.value - B - 4>(c, p.y, p.u2, 1);
  };
  if constexpr (R == 0) {
    run<R, 140, 164, 999>(p, epi2);
    wait_bar_v<0>();
    flag_publish(c, 2);
    run<R, 164, 172, 999>(p, [&](auto J) { unit_half<71 - (J.value - 164) / 2, J.value & 1>(c); });
    flag_spin(c, __builtin_amdgcn_raw_buffer_load_b64(c.sw, vflag2, 0, 16), vflag2);
    ld_h<0, 10>(c, p.h2, l16h, p.hbase);
    run<R, 172, 200, 999>(p, [&](auto J) {
      if constexpr (J.value < 176) ld_h<J.value - 162, J.value - 161>(c, p.h2, l16h, p.hbase);
      if constexpr (J.value == 187) asm volatile("s_barrier" ::: "memory");
      if constexpr (J.value >= 190 && J.value < 198) ld_1k<J.value - 190, J.value - 189>(c, p.of, obase);
    });
  } else {
    run<R, 168, 172, 999>(p, epi2);
    fm_slices<0, 2>(p);
    SB0();
    h_store<0>(c, p.y, p.u2, 1); h_store<1>(c, p.y, p.u2, 1);
    SB0();
    __builtin_amdgcn_s_setprio(1);
    fmq<3>(p, [&](auto K) {
      if constexpr (K.value == 14) {
        wait_bar_v<0>();
        ld_1k<0, 4>(c, p.kv, WS_KVR + (u32)c.hd * 4096);
      }
      if constexpr (K.value > 14 && K.value <= 20) ld_h<K.value - 15, K.value - 14>(c, p.h1, c.l16, p.hbase);
    });
    chk<0, 4>(p.kv, [&] { ld_1k<0, 4>(c, p.kv, WS_KVR + (u32)c.hd * 4096); });
    o_publish(c, p.kv, p.ph, p.gq);
    __builtin_amdgcn_s_setprio(0);
    u32x2 f2;
    run<R, 172, 196, 999>(p, [&](auto J) {
      if constexpr (J.value < 180) ld_h<J.value - 166, J.value - 165>(c, p.h1, c.l16, p.hbase);
      if constexpr (J.value == 186) f2 = __builtin_amdgcn_raw_buffer_load_b64(c.sw, vflag2, 0, 16);
    });
    flag_spin(c, f2, vflag2);
    ld_h<0, 2>(c, p.h2, l16h, p.hbase);
    run<R, 196, 228, 999>(p, [&](auto J) {
      if constexpr (J.value < 208) ld_h<J.value - 194, J.value - 193>(c, p.h2, l16h, p.hbase);
      if constexpr (J.value == 199) asm volatile("s_barrier" ::: "memory");
      if constexpr (J.value >= 204 && J.value < 212) ld_1k<J.value - 204, J.value - 203>(c, p.of, obase);
    });
  }
  chk<0, 8>(p.of, [&] { ld_1k<0, 8>(c, p.of, obase); });
  const u32 xw = R ? (c.tg ? spos(8) + c.tg * UNIT : spos(6)) : spos(2) + c.tg * UNIT;
  const u32 xo = R ? (c.tg != 2 ? spos(8) + (c.tg ^ 2) * UNIT : spos(6)) : spos(2) + (c.tg ^ 2) * UNIT;
  const u32 wa = xw + (l & 31) * 256 + ((h ^ (l & 7)) << 4);
  auto put = [&](int g) {
    const f32x16& v = p.c[g >> 2];
    *(LAS f32x4*)((wa ^ (u32)((g & 3) << 5)) + (g >> 2) * 128) = (f32x4){v[4 * (g & 3)], v[4 * (g & 3) + 1], v[4 * (g & 3) + 2], v[4 * (g & 3) + 3]};
  };
  run<R, B + 60, B + 76, 999>(p, [&](auto J) { if constexpr (J.value >= B + 71 && J.value < B + 75) put(J.value - B - 71); });
  u32x4 pp[2];
  auto ld_p = [&] { pp[0] = ld_dev(c, c.l16, c.op ^ (8 * 16384)); pp[1] = ld_dev(c, c.l16 + 1024, c.op ^ (8 * 16384)); };
  put(4); put(5); put(6); put(7);
  volatile LAS u32* fl = (volatile LAS u32*)(spos(1) + 4096);
  asm volatile("" ::: "memory");
  fl[c.w] = 1u;
  if constexpr (R == 1) ld_p();
  const u32 ra = (ks * 16 + (l >> 3)) * 256 + (l & 4) * 32 + (((2 * (l & 3)) ^ (l >> 3)) << 4);
  f32x4 own[4];
#pragma unroll
  for (int q = 0; q < 4; ++q) own[q] = *(LAS f32x4*)(xw + (ra ^ (u32)((q & 1) << 4)) + (q >> 1) * 2048);
  { u32 spin = 0; while (__builtin_amdgcn_readfirstlane(fl[c.w ^ 2]) == 0 && ++spin < (1u << 22)) {} }
  asm volatile("" ::: "memory");
  float r16[16];
#pragma unroll
  for (int q = 0; q < 4; ++q) {
    const f32x4 b = *(LAS f32x4*)(xo + (ra ^ (u32)((q & 1) << 4)) + (q >> 1) * 2048);
#pragma unroll
    for (int i = 0; i < 4; ++i) r16[4 * q + i] = own[q][i] + b[i];
  }
  if constexpr (R == 0) {
    st_dev(c, sanit(pack8<0>(r16)), c.l16, c.op);
    st_dev(c, sanit(pack8<1>(r16)), c.l16 + 1024, c.op);
  } else {
#pragma unroll
    for (int i = 0; i < 16; ++i) PIN1(r16[i]);
    chk<0, 2>(pp, ld_p);
#pragma unroll
    for (int d = 0; d < 4; ++d) {
      r16[2 * d] += blo(pp[0][d]); r16[2 * d + 1] += bhi(pp[0][d]);
      r16[8 + 2 * d] += blo(pp[1][d]); r16[9 + 2 * d] += bhi(pp[1][d]);
    }
    const u32 vo = (l >> 3) * 896 + (l & 7) * 16, oo = (c.t * 128 + c.kh * 64 + tg2 * 32 + ks * 16) * 896 + c.jj * 128;
#pragma unroll
    for (int s = 0; s < 2; ++s) {
      u32x4 o = s ? pack8<1>(r16) : pack8<0>(r16);
      __builtin_amdgcn_raw_buffer_store_b128(o, mk(c.out, 2048 * 896), vo, oo + s * (8 * 896), 0);
      asm volatile("s_nop 1" :: "v"(o) : "memory");
    }
    if (__builtin_amdgcn_workgroup_id_x() == 0 && c.w == 4) {
      u32x2 v = {(u32)c.tag, (u32)(c.tag >> 32)};
      __builtin_amdgcn_raw_buffer_store_b64(v, c.sw, l == 0 ? 0u : OOB, 0, 16);
    }
  }
}

template <int R, int A0, int A, int B, int ST> DI void pipe(P1& p) {
  if constexpr (A - 3 * ST < B) {
    if constexpr (A - 3 * ST >= A0) mm_item<R, A - 3 * ST>(p);
    if constexpr (A < B) rd_item<R, A>(p);
    SB0();
    pipe<R, A0, A + ST, B, ST>(p);
  }
}
constexpr u32 sxpos(int ch) { return spos(2 * ch - 2); }
template <int N, int E> DI void spare_x(const Ctx& c) {
  if constexpr (N < E) { set_m0<sxpos(N >> 1) + (N & 1) * UNIT>(c); asm volatile("s_nop 0"); dma<0>((N & 1) ? c.vx2 : c.vx, c.sx, c.rot[N >> 1] * 128); spare_x<N + 1, E>(c); }
}
template <int R> DI void spare_wg(Ctx& c, P1& p) {
  const u32 l = c.lane;
  set_m0<spos(12)>(c); asm volatile("s_nop 0"); dma<0>(c.l16, mk(c.gw, 896), c.w == 0 ? 0u : 0x40000000u);
  const u32 okvr = WS_KVR + (u32)(c.t * 4096 + (2 * c.kh + (c.w & 1)) * 1024);
  const u32x4 ones = {~0u, ~0u, ~0u, ~0u};
  st_dev(c, ones, R == 0 || c.tg < 2 ? c.l16 : OOB, R == 0 ? c.okvp : okvr);
  spare_x<2, 4>(c);
  asm volatile("s_waitcnt vmcnt(4)" ::: "memory");
  repack(c);
  spare_x<4, 14>(c);
  wait_bar<0>();
  if constexpr (R == 0) flag_publish(c, 0);
#pragma unroll
  for (int s = 0; s < 28; ++s) p.xf[s] = as_frag(lds_rd128((p.ax ^ (u32)(32 * (s & 3))) + sxpos(s >> 2)));
  f32x16 ga = {};
#pragma unroll
  for (int s = 14 * R; s < 14 * R + 14; ++s) ga = mfma(as_frag(lds_rd128(spos(12) + c.rot[s >> 2] * 128 + (s & 3) * 32 + (l >> 5) * 16)), p.xf[s], ga);
  PIN1(ga);
  float g1 = 0.f;
  if constexpr (R == 0) *(LAS float*)(spos(17) + c.tg * 256 + l * 4) = ga[0];
  u32x2 f0 = __builtin_amdgcn_raw_buffer_load_b64(c.sw, c.vflag0, 0, 16);
  bar_l();
  if constexpr (R == 1) {
    g1 = __builtin_amdgcn_rcpf(1.0f + __builtin_amdgcn_exp2f(-LOG2E * (ga[0] + *(LAS float*)(spos(17) + c.tg * 256 + l * 4))));
    __builtin_amdgcn_raw_buffer_store_b64((u32x2){f2u(g1), (u32)c.tag}, c.sw, l < 32 ? l * 8 : OOB, c.og, 16);
  }
  flag_spin(c, f0, c.vflag0);
  issue_range<R, 29, 36>(c); issue_range<R, 28, 29>(c);
  issue_range<R, 39, 40>(c); issue_range<R, 42, 43>(c);
  p.a0 = p.a1 = (f32x16){};
  wait_bar<6>();
  issue_range<R, 36, 37>(c);
  {
    const u32 mo = WS_O + ((c.t * 4 + c.tg) * 16 + 8 * c.kh + 4 * R) * 2048;
#pragma unroll
    for (int i = 0; i < 8; ++i) st_dev(c, ones, c.l16 + (i & 3) * 1024, mo + (i >> 2) * 4096);
  }
  pipe<R, 44, 44, 60, 1>(p);
  wait_bar_v<8>();
  issue_range<R, 45, 46>(c);
  wphi_rd(c, p);
  pipe<R, 60, 60, 72, 1>(p);
  wait_bar<0>();
  if constexpr (R == 0) { fm_slices<0, 16>(p); flag_publish(c, 1); fm_slices<16, 24>(p); phi_image<0>(c, p); phi_image<1>(c, p); }
  else { v_handoff<spos(1), 0>(c, p.a1); v_handoff<spos(1), 1>(c, p.a1); pipe<R, 85, 85, 140, 2>(p); PIN1(p.a0); }
  bar_l();
  if constexpr (R == 0) {
    if (c.w < 2) {
      kv_stage<spos(1), 0>(c, p); kv_stage<spos(1), 1>(c, p); kv_stage<spos(1), 2>(c, p); kv_stage<spos(1), 3>(c, p);
      kv_stage<spos(1), 4>(c, p); kv_stage<spos(1), 5>(c, p); kv_stage<spos(1), 7>(c, p);
    }
    flag_spin(c, __builtin_amdgcn_raw_buffer_load_b64(c.sw, c.vflag, 0, 16), c.vflag);
    u32x4 v[8];
    const u32 ro = WS_KVP + (u32)(c.t * 65536 + (2 * c.kh + (c.w & 1)) * 1024 + (c.w >> 1) * 512), vr = (l & 31) * 16 + (l >> 5) * 32768;
    auto ld = [&] {
#pragma unroll
      for (int i = 0; i < 8; ++i) v[i] = ld_dev(c, vr, ro + i * 4096);
    };
    __builtin_amdgcn_s_sleep(6);
    ld();
    chk<0, 8>(v, ld);
    float s[8];
    set8(s, v[0]);
#pragma unroll
    for (int i = 1; i < 8; ++i) add8(s, v[i]);
#pragma unroll
    for (int i = 0; i < 8; ++i) { const f32x2 t = xhalf(s[i]); s[i] = t[0] + t[1]; }
    st_dev(c, sanit(pack8<0>(s)), l < 32 ? c.l16 : OOB, okvr + (c.w >> 1) * 512);
  } else {
    fm_slices<0, 24>(p);
    flag_spin(c, __builtin_amdgcn_raw_buffer_load_b64(c.sw, c.vflag, 0, 16), c.vflag);
    __builtin_amdgcn_s_sleep(20);
    u32x4 v[4];
    const u32 kvo = WS_KVR + (u32)c.hd * 4096;
    ld_1k<0, 4>(c, v, kvo);
    chk<0, 4>(v, [&] { __builtin_amdgcn_s_sleep(16); ld_1k<0, 4>(c, v, kvo); });
    o_publish(c, v, p.ph, g1);
  }
}

extern "C" __global__ __launch_bounds__(512, 2) void fused_kernel(const char* x, const char* wqkv, const char* wphi, const char* wo, const char* wg,
                                                                   const char* wu, const char* wd, const char* gw, char* out, char* ws) {
  Ctx c;
  const int bid = __builtin_amdgcn_workgroup_id_x(), xcd = bid & 7, idx = bid >> 3;
  const u32 tid = __builtin_amdgcn_workitem_id_x();
  c.gw = gw; c.out = out;
  c.t = 4 * (xcd >> 1) + (idx & 3); c.kh = xcd & 1; c.jj = idx >> 2; c.hd = 8 * c.kh + c.jj; c.j = 7 * c.kh + c.jj;
  c.w = __builtin_amdgcn_readfirstlane(tid >> 6); c.tg = c.w & 3; c.r = c.w >> 2;
  c.lane = tid & 63; c.l16 = c.lane * 16; c.m0w = c.w * 1024;
  const u32 l = c.lane, w = c.w;
  const u64 rt = 0x2106543210654321ull, r0 = c.jj + 2 * (idx & 3) + (xcd >> 1);
  const u32 rs = (u32)(rt >> (4 * (r0 > 9 ? r0 - 7 : r0)));
#pragma unroll
  for (int i = 0; i < 7; ++i) c.rot[i] = (rs >> (4 * i)) & 7;
  const u32 row8 = 8 * w + (l >> 3), sw2 = ((l & 7) ^ (((l >> 4) & 1) << 2)) << 4;
  c.vx = row8 * 896 + (((l & 7) ^ ((4 * w + (l >> 4)) & 7)) << 4); c.vx2 = c.vx + 64 * 896;
  c.vw = row8 * 3584 + sw2;
  asm volatile("" : "+v"(c.vx), "+v"(c.vx2), "+v"(c.vw));
  asm volatile("" : "+s"(c.rot[0]), "+s"(c.m0w));
  SB0();
  const u64 epoch = *(const __attribute__((address_space(4))) u64*)ws;
  c.sx = mk(x + c.t * 114688, 114688);
  c.sg = mk(wg + c.j * 256, 448 * 3584 - c.j * 256);
  if (c.jj < 7) issue_range<0, 0, 3>(c);
  else {
    u32 lo; u64 pc;
    asm volatile("s_getpc_b64 %0\n.Lpf_here:\n\ts_mov_b32 %1, .Lpf_here-fused_kernel" : "=s"(pc), "=s"(lo));
    asm volatile("s_mov_b32 m0, %0\n\ts_nop 0\n\tbuffer_load_dword %1, %2, 0 offen lds"
                 :: "i"(spos(13)), "v"((w * 64 + l) * 128), "s"(mk((const char*)(pc - lo), CODE_BYTES)) : "memory");
    spare_x<0, 2>(c);
  }
  {
    const u32 win = 56 * c.hd - 8 * (c.hd & 1), F = 6 * c.t + w, part = (F * 147) >> 12;
    const bool ok = F < 86 && w < 6;
    set_m0<spos(17)>(c); asm volatile("s_nop 0");
    if (c.t == 14) dma<0>((c.l16 + 896 * w - 224 * (c.hd & 1)) & 0x7fffffffu, mk(wphi + c.hd * 1568, 1568), 0);
    else dma<0>((l >> 2) * 2688 + (l & 3) * 16, mk(wqkv, 448 * 2688), ok ? (F - 28 * part) * (16 * 2688) + part * 896 + win : 0x40000000u);
    c.vrp = (ok ? WS_WA + c.hd * WA_HEAD + F * 1024 : OOB) + c.l16;
  }
  SB0();
  c.tag = epoch + 1;
  c.v2 = row8 * 896 + sw2; c.v2b = c.v2 + 64 * 896;
  c.vq = WS_WA + c.hd * WA_HEAD + c.tg * 1024 + c.l16; c.vkv = c.vq + 28672 * (1 + c.r);
  c.vflag0 = WS_FLAG + (((l & 15) * 16 + c.hd) << 7);
  c.sw = mk(ws, WS_END);
  c.oh = WS_H + (u32)(((c.t * 4 + c.tg) * 112 + 8 * c.j + 2 * c.r) * 1024);
  c.oo = WS_O + (u32)(((c.t * 4 + c.tg) * 16 + c.hd) * 2048);
  c.op = WS_P + (u32)((c.t * 16 + c.hd) * 16384 + c.tg * 2048);
  c.okvp = WS_KVP + (u32)((c.hd * 16 + c.t) * 4096 + c.tg * 1024);
  c.oflag = WS_FLAG + (u32)(c.t * 16 + c.hd) * 128;
  c.og = WS_G + (u32)(c.kh * 2048 + c.t * 128 + c.tg * 32) * 8;
  static_assert(WS_P % (16 * 16384) == 0, "the partner's slot is op ^ (8 * 16384)");
  c.vflag = flag_off(c, c.jj == 7); c.vone = tid == 0 ? 0u : OOB;
  asm volatile("" : "+s"(c.tag), "+s"(c.oh), "+s"(c.oo), "+s"(c.op), "+s"(c.okvp), "+s"(c.oflag), "+s"(c.og) :: "memory");
  asm volatile("" : "+v"(c.vflag), "+v"(c.vflag0), "+v"(c.vone), "+v"(c.vrp));
  P1 p;
  {
    const u32 q = (l & 15) >> 2, pp = l & 3, blk = (l >> 4) & 1, h = l >> 5, r = l & 31;
    p.aw = mkseg((8 * h + q) * 128 + ((64 * c.r) ^ ((q >> 1) << 6)) + 32 * blk + 8 * pp);
    p.aq = mkseg(c.l16);
    p.ax = (32 * c.tg + r) * 128 + ((h ^ ((r >> 1) & 7)) << 4);
  }
  if (c.jj < 7) {
    c.su = mk(wu + c.j * 256, 448 * 3584 - c.j * 256);
    c.sd = mk(wd + c.kh * 896 * 896 + c.jj * 128, 896 * 896 - c.jj * 128);
    c.so = mk(wo + c.kh * 224 * 896 + c.jj * 128, 224 * 896 - c.jj * 128);
    if (c.r == 0) { ffn_wg<0>(c, p); __builtin_amdgcn_endpgm(); }
    ffn_wg<1>(c, p);
    __builtin_amdgcn_endpgm();
  }
  if (c.r == 0) { spare_wg<0>(c, p); __builtin_amdgcn_endpgm(); }
  spare_wg<1>(c, p);
}

#if defined(__HIP_DEVICE_COMPILE__)
#pragma clang attribute pop
#endif

extern "C" void kernel_launch(void* const* d_in, const int* in_sizes, int n_in, void* d_out, int out_size, void* d_ws, size_t ws_size,
                              hipStream_t stream) {
  (void)hipFuncSetAttribute((const void*)fused_kernel, hipFuncAttributeMaxDynamicSharedMemorySize, LDS_BYTES);
  fused_kernel<<<256, 512, LDS_BYTES, stream>>>((const char*)d_in[0], (const char*)d_in[1], (const char*)d_in[2], (const char*)d_in[3],
                                                (const char*)d_in[5], (const char*)d_in[6], (const char*)d_in[7], (const char*)d_in[8],
                                                (char*)d_out, (char*)d_ws);
}
```
